```python
import torch

def _softmax(x, axis=-1):
    return torch.softmax(x, dim=axis)
def _gelu(x):
    return torch.nn.functional.gelu(x, approximate="tanh")
def _silu(x):
    return torch.nn.functional.silu(x)
def _sigmoid(x):
    return torch.sigmoid(x)
def _layernorm(x, axis=-1, eps=1e-5):
    m = x.mean(dim=axis, keepdim=True)
    v = x.var(dim=axis, keepdim=True, unbiased=False)
    return (x - m) / torch.sqrt(v + eps)
def _scatter_add(tgt, idx, src, axis=0):
    return tgt.clone().index_add_(axis, idx, src)
def _segment_sum(src, seg, n):
    out = torch.zeros((n,) + src.shape[1:], dtype=src.dtype)
    return out.index_add_(0, seg, src)
def _pad(x, pw):
    flat = tuple(v for pair in reversed(pw) for v in pair)
    return torch.nn.functional.pad(x, flat)

kernel_name = 'standalone__mha_block_cached__near_ridge'

def setup_inputs():

    _w = (lambda a: (lambda t: t.to(torch.bfloat16) if t.is_floating_point() else t)(torch.as_tensor(a)))
    B, Sq, D = 8, 192, 640
    H, Sk, Dh = 32, 2048, 128
    return {
        "x": _w((lambda *s: torch.randn(*s, dtype=torch.bfloat16) * 0.8)(B, Sq, D)),
        "k_cache": _w((lambda *s: torch.randn(*s, dtype=torch.bfloat16) * 0.8)(B, H, Sk, Dh)),
        "v_cache": _w((lambda *s: torch.randn(*s, dtype=torch.bfloat16) * 0.05)(B, H, Sk, Dh)),
        "wq": _w((lambda *s: torch.randn(*s, dtype=torch.bfloat16) * 0.8)(D, H * Dh) * (D ** -0.5)),
        "wo": _w((lambda *s: torch.randn(*s, dtype=torch.bfloat16) * 0.05)(H * Dh, D) * ((H * Dh) ** -0.5)),
    }

def reference(x, k_cache, v_cache, wq, wo):
    B, Sq, D = x.shape
    H, Dh = 32, 128
    q = (lambda x, *d: x.permute(*d))((x @ wq).reshape(B, Sq, H, Dh), 0, 2, 1, 3)
    scores = torch.einsum("bhqd,bhkd->bhqk", q, k_cache) * (Dh ** -0.5)
    p = _softmax(scores, axis=-1)
    o = torch.einsum("bhqk,bhkd->bhqd", p, v_cache)
    return (lambda x, *d: x.permute(*d))(o, 0, 2, 1, 3).reshape(B, Sq, H * Dh) @ wo
```

```cpp
#include <hip/hip_runtime.h>
#include <hip/hip_bf16.h>
typedef __attribute__((ext_vector_type(8))) short bf16x8;
typedef __attribute__((ext_vector_type(16))) float f32x16;
typedef unsigned short u16;
constexpr int B=8,SQ=192,DM=640,H=32,SK=2048,DH=128;
__device__ __forceinline__ float bf2f(u16 x){ return __uint_as_float(((unsigned)x)<<16); }
__device__ __forceinline__ u16 f2bf(float f){ __hip_bfloat16 b = __float2bfloat16(f); return *(u16*)&b; }

__global__ __launch_bounds__(64) void gemm_naive(const u16* A, const u16* Bm, u16* C, int M, int N, int K){
  int lane = threadIdx.x & 63; int r = lane&31, h = lane>>5;
  int tm = blockIdx.y*32, tn = blockIdx.x*32;
  f32x16 acc = {};
  for(int k=0;k<K;k+=16){
    bf16x8 a = *(const bf16x8*)(A + (size_t)(tm+r)*K + k + 8*h);
    bf16x8 b;
    for(int j=0;j<8;j++) b[j] = Bm[(size_t)(k+8*h+j)*N + tn + r];
    acc = __builtin_amdgcn_mfma_f32_32x32x16_bf16(a,b,acc,0,0,0);
  }
  for(int i=0;i<16;i++){ int row=(i&3)+8*(i>>2)+4*h; C[(size_t)(tm+row)*N+tn+r]=f2bf(acc[i]); }
}
__global__ __launch_bounds__(64) void attn_naive(const u16* Q, const u16* Kc, const u16* Vc, u16* O){
  int lane = threadIdx.x & 63; int r = lane&31, hh = lane>>5;
  int qb = blockIdx.x, h = blockIdx.y, b = blockIdx.z;
  const u16* Qp = Q + (size_t)(b*SQ + qb*32 + r)*(H*DH) + h*DH;
  const u16* Kp = Kc + (size_t)(b*H+h)*SK*DH;
  const u16* Vp = Vc + (size_t)(b*H+h)*SK*DH;
  bf16x8 q[8];
  for(int d0=0;d0<8;d0++) q[d0] = *(const bf16x8*)(Qp + d0*16 + 8*hh);
  f32x16 o[4] = {};
  float m=-1e30f, l=0.f;
  const float c = 0.088388347648318440f*1.4426950408889634f;
  for(int k0=0;k0<SK;k0+=32){
    f32x16 s = {};
    for(int d0=0;d0<8;d0++){
      bf16x8 a = *(const bf16x8*)(Kp + (size_t)(k0+r)*DH + d0*16 + 8*hh);
      s = __builtin_amdgcn_mfma_f32_32x32x16_bf16(a,q[d0],s,0,0,0);
    }
    float mx = s[0]; for(int i=1;i<16;i++) mx=fmaxf(mx,s[i]);
    mx = fmaxf(mx, __shfl_xor(mx,32));
    float mn = fmaxf(m,mx); float al = exp2f((m-mn)*c); m=mn;
    float ps=0; float p[16];
    for(int i=0;i<16;i++){ p[i]=exp2f((s[i]-mn)*c); ps+=p[i]; }
    ps += __shfl_xor(ps,32);
    l = l*al+ps;
    for(int d=0;d<4;d++) for(int i=0;i<16;i++) o[d][i]*=al;
    for(int ks=0;ks<2;ks++){
      bf16x8 pb; for(int j=0;j<8;j++) pb[j]=f2bf(p[8*ks+j]);
      for(int d=0;d<4;d++){
        bf16x8 a;
        for(int j=0;j<8;j++){ int key = 16*ks + 8*(j>>2) + 4*hh + (j&3); a[j] = Vp[(size_t)(k0+key)*DH + d*32 + r]; }
        o[d] = __builtin_amdgcn_mfma_f32_32x32x16_bf16(a,pb,o[d],0,0,0);
      }
    }
  }
  float il = 1.f/l;
  u16* Op = O + (size_t)(b*SQ + qb*32 + r)*(H*DH) + h*DH;
  for(int d=0;d<4;d++) for(int i=0;i<16;i++){ int dd = d*32 + (i&3)+8*(i>>2)+4*hh; Op[dd]=f2bf(o[d][i]*il); }
}
extern "C" void kernel_launch(void* const* d_in, const int* in_sizes, int n_in, void* d_out, int out_size, void* d_ws, size_t ws_size, hipStream_t stream){
  const u16* x=(const u16*)d_in[0]; const u16* k=(const u16*)d_in[1]; const u16* v=(const u16*)d_in[2];
  const u16* wq=(const u16*)d_in[3]; const u16* wo=(const u16*)d_in[4];
  u16* Q=(u16*)d_ws; u16* O=Q + (size_t)B*SQ*H*DH;
  gemm_naive<<<dim3(H*DH/32, B*SQ/32),64,0,stream>>>(x,wq,Q,B*SQ,H*DH,DM);
  attn_naive<<<dim3(SQ/32,H,B),64,0,stream>>>(Q,k,v,O);
  gemm_naive<<<dim3(DM/32, B*SQ/32),64,0,stream>>>(O,wo,(u16*)d_out,B*SQ,DM,H*DH);
}
```

```cpp
#include <hip/hip_runtime.h>
#include <hip/hip_bf16.h>
typedef __attribute__((ext_vector_type(8))) short bf16x8;
typedef __attribute__((ext_vector_type(4))) short s16x4;
typedef __attribute__((ext_vector_type(16))) float f32x16;
typedef __attribute__((ext_vector_type(4))) unsigned u32x4;
typedef __attribute__((ext_vector_type(2))) unsigned u32x2;
typedef __attribute__((ext_vector_type(2))) float f32x2;
typedef __attribute__((ext_vector_type(2))) __bf16 bf16x2;
typedef unsigned short u16;
constexpr int NB = 8, SQ = 192, DM = 640, NH = 32, SK = 2048, DH = 128;

__device__ __forceinline__ unsigned cvtpk(float lo, float hi) {
  f32x2 v = {lo, hi}; bf16x2 b = __builtin_convertvector(v, bf16x2); return __builtin_bit_cast(unsigned, b);
}

__device__ __forceinline__ void slow_path(const u16* x, const u16* kc, const u16* vc, const u16* wq, char* ows, int b, int h, int w) {
  const int lane = __builtin_amdgcn_mbcnt_hi(~0u, __builtin_amdgcn_mbcnt_lo(~0u, 0u));
  const int r = lane & 31, hh = lane >> 5, wi = w >> 1, wj = w & 1;
  const float c = 0.12751743082f;
  const u16* Kp = kc + (size_t)(b * NH + h) * SK * DH;
  const u16* Vp = vc + (size_t)(b * NH + h) * SK * DH;
#pragma unroll 1
  for (int qb = 0; qb < 3; ++qb) {
    const u16* xp = x + (size_t)(b * SQ + wi * 96 + qb * 32 + r) * DM;
    u32x4 q[8];
    const int dperm = (r & 19) | ((r & 4) << 1) | ((r & 8) >> 1);
#pragma unroll
    for (int db = 0; db < 4; ++db) {
      f32x16 acc = {};
#pragma unroll 1
      for (int k = 0; k < DM; k += 16) {
        bf16x8 bb = *(const bf16x8*)(xp + k + 8 * hh);
        bf16x8 aa;
#pragma unroll
        for (int j = 0; j < 8; ++j) aa[j] = wq[(size_t)(k + 8 * hh + j) * (NH * DH) + h * DH + db * 32 + dperm];
        acc = __builtin_amdgcn_mfma_f32_32x32x16_bf16(aa, bb, acc, 0, 0, 0);
      }
#pragma unroll
      for (int s = 0; s < 2; ++s)
#pragma unroll
        for (int e = 0; e < 4; ++e) q[2 * db + s][e] = cvtpk(acc[8 * s + 2 * e] * c, acc[8 * s + 2 * e + 1] * c);
    }
    f32x16 o0 = {}, o1 = {};
    float m = -__builtin_inff(), l = 0.f;
#pragma unroll 1
    for (int k0 = 0; k0 < SK; k0 += 32) {
      f32x16 s = {};
#pragma unroll
      for (int d0 = 0; d0 < 8; ++d0) {
        bf16x8 aa = *(const bf16x8*)(Kp + (size_t)(k0 + r) * DH + d0 * 16 + 8 * hh);
        s = __builtin_amdgcn_mfma_f32_32x32x16_bf16(aa, __builtin_bit_cast(bf16x8, q[d0]), s, 0, 0, 0);
      }
      float mx = s[0];
#pragma unroll
      for (int i = 1; i < 16; ++i) mx = fmaxf(mx, s[i]);
      mx = fmaxf(mx, __shfl_xor(mx, 32));
      const float mn = fmaxf(m, mx);
      const float al = __builtin_amdgcn_exp2f(m - mn);
      m = mn;
      float ps = 0.f;
#pragma unroll
      for (int i = 0; i < 16; ++i) { s[i] = __builtin_amdgcn_exp2f(s[i] - mn); ps += s[i]; }
      ps += __shfl_xor(ps, 32);
      l = l * al + ps;
#pragma unroll
      for (int i = 0; i < 16; ++i) { o0[i] *= al; o1[i] *= al; }
#pragma unroll
      for (int ks = 0; ks < 2; ++ks) {
        u32x4 pb;
#pragma unroll
        for (int e = 0; e < 4; ++e) pb[e] = cvtpk(s[8 * ks + 2 * e], s[8 * ks + 2 * e + 1]);
        bf16x8 a0, a1;
#pragma unroll
        for (int j = 0; j < 8; ++j) {
          const u16* vp = Vp + (size_t)(k0 + 16 * ks + 8 * (j >> 2) + 4 * hh + (j & 3)) * DH + wj * 64 + r;
          a0[j] = vp[0]; a1[j] = vp[32];
        }
        o0 = __builtin_amdgcn_mfma_f32_32x32x16_bf16(a0, __builtin_bit_cast(bf16x8, pb), o0, 0, 0, 0);
        o1 = __builtin_amdgcn_mfma_f32_32x32x16_bf16(a1, __builtin_bit_cast(bf16x8, pb), o1, 0, 0, 0);
      }
    }
    const float il = 1.f / l;
    char* op = ows + ((size_t)((b * 12 + wi * 6 + qb * 2 + (r >> 4)) * NH + h) * 4 + 2 * wj) * 1024 + (r & 15) * 16 + hh * 256;
#pragma unroll
    for (int gq = 0; gq < 4; ++gq) {
      u32x2 w0 = {cvtpk(o0[4 * gq] * il, o0[4 * gq + 1] * il), cvtpk(o0[4 * gq + 2] * il, o0[4 * gq + 3] * il)};
      u32x2 w1 = {cvtpk(o1[4 * gq] * il, o1[4 * gq + 1] * il), cvtpk(o1[4 * gq + 2] * il, o1[4 * gq + 3] * il)};
      __hip_atomic_store((unsigned long long*)(op + (gq & 1) * 512 + (gq >> 1) * 8), ((unsigned long long)w0[1] << 32) | w0[0], __ATOMIC_RELAXED, __HIP_MEMORY_SCOPE_AGENT);
      __hip_atomic_store((unsigned long long*)(op + 1024 + (gq & 1) * 512 + (gq >> 1) * 8), ((unsigned long long)w1[1] << 32) | w1[0], __ATOMIC_RELAXED, __HIP_MEMORY_SCOPE_AGENT);
    }
  }
}

__global__ __launch_bounds__(256, 1) void mha_kernel(const u16* __restrict__ x, const u16* __restrict__ kc, const u16* __restrict__ vc,
                                                     const u16* __restrict__ wq, const u16* __restrict__ wo, u16* __restrict__ out, char* __restrict__ ows
                                                      ) {
  const int bid = blockIdx.x;
  const int xcd = bid & 7, rk = bid >> 3;
  int b = 4 * (xcd & 1) + (rk & 3), h = 8 * (xcd >> 1) + (rk >> 2);
  int w = __builtin_amdgcn_readfirstlane(threadIdx.x >> 6);
  unsigned e0, e1;
  typedef const __attribute__((address_space(4))) unsigned long long* kptr;
  kptr ka = (kptr)__builtin_amdgcn_kernarg_segment_ptr();
  asm volatile(
    "s_memrealtime s[80:81]\n"
    "s_memtime s[90:91]\n"
    "s_load_dwordx8 s[16:23], %[ka], 0x0\n"
    "s_load_dwordx4 s[24:27], %[ka], 0x20\n"
    "s_load_dwordx2 s[28:29], %[ka], 0x30\n"
    "s_mov_b32 s56, %[w]\n"
    "v_mbcnt_lo_u32_b32 v160, -1, 0\n"
    "v_mbcnt_hi_u32_b32 v160, -1, v160\n"
    "s_and_b32 s72, %[b], 3\n"
    "s_mul_i32 s72, s72, 5\n"
    "v_bfe_u32 v161, v160, 2, 2\n"
    "v_lshrrev_b32 v162, 4, v160\n"
    "v_and_b32 v163, 3, v160\n"
    "s_lshr_b32 s72, s72, 1\n"
    "s_and_b32 s61, %[h], 7\n"
    "s_add_u32 s61, s61, s72\n"
    "s_sub_u32 s72, s61, 10\n"
    "s_cmp_ge_u32 s61, 10\n"
    "s_cselect_b32 s61, s72, s61\n"
    "v_lshlrev_b32 v165, 13, v161\n"
    "v_lshl_add_u32 v165, v162, 6, v165\n"
    "v_lshl_add_u32 v174, v163, 4, v165\n"
    "v_xor_b32 v166, 32, v174\n"
    "v_add_u32 v175, 31744, v174\n"
    "v_add_u32 v176, 63488, v166\n"
    "v_add_u32 v177, 95232, v166\n"
    "s_mul_i32 s67, s56, 61440\n"
    "s_lshl_b32 s68, s56, 17\n"
    "s_mul_i32 s69, s56, 6144\n"
    "s_lshl_b32 s66, s56, 12\n"
    "s_lshl_b32 s72, %[h], 8\n"
    "s_sub_u32 s42, 5242880, s72\n"
    "s_mov_b32 s43, 0x20000\n"
    "v_lshrrev_b32 v164, 1, v162\n"
    "v_lshl_add_u32 v165, v164, 2, v161\n"
    "v_mul_u32_u24 v165, 1280, v165\n"
    "v_and_b32 v166, 1, v162\n"
    "v_lshl_add_u32 v165, v166, 6, v165\n"
    "v_and_b32 v166, 2, v162\n"
    "v_xor_b32 v166, v163, v166\n"
    "v_lshl_add_u32 v168, v166, 4, v165\n"
    "v_add_u32 v169, 9216, v168\n"
    "v_add_u32 v170, 18432, v168\n"
    "v_add_u32 v171, 27648, v168\n"
    "v_add_u32 v172, 40960, v168\n"
    "v_add_u32 v173, 50176, v168\n"
    "s_mul_i32 s73, %[b], 245760\n"
    "s_mov_b32 s38, 245760\n"
    "s_mov_b32 s39, 0x20000\n"
    "s_waitcnt lgkmcnt(0)\n"
    "s_mov_b32 s91, s90\n"
    "s_mov_b64 s[40:41], s[22:23]\n"
    "s_add_u32 s40, s40, s72\n"
    "s_addc_u32 s41, s41, 0\n"
    "s_and_b32 s41, s41, 0xffff\n"
    "s_mov_b64 s[36:37], s[16:17]\n"
    "s_add_u32 s36, s36, s73\n"
    "s_addc_u32 s37, s37, 0\n"
    "s_and_b32 s37, s37, 0xffff\n"
    "s_lshl_b32 s63, s61, 19\n"
    "s_add_u32 m0, s66, 147456\n"
    "s_add_u32 s63, s63, s68\n"
    "buffer_load_dwordx4 v174, s[40:43], s63 offen offset:0 lds\n"
    "buffer_load_dwordx4 v175, s[40:43], s63 offen offset:1024 lds\n"
    "buffer_load_dwordx4 v176, s[40:43], s63 offen offset:2048 lds\n"
    "buffer_load_dwordx4 v177, s[40:43], s63 offen offset:3072 lds\n"
    "s_lshl_b32 s62, s61, 7\n"
    "s_add_u32 m0, s69, 122880\n"
    "s_add_u32 s62, s62, s67\n"
    "buffer_load_dwordx4 v168, s[36:39], s62 offen offset:0 lds\n"
    "buffer_load_dwordx4 v169, s[36:39], s62 offen offset:1024 lds\n"
    "buffer_load_dwordx4 v170, s[36:39], s62 offen offset:2048 lds\n"
    "buffer_load_dwordx4 v171, s[36:39], s62 offen offset:3072 lds\n"
    "s_add_u32 m0, s69, 126976\n"
    "s_add_u32 s61, s61, 1\n"
    "buffer_load_dwordx4 v172, s[36:39], s62 offen offset:0 lds\n"
    "s_cmp_eq_u32 s61, 10\n"
    "s_cselect_b32 s61, 0, s61\n"
    "buffer_load_dwordx4 v173, s[36:39], s62 offen offset:1024 lds\n"
    "s_getpc_b64 s[92:93]\n"
    "s_and_b32 s92, s92, 0xffffff80\n"
    "s_and_b32 s93, s93, 0xffff\n"
    "s_mov_b32 s94, 49152\n"
    "s_mov_b32 s95, 0x20000\n"
    "s_lshl_b32 s72, %[rk], 2\n"
    "s_add_u32 s72, s72, s56\n"
    "s_lshl_b32 s72, s72, 9\n"
    "v_lshlrev_b32 v164, 7, v163\n"
    "v_add_u32 v164, s72, v164\n"
    "buffer_load_dword v233, v164, s[92:95], 0 offen\n"
    "s_getpc_b64 s[94:95]\n"
    "v_lshrrev_b32 v255, 1, v164\n"
    "v_and_b32 v255, 0xffffff80, v255\n"
    "v_add_u32 v255, 14336, v255\n"
    "v_mov_b32 v164, 42008576\n"
    "global_load_dwordx2 v[188:189], v164, s[28:29] sc1\n"
    "s_lshr_b32 s57, s56, 1\n"
    "s_and_b32 s58, s56, 1\n"
    "s_lshl_b32 s64, s58, 14\n"
    "s_lshl_b32 s72, s57, 13\n"
    "s_add_u32 s64, s64, s72\n"
    "s_lshl_b32 s72, %[b], 5\n"
    "s_add_u32 s72, s72, %[h]\n"
    "s_lshl_b32 s73, s72, 19\n"
    "s_mov_b64 s[44:45], s[18:19]\n"
    "s_add_u32 s44, s44, s73\n"
    "s_addc_u32 s45, s45, 0\n"
    "s_and_b32 s45, s45, 0xffff\n"
    "s_mov_b32 s46, 0x80000\n"
    "s_mov_b32 s47, 0x20000\n"
    "s_mov_b64 s[48:49], s[20:21]\n"
    "s_add_u32 s48, s48, s73\n"
    "s_addc_u32 s49, s49, 0\n"
    "s_and_b32 s49, s49, 0xffff\n"
    "s_mov_b32 s50, 0x80000\n"
    "s_mov_b32 s51, 0x20000\n"
    "v_lshlrev_b32 v165, 8, v161\n"
    "v_lshl_add_u32 v165, v162, 6, v165\n"
    "v_lshl_add_u32 v165, v163, 4, v165\n"
    "s_lshl_b32 s75, s58, 7\n"
    "v_xor_b32 v253, s75, v165\n"
    "v_xor_b32 v254, 32, v253\n"
    "v_and_b32 v164, 1, v161\n"
    "v_lshlrev_b32 v164, 1, v164\n"
    "v_xor_b32 v164, v162, v164\n"
    "v_lshlrev_b32 v164, 4, v164\n"
    "v_lshl_add_u32 v164, v163, 6, v164\n"
    "v_lshl_add_u32 v166, v161, 10, v164\n"
    "s_lshl_b32 s75, s58, 14\n"
    "v_add_u32 v250, s75, v166\n"
    "s_lshl_b32 s76, s58, 9\n"
    "v_add_u32 v190, s76, v166\n"
    "v_xor_b32 v191, 512, v190\n"
    "v_lshl_add_u32 v166, v161, 9, v164\n"
    "s_mul_i32 s76, s57, 12288\n"
    "s_add_u32 s77, s76, 40960\n"
    "v_add_u32 v178, s77, v166\n"
    "s_add_u32 s77, s76, 81920\n"
    "v_add_u32 v179, s77, v166\n"
    "s_add_u32 s77, s76, 122880\n"
    "v_add_u32 v180, s77, v166\n"
    "v_and_b32 v165, 1, v162\n"
    "v_lshlrev_b32 v165, 5, v165\n"
    "v_lshl_add_u32 v165, v161, 6, v165\n"
    "v_lshl_add_u32 v165, v163, 3, v165\n"
    "v_lshl_add_u32 v166, v162, 10, v165\n"
    "s_add_u32 s75, s75, 32768\n"
    "v_add_u32 v251, s75, v166\n"
    "v_xor_b32 v252, 32, v251\n"
    "v_lshl_add_u32 v166, v162, 11, v165\n"
    "s_lshl_b32 s76, s58, 9\n"
    "s_add_u32 s77, s76, 65536\n"
    "v_add_u32 v181, s77, v166\n"
    "v_xor_b32 v184, 32, v181\n"
    "s_add_u32 s77, s76, 106496\n"
    "v_add_u32 v182, s77, v166\n"
    "v_xor_b32 v185, 32, v182\n"
    "s_add_u32 s77, s76, 147456\n"
    "v_add_u32 v183, s77, v166\n"
    "v_xor_b32 v186, 32, v183\n"
    "s_mul_i32 s76, s57, 24576\n"
    "s_add_u32 s76, s76, 114688\n"
    "s_lshl_b32 s77, s58, 11\n"
    "s_add_u32 s76, s76, s77\n"
    "v_lshlrev_b32 v164, 4, v160\n"
    "v_add_u32 v205, s76, v164\n"
    "v_xor_b32 v206, 2048, v205\n"
    "v_and_b32 v164, 15, v160\n"
    "v_cmp_eq_u32 vcc, 0, v164\n"
    "v_lshlrev_b32 v164, 4, v164\n"
    "v_lshrrev_b32 v166, 1, v162\n"
    "v_lshl_add_u32 v164, v166, 8, v164\n"
    "v_and_b32 v166, 1, v162\n"
    "v_lshl_add_u32 v164, v166, 3, v164\n"
    "v_add_u32 v204, s76, v164\n"
    "v_mov_b32 v166, 0x3f803f80\n"
    "v_cndmask_b32 v246, 0, v166, vcc\n"
    "v_mov_b32 v247, v246\n"
    "v_mov_b32 v248, v246\n"
    "v_mov_b32 v249, v246\n"
    "s_mov_b64 s[52:53], s[28:29]\n"
    "s_and_b32 s53, s53, 0xffff\n"
    "s_mov_b32 s54, 41943040\n"
    "s_mov_b32 s55, 0x20000\n"
    "s_mul_i32 s76, %[b], 12\n"
    "s_mul_i32 s78, s57, 6\n"
    "s_add_u32 s76, s76, s78\n"
    "s_lshl_b32 s76, s76, 5\n"
    "s_add_u32 s76, s76, %[h]\n"
    "s_lshl_b32 s76, s76, 12\n"
    "s_add_u32 s71, s76, s77\n"
    "v_mov_b32 v200, 0\n"
    "v_mov_b32 v201, 0\n"
    "v_mov_b32 v202, 0\n"
    "v_mov_b32 v203, 0\n"
    "v_mov_b32 v232, 0\n"
    "v_mov_b32 v234, 0\n"
    "v_mov_b32 v235, 0\n"
    "v_mov_b32 v236, 0\n"
    "v_mov_b32 v237, 0\n"
    "v_mov_b32 v238, 0\n"
    "v_mov_b32 v239, 0\n"
    "v_mov_b32 v240, 0\n"
    "v_mov_b32 v242, 0\n"
    "v_mov_b32 v243, 0\n"
    "v_mov_b32 v244, 0\n"
    "v_mov_b32 v245, 0\n"
    "s_mov_b32 s70, 0x3e0293ee\n"
    "s_mov_b32 s82, 0x3e0293ee\n"
    "s_mov_b32 s83, 0x3e0293ee\n"
    "s_lshr_b32 s89, %[xcd], 1\n"
    "s_mul_i32 s90, %[rk], 43\n"
    "s_lshr_b32 s90, s90, 8\n"
    "s_mul_i32 s72, s89, 1310720\n"
    "s_lshl_b32 s73, s90, 8\n"
    "s_add_u32 s72, s72, s73\n"
    "s_mov_b64 s[84:85], s[24:25]\n"
    "s_add_u32 s84, s84, s72\n"
    "s_addc_u32 s85, s85, 0\n"
    "s_and_b32 s85, s85, 0xffff\n"
    "s_mov_b32 s72, 1309696\n"
    "s_cmp_lt_u32 %[rk], 30\n"
    "s_cselect_b32 s86, s72, 0\n"
    "s_mov_b32 s87, 0x20000\n"
    "s_mov_b32 s88, 0\n"
    "s_mul_i32 s65, s56, 20480\n"
    "s_lshl_b32 s63, s61, 19\n"
    "s_add_u32 m0, s66, 65536\n"
    "s_add_u32 s63, s63, s68\n"
    "buffer_load_dwordx4 v174, s[40:43], s63 offen offset:0 lds\n"
    "buffer_load_dwordx4 v175, s[40:43], s63 offen offset:1024 lds\n"
    "buffer_load_dwordx4 v176, s[40:43], s63 offen offset:2048 lds\n"
    "buffer_load_dwordx4 v177, s[40:43], s63 offen offset:3072 lds\n"
    "s_lshl_b32 s62, s61, 7\n"
    "s_add_u32 m0, s69, 40960\n"
    "s_add_u32 s62, s62, s67\n"
    "buffer_load_dwordx4 v168, s[36:39], s62 offen offset:0 lds\n"
    "buffer_load_dwordx4 v169, s[36:39], s62 offen offset:1024 lds\n"
    "buffer_load_dwordx4 v170, s[36:39], s62 offen offset:2048 lds\n"
    "buffer_load_dwordx4 v171, s[36:39], s62 offen offset:3072 lds\n"
    "s_add_u32 m0, s69, 45056\n"
    "s_add_u32 s61, s61, 1\n"
    "buffer_load_dwordx4 v172, s[36:39], s62 offen offset:0 lds\n"
    "s_cmp_eq_u32 s61, 10\n"
    "s_cselect_b32 s61, 0, s61\n"
    "buffer_load_dwordx4 v173, s[36:39], s62 offen offset:1024 lds\n"
    "s_waitcnt vmcnt(12)\n"
    "s_barrier\n"
    "s_memtime s[82:83]\n"
    "s_memrealtime s[92:93]\n"
    "ds_read_b64_tr_b16 a[0:1], v183 offset:0\n"
    "ds_read_b64_tr_b16 a[2:3], v183 offset:1024\n"
    "ds_read_b64_tr_b16 a[4:5], v183 offset:8192\n"
    "ds_read_b64_tr_b16 a[6:7], v183 offset:9216\n"
    "ds_read_b128 a[64:67], v180 offset:0\n"
    "ds_read_b128 a[68:71], v180 offset:256\n"
    "ds_read_b64_tr_b16 a[8:9], v186 offset:0\n"
    "ds_read_b64_tr_b16 a[10:11], v186 offset:1024\n"
    "ds_read_b64_tr_b16 a[12:13], v186 offset:8192\n"
    "ds_read_b64_tr_b16 a[14:15], v186 offset:9216\n"
    "ds_read_b64_tr_b16 a[16:17], v183 offset:256\n"
    "ds_read_b64_tr_b16 a[18:19], v183 offset:1280\n"
    "ds_read_b64_tr_b16 a[20:21], v183 offset:8448\n"
    "ds_read_b64_tr_b16 a[22:23], v183 offset:9472\n"
    "ds_read_b64_tr_b16 a[24:25], v186 offset:256\n"
    "ds_read_b64_tr_b16 a[26:27], v186 offset:1280\n"
    "ds_read_b64_tr_b16 a[28:29], v186 offset:8448\n"
    "ds_read_b64_tr_b16 a[30:31], v186 offset:9472\n"
    "ds_read_b128 a[72:75], v180 offset:2048\n"
    "ds_read_b128 a[76:79], v180 offset:2304\n"
    "ds_read_b128 a[80:83], v180 offset:4096\n"
    "ds_read_b128 a[84:87], v180 offset:4352\n"
    "ds_read_b128 a[88:91], v180 offset:6144\n"
    "ds_read_b128 a[92:95], v180 offset:6400\n"
    "ds_read_b128 a[96:99], v180 offset:8192\n"
    "ds_read_b128 a[100:103], v180 offset:8448\n"
    "ds_read_b128 a[104:107], v180 offset:10240\n"
    "ds_read_b128 a[108:111], v180 offset:10496\n"
    "s_waitcnt lgkmcnt(15)\n"
    "v_mfma_f32_16x16x32_bf16 v[0:3], a[0:3], a[64:67], 0\n"
    "v_mfma_f32_16x16x32_bf16 v[0:3], a[4:7], a[68:71], v[0:3]\n"
    "s_lshl_b32 s63, s61, 19\n"
    "s_add_u32 m0, s66, 106496\n"
    "s_add_u32 s63, s63, s68\n"
    "buffer_load_dwordx4 v174, s[40:43], s63 offen offset:0 lds\n"
    "v_mfma_f32_16x16x32_bf16 v[4:7], a[8:11], a[64:67], 0\n"
    "v_mfma_f32_16x16x32_bf16 v[4:7], a[12:15], a[68:71], v[4:7]\n"
    "buffer_load_dwordx4 v175, s[40:43], s63 offen offset:1024 lds\n"
    "v_mfma_f32_16x16x32_bf16 v[8:11], a[16:19], a[64:67], 0\n"
    "s_waitcnt lgkmcnt(8)\n"
    "v_mfma_f32_16x16x32_bf16 v[8:11], a[20:23], a[68:71], v[8:11]\n"
    "buffer_load_dwordx4 v176, s[40:43], s63 offen offset:2048 lds\n"
    "v_mfma_f32_16x16x32_bf16 v[12:15], a[24:27], a[64:67], 0\n"
    "v_mfma_f32_16x16x32_bf16 v[12:15], a[28:31], a[68:71], v[12:15]\n"
    "buffer_load_dwordx4 v177, s[40:43], s63 offen offset:3072 lds\n"
    "v_mfma_f32_16x16x32_bf16 v[28:31], a[24:27], a[72:75], 0\n"
    "v_mfma_f32_16x16x32_bf16 v[28:31], a[28:31], a[76:79], v[28:31]\n"
    "s_lshl_b32 s62, s61, 7\n"
    "s_add_u32 m0, s69, 81920\n"
    "s_add_u32 s62, s62, s67\n"
    "buffer_load_dwordx4 v168, s[36:39], s62 offen offset:0 lds\n"
    "v_mfma_f32_16x16x32_bf16 v[24:27], a[16:19], a[72:75], 0\n"
    "v_mfma_f32_16x16x32_bf16 v[24:27], a[20:23], a[76:79], v[24:27]\n"
    "buffer_load_dwordx4 v169, s[36:39], s62 offen offset:1024 lds\n"
    "v_mfma_f32_16x16x32_bf16 v[20:23], a[8:11], a[72:75], 0\n"
    "v_mfma_f32_16x16x32_bf16 v[20:23], a[12:15], a[76:79], v[20:23]\n"
    "v_mfma_f32_16x16x32_bf16 v[16:19], a[0:3], a[72:75], 0\n"
    "buffer_load_dwordx4 v170, s[36:39], s62 offen offset:2048 lds\n"
    "v_mfma_f32_16x16x32_bf16 v[16:19], a[4:7], a[76:79], v[16:19]\n"
    "s_waitcnt lgkmcnt(0)\n"
    "v_mfma_f32_16x16x32_bf16 v[32:35], a[0:3], a[80:83], 0\n"
    "buffer_load_dwordx4 v171, s[36:39], s62 offen offset:3072 lds\n"
    "v_mfma_f32_16x16x32_bf16 v[32:35], a[4:7], a[84:87], v[32:35]\n"
    "v_mfma_f32_16x16x32_bf16 v[36:39], a[8:11], a[80:83], 0\n"
    "s_add_u32 m0, s69, 86016\n"
    "s_add_u32 s61, s61, 1\n"
    "buffer_load_dwordx4 v172, s[36:39], s62 offen offset:0 lds\n"
    "v_mfma_f32_16x16x32_bf16 v[36:39], a[12:15], a[84:87], v[36:39]\n"
    "v_mfma_f32_16x16x32_bf16 v[40:43], a[16:19], a[80:83], 0\n"
    "s_cmp_eq_u32 s61, 10\n"
    "s_cselect_b32 s61, 0, s61\n"
    "buffer_load_dwordx4 v173, s[36:39], s62 offen offset:1024 lds\n"
    "v_mfma_f32_16x16x32_bf16 v[40:43], a[20:23], a[84:87], v[40:43]\n"
    "v_mfma_f32_16x16x32_bf16 v[44:47], a[24:27], a[80:83], 0\n"
    "v_mfma_f32_16x16x32_bf16 v[44:47], a[28:31], a[84:87], v[44:47]\n"
    "s_waitcnt lgkmcnt(0)\n"
    "s_waitcnt vmcnt(10)\n"
    "s_barrier\n"
    "s_sub_u32 s91, s82, s91\n"
    "s_sub_u32 s92, s92, s80\n"
    "s_lshl_b32 s91, s91, 16\n"
    "s_or_b32 s91, s91, s92\n"
    "s_memtime s[82:83]\n"
    "s_memrealtime s[92:93]\n"
    "v_mfma_f32_16x16x32_bf16 v[48:51], a[0:3], a[88:91], 0\n"
    "ds_read_b64_tr_b16 a[32:33], v181 offset:0\n"
    "s_lshl_b32 s63, s61, 19\n"
    "v_mfma_f32_16x16x32_bf16 v[48:51], a[4:7], a[92:95], v[48:51]\n"
    "ds_read_b64_tr_b16 a[34:35], v181 offset:1024\n"
    "s_add_u32 m0, s66, 147456\n"
    "v_mfma_f32_16x16x32_bf16 v[52:55], a[8:11], a[88:91], 0\n"
    "ds_read_b64_tr_b16 a[36:37], v181 offset:8192\n"
    "s_add_u32 s63, s63, s68\n"
    "v_mfma_f32_16x16x32_bf16 v[52:55], a[12:15], a[92:95], v[52:55]\n"
    "ds_read_b64_tr_b16 a[38:39], v181 offset:9216\n"
    "buffer_load_dwordx4 v174, s[40:43], s63 offen offset:0 lds\n"
    "v_mfma_f32_16x16x32_bf16 v[56:59], a[16:19], a[88:91], 0\n"
    "ds_read_b128 a[64:67], v178 offset:0\n"
    "s_lshl_b32 s62, s61, 7\n"
    "v_mfma_f32_16x16x32_bf16 v[56:59], a[20:23], a[92:95], v[56:59]\n"
    "ds_read_b128 a[68:71], v178 offset:256\n"
    "s_add_u32 s62, s62, s67\n"
    "v_mfma_f32_16x16x32_bf16 v[60:63], a[24:27], a[88:91], 0\n"
    "ds_read_b64_tr_b16 a[40:41], v184 offset:0\n"
    "s_add_u32 s61, s61, 1\n"
    "v_mfma_f32_16x16x32_bf16 v[60:63], a[28:31], a[92:95], v[60:63]\n"
    "ds_read_b64_tr_b16 a[42:43], v184 offset:1024\n"
    "s_cmp_eq_u32 s61, 10\n"
    "s_cselect_b32 s61, 0, s61\n"
    "v_mfma_f32_16x16x32_bf16 v[76:79], a[24:27], a[96:99], 0\n"
    "ds_read_b64_tr_b16 a[44:45], v184 offset:8192\n"
    "buffer_load_dwordx4 v175, s[40:43], s63 offen offset:1024 lds\n"
    "v_mfma_f32_16x16x32_bf16 v[76:79], a[28:31], a[100:103], v[76:79]\n"
    "ds_read_b64_tr_b16 a[46:47], v184 offset:9216\n"
    "v_mfma_f32_16x16x32_bf16 v[72:75], a[16:19], a[96:99], 0\n"
    "ds_read_b64_tr_b16 a[48:49], v181 offset:256\n"
    "v_mfma_f32_16x16x32_bf16 v[72:75], a[20:23], a[100:103], v[72:75]\n"
    "ds_read_b64_tr_b16 a[50:51], v181 offset:1280\n"
    "v_mfma_f32_16x16x32_bf16 v[68:71], a[8:11], a[96:99], 0\n"
    "ds_read_b64_tr_b16 a[52:53], v181 offset:8448\n"
    "v_mfma_f32_16x16x32_bf16 v[68:71], a[12:15], a[100:103], v[68:71]\n"
    "ds_read_b64_tr_b16 a[54:55], v181 offset:9472\n"
    "buffer_load_dwordx4 v176, s[40:43], s63 offen offset:2048 lds\n"
    "v_mfma_f32_16x16x32_bf16 v[64:67], a[0:3], a[96:99], 0\n"
    "ds_read_b64_tr_b16 a[56:57], v184 offset:256\n"
    "v_mfma_f32_16x16x32_bf16 v[64:67], a[4:7], a[100:103], v[64:67]\n"
    "ds_read_b64_tr_b16 a[58:59], v184 offset:1280\n"
    "v_mfma_f32_16x16x32_bf16 v[80:83], a[0:3], a[104:107], 0\n"
    "ds_read_b64_tr_b16 a[60:61], v184 offset:8448\n"
    "v_mfma_f32_16x16x32_bf16 v[80:83], a[4:7], a[108:111], v[80:83]\n"
    "ds_read_b64_tr_b16 a[62:63], v184 offset:9472\n"
    "v_mfma_f32_16x16x32_bf16 v[84:87], a[8:11], a[104:107], 0\n"
    "ds_read_b128 a[72:75], v178 offset:2048\n"
    "buffer_load_dwordx4 v177, s[40:43], s63 offen offset:3072 lds\n"
    "v_mfma_f32_16x16x32_bf16 v[84:87], a[12:15], a[108:111], v[84:87]\n"
    "ds_read_b128 a[76:79], v178 offset:2304\n"
    "s_add_u32 m0, s69, 122880\n"
    "v_mfma_f32_16x16x32_bf16 v[88:91], a[16:19], a[104:107], 0\n"
    "ds_read_b128 a[80:83], v178 offset:4096\n"
    "v_mfma_f32_16x16x32_bf16 v[88:91], a[20:23], a[108:111], v[88:91]\n"
    "ds_read_b128 a[84:87], v178 offset:4352\n"
    "v_mfma_f32_16x16x32_bf16 v[92:95], a[24:27], a[104:107], 0\n"
    "ds_read_b128 a[88:91], v178 offset:6144\n"
    "v_mfma_f32_16x16x32_bf16 v[92:95], a[28:31], a[108:111], v[92:95]\n"
    "ds_read_b128 a[92:95], v178 offset:6400\n"
    "buffer_load_dwordx4 v168, s[36:39], s62 offen offset:0 lds\n"
    "s_waitcnt lgkmcnt(8)\n"
    "v_mfma_f32_16x16x32_bf16 v[0:3], a[32:35], a[64:67], v[0:3]\n"
    "ds_read_b128 a[96:99], v178 offset:8192\n"
    "v_mfma_f32_16x16x32_bf16 v[0:3], a[36:39], a[68:71], v[0:3]\n"
    "ds_read_b128 a[100:103], v178 offset:8448\n"
    "v_mfma_f32_16x16x32_bf16 v[4:7], a[40:43], a[64:67], v[4:7]\n"
    "ds_read_b128 a[104:107], v178 offset:10240\n"
    "v_mfma_f32_16x16x32_bf16 v[4:7], a[44:47], a[68:71], v[4:7]\n"
    "ds_read_b128 a[108:111], v178 offset:10496\n"
    "v_mfma_f32_16x16x32_bf16 v[8:11], a[48:51], a[64:67], v[8:11]\n"
    "buffer_load_dwordx4 v169, s[36:39], s62 offen offset:1024 lds\n"
    "v_mfma_f32_16x16x32_bf16 v[8:11], a[52:55], a[68:71], v[8:11]\n"
    "v_mfma_f32_16x16x32_bf16 v[12:15], a[56:59], a[64:67], v[12:15]\n"
    "s_waitcnt lgkmcnt(7)\n"
    "v_mfma_f32_16x16x32_bf16 v[12:15], a[60:63], a[68:71], v[12:15]\n"
    "v_mfma_f32_16x16x32_bf16 v[28:31], a[56:59], a[72:75], v[28:31]\n"
    "v_mfma_f32_16x16x32_bf16 v[28:31], a[60:63], a[76:79], v[28:31]\n"
    "buffer_load_dwordx4 v170, s[36:39], s62 offen offset:2048 lds\n"
    "v_mfma_f32_16x16x32_bf16 v[24:27], a[48:51], a[72:75], v[24:27]\n"
    "v_mfma_f32_16x16x32_bf16 v[24:27], a[52:55], a[76:79], v[24:27]\n"
    "v_mfma_f32_16x16x32_bf16 v[20:23], a[40:43], a[72:75], v[20:23]\n"
    "v_mfma_f32_16x16x32_bf16 v[20:23], a[44:47], a[76:79], v[20:23]\n"
    "v_mfma_f32_16x16x32_bf16 v[16:19], a[32:35], a[72:75], v[16:19]\n"
    "buffer_load_dwordx4 v171, s[36:39], s62 offen offset:3072 lds\n"
    "s_add_u32 m0, s69, 126976\n"
    "v_mfma_f32_16x16x32_bf16 v[16:19], a[36:39], a[76:79], v[16:19]\n"
    "v_mfma_f32_16x16x32_bf16 v[32:35], a[32:35], a[80:83], v[32:35]\n"
    "s_waitcnt lgkmcnt(1)\n"
    "v_mfma_f32_16x16x32_bf16 v[32:35], a[36:39], a[84:87], v[32:35]\n"
    "v_mfma_f32_16x16x32_bf16 v[36:39], a[40:43], a[80:83], v[36:39]\n"
    "v_mfma_f32_16x16x32_bf16 v[36:39], a[44:47], a[84:87], v[36:39]\n"
    "buffer_load_dwordx4 v172, s[36:39], s62 offen offset:0 lds\n"
    "v_mfma_f32_16x16x32_bf16 v[40:43], a[48:51], a[80:83], v[40:43]\n"
    "v_mfma_f32_16x16x32_bf16 v[40:43], a[52:55], a[84:87], v[40:43]\n"
    "v_mfma_f32_16x16x32_bf16 v[44:47], a[56:59], a[80:83], v[44:47]\n"
    "v_mfma_f32_16x16x32_bf16 v[44:47], a[60:63], a[84:87], v[44:47]\n"
    "buffer_load_dwordx4 v173, s[36:39], s62 offen offset:1024 lds\n"
    "s_waitcnt lgkmcnt(0)\n"
    "s_waitcnt vmcnt(10)\n"
    "s_barrier\n"
    "s_mov_b32 s83, s92\n"
    "v_mfma_f32_16x16x32_bf16 v[48:51], a[32:35], a[88:91], v[48:51]\n"
    "ds_read_b64_tr_b16 a[0:1], v182 offset:0\n"
    "s_lshl_b32 s63, s61, 19\n"
    "v_mfma_f32_16x16x32_bf16 v[48:51], a[36:39], a[92:95], v[48:51]\n"
    "ds_read_b64_tr_b16 a[2:3], v182 offset:1024\n"
    "s_add_u32 m0, s66, 65536\n"
    "v_mfma_f32_16x16x32_bf16 v[52:55], a[40:43], a[88:91], v[52:55]\n"
    "ds_read_b64_tr_b16 a[4:5], v182 offset:8192\n"
    "s_add_u32 s63, s63, s68\n"
    "v_mfma_f32_16x16x32_bf16 v[52:55], a[44:47], a[92:95], v[52:55]\n"
    "ds_read_b64_tr_b16 a[6:7], v182 offset:9216\n"
    "buffer_load_dwordx4 v174, s[40:43], s63 offen offset:0 lds\n"
    "v_mfma_f32_16x16x32_bf16 v[56:59], a[48:51], a[88:91], v[56:59]\n"
    "ds_read_b128 a[64:67], v179 offset:0\n"
    "s_lshl_b32 s62, s61, 7\n"
    "v_mfma_f32_16x16x32_bf16 v[56:59], a[52:55], a[92:95], v[56:59]\n"
    "ds_read_b128 a[68:71], v179 offset:256\n"
    "s_add_u32 s62, s62, s67\n"
    "v_mfma_f32_16x16x32_bf16 v[60:63], a[56:59], a[88:91], v[60:63]\n"
    "ds_read_b64_tr_b16 a[8:9], v185 offset:0\n"
    "s_add_u32 s61, s61, 1\n"
    "v_mfma_f32_16x16x32_bf16 v[60:63], a[60:63], a[92:95], v[60:63]\n"
    "ds_read_b64_tr_b16 a[10:11], v185 offset:1024\n"
    "s_cmp_eq_u32 s61, 10\n"
    "s_cselect_b32 s61, 0, s61\n"
    "v_mfma_f32_16x16x32_bf16 v[76:79], a[56:59], a[96:99], v[76:79]\n"
    "ds_read_b64_tr_b16 a[12:13], v185 offset:8192\n"
    "buffer_load_dwordx4 v175, s[40:43], s63 offen offset:1024 lds\n"
    "v_mfma_f32_16x16x32_bf16 v[76:79], a[60:63], a[100:103], v[76:79]\n"
    "ds_read_b64_tr_b16 a[14:15], v185 offset:9216\n"
    "v_mfma_f32_16x16x32_bf16 v[72:75], a[48:51], a[96:99], v[72:75]\n"
    "ds_read_b64_tr_b16 a[16:17], v182 offset:256\n"
    "v_mfma_f32_16x16x32_bf16 v[72:75], a[52:55], a[100:103], v[72:75]\n"
    "ds_read_b64_tr_b16 a[18:19], v182 offset:1280\n"
    "v_mfma_f32_16x16x32_bf16 v[68:71], a[40:43], a[96:99], v[68:71]\n"
    "ds_read_b64_tr_b16 a[20:21], v182 offset:8448\n"
    "v_mfma_f32_16x16x32_bf16 v[68:71], a[44:47], a[100:103], v[68:71]\n"
    "ds_read_b64_tr_b16 a[22:23], v182 offset:9472\n"
    "buffer_load_dwordx4 v176, s[40:43], s63 offen offset:2048 lds\n"
    "v_mfma_f32_16x16x32_bf16 v[64:67], a[32:35], a[96:99], v[64:67]\n"
    "ds_read_b64_tr_b16 a[24:25], v185 offset:256\n"
    "v_mfma_f32_16x16x32_bf16 v[64:67], a[36:39], a[100:103], v[64:67]\n"
    "ds_read_b64_tr_b16 a[26:27], v185 offset:1280\n"
    "v_mfma_f32_16x16x32_bf16 v[80:83], a[32:35], a[104:107], v[80:83]\n"
    "ds_read_b64_tr_b16 a[28:29], v185 offset:8448\n"
    "v_mfma_f32_16x16x32_bf16 v[80:83], a[36:39], a[108:111], v[80:83]\n"
    "ds_read_b64_tr_b16 a[30:31], v185 offset:9472\n"
    "v_mfma_f32_16x16x32_bf16 v[84:87], a[40:43], a[104:107], v[84:87]\n"
    "ds_read_b128 a[72:75], v179 offset:2048\n"
    "buffer_load_dwordx4 v177, s[40:43], s63 offen offset:3072 lds\n"
    "v_mfma_f32_16x16x32_bf16 v[84:87], a[44:47], a[108:111], v[84:87]\n"
    "ds_read_b128 a[76:79], v179 offset:2304\n"
    "s_add_u32 m0, s69, 40960\n"
    "v_mfma_f32_16x16x32_bf16 v[88:91], a[48:51], a[104:107], v[88:91]\n"
    "ds_read_b128 a[80:83], v179 offset:4096\n"
    "v_mfma_f32_16x16x32_bf16 v[88:91], a[52:55], a[108:111], v[88:91]\n"
    "ds_read_b128 a[84:87], v179 offset:4352\n"
    "v_mfma_f32_16x16x32_bf16 v[92:95], a[56:59], a[104:107], v[92:95]\n"
    "ds_read_b128 a[88:91], v179 offset:6144\n"
    "v_mfma_f32_16x16x32_bf16 v[92:95], a[60:63], a[108:111], v[92:95]\n"
    "ds_read_b128 a[92:95], v179 offset:6400\n"
    "buffer_load_dwordx4 v168, s[36:39], s62 offen offset:0 lds\n"
    "s_waitcnt lgkmcnt(8)\n"
    "v_mfma_f32_16x16x32_bf16 v[0:3], a[0:3], a[64:67], v[0:3]\n"
    "ds_read_b128 a[96:99], v179 offset:8192\n"
    "v_mfma_f32_16x16x32_bf16 v[0:3], a[4:7], a[68:71], v[0:3]\n"
    "ds_read_b128 a[100:103], v179 offset:8448\n"
    "v_mfma_f32_16x16x32_bf16 v[4:7], a[8:11], a[64:67], v[4:7]\n"
    "ds_read_b128 a[104:107], v179 offset:10240\n"
    "v_mfma_f32_16x16x32_bf16 v[4:7], a[12:15], a[68:71], v[4:7]\n"
    "ds_read_b128 a[108:111], v179 offset:10496\n"
    "v_mfma_f32_16x16x32_bf16 v[8:11], a[16:19], a[64:67], v[8:11]\n"
    "buffer_load_dwordx4 v169, s[36:39], s62 offen offset:1024 lds\n"
    "v_mfma_f32_16x16x32_bf16 v[8:11], a[20:23], a[68:71], v[8:11]\n"
    "v_mfma_f32_16x16x32_bf16 v[12:15], a[24:27], a[64:67], v[12:15]\n"
    "s_waitcnt lgkmcnt(7)\n"
    "v_mfma_f32_16x16x32_bf16 v[12:15], a[28:31], a[68:71], v[12:15]\n"
    "v_mfma_f32_16x16x32_bf16 v[28:31], a[24:27], a[72:75], v[28:31]\n"
    "v_mfma_f32_16x16x32_bf16 v[28:31], a[28:31], a[76:79], v[28:31]\n"
    "buffer_load_dwordx4 v170, s[36:39], s62 offen offset:2048 lds\n"
    "v_mfma_f32_16x16x32_bf16 v[24:27], a[16:19], a[72:75], v[24:27]\n"
    "v_mfma_f32_16x16x32_bf16 v[24:27], a[20:23], a[76:79], v[24:27]\n"
    "v_mfma_f32_16x16x32_bf16 v[20:23], a[8:11], a[72:75], v[20:23]\n"
    "v_mfma_f32_16x16x32_bf16 v[20:23], a[12:15], a[76:79], v[20:23]\n"
    "v_mfma_f32_16x16x32_bf16 v[16:19], a[0:3], a[72:75], v[16:19]\n"
    "buffer_load_dwordx4 v171, s[36:39], s62 offen offset:3072 lds\n"
    "s_add_u32 m0, s69, 45056\n"
    "v_mfma_f32_16x16x32_bf16 v[16:19], a[4:7], a[76:79], v[16:19]\n"
    "v_mfma_f32_16x16x32_bf16 v[32:35], a[0:3], a[80:83], v[32:35]\n"
    "s_waitcnt lgkmcnt(1)\n"
    "v_mfma_f32_16x16x32_bf16 v[32:35], a[4:7], a[84:87], v[32:35]\n"
    "v_mfma_f32_16x16x32_bf16 v[36:39], a[8:11], a[80:83], v[36:39]\n"
    "v_mfma_f32_16x16x32_bf16 v[36:39], a[12:15], a[84:87], v[36:39]\n"
    "buffer_load_dwordx4 v172, s[36:39], s62 offen offset:0 lds\n"
    "v_mfma_f32_16x16x32_bf16 v[40:43], a[16:19], a[80:83], v[40:43]\n"
    "v_mfma_f32_16x16x32_bf16 v[40:43], a[20:23], a[84:87], v[40:43]\n"
    "v_mfma_f32_16x16x32_bf16 v[44:47], a[24:27], a[80:83], v[44:47]\n"
    "v_mfma_f32_16x16x32_bf16 v[44:47], a[28:31], a[84:87], v[44:47]\n"
    "buffer_load_dwordx4 v173, s[36:39], s62 offen offset:1024 lds\n"
    "s_waitcnt lgkmcnt(0)\n"
    "s_waitcnt vmcnt(10)\n"
    "s_barrier\n"
    "v_mfma_f32_16x16x32_bf16 v[48:51], a[0:3], a[88:91], v[48:51]\n"
    "ds_read_b64_tr_b16 a[32:33], v183 offset:0\n"
    "s_lshl_b32 s63, s61, 19\n"
    "v_mfma_f32_16x16x32_bf16 v[48:51], a[4:7], a[92:95], v[48:51]\n"
    "ds_read_b64_tr_b16 a[34:35], v183 offset:1024\n"
    "s_add_u32 m0, s66, 106496\n"
    "v_mfma_f32_16x16x32_bf16 v[52:55], a[8:11], a[88:91], v[52:55]\n"
    "ds_read_b64_tr_b16 a[36:37], v183 offset:8192\n"
    "s_add_u32 s63, s63, s68\n"
    "v_mfma_f32_16x16x32_bf16 v[52:55], a[12:15], a[92:95], v[52:55]\n"
    "ds_read_b64_tr_b16 a[38:39], v183 offset:9216\n"
    "buffer_load_dwordx4 v174, s[40:43], s63 offen offset:0 lds\n"
    "v_mfma_f32_16x16x32_bf16 v[56:59], a[16:19], a[88:91], v[56:59]\n"
    "ds_read_b128 a[64:67], v180 offset:0\n"
    "s_lshl_b32 s62, s61, 7\n"
    "v_mfma_f32_16x16x32_bf16 v[56:59], a[20:23], a[92:95], v[56:59]\n"
    "ds_read_b128 a[68:71], v180 offset:256\n"
    "s_add_u32 s62, s62, s67\n"
    "v_mfma_f32_16x16x32_bf16 v[60:63], a[24:27], a[88:91], v[60:63]\n"
    "ds_read_b64_tr_b16 a[40:41], v186 offset:0\n"
    "s_add_u32 s61, s61, 1\n"
    "v_mfma_f32_16x16x32_bf16 v[60:63], a[28:31], a[92:95], v[60:63]\n"
    "ds_read_b64_tr_b16 a[42:43], v186 offset:1024\n"
    "buffer_load_dwordx4 v175, s[40:43], s63 offen offset:1024 lds\n"
    "v_mfma_f32_16x16x32_bf16 v[76:79], a[24:27], a[96:99], v[76:79]\n"
    "ds_read_b64_tr_b16 a[44:45], v186 offset:8192\n"
    "s_cmp_eq_u32 s61, 10\n"
    "s_cselect_b32 s61, 0, s61\n"
    "v_mfma_f32_16x16x32_bf16 v[76:79], a[28:31], a[100:103], v[76:79]\n"
    "ds_read_b64_tr_b16 a[46:47], v186 offset:9216\n"
    "s_add_u32 s74, s64, 0\n"
    "v_mfma_f32_16x16x32_bf16 v[72:75], a[16:19], a[96:99], v[72:75]\n"
    "ds_read_b64_tr_b16 a[48:49], v183 offset:256\n"
    "buffer_load_dwordx4 v176, s[40:43], s63 offen offset:2048 lds\n"
    "v_mfma_f32_16x16x32_bf16 v[72:75], a[20:23], a[100:103], v[72:75]\n"
    "ds_read_b64_tr_b16 a[50:51], v183 offset:1280\n"
    "v_mfma_f32_16x16x32_bf16 v[68:71], a[8:11], a[96:99], v[68:71]\n"
    "ds_read_b64_tr_b16 a[52:53], v183 offset:8448\n"
    "v_mfma_f32_16x16x32_bf16 v[68:71], a[12:15], a[100:103], v[68:71]\n"
    "ds_read_b64_tr_b16 a[54:55], v183 offset:9472\n"
    "buffer_load_dwordx4 v177, s[40:43], s63 offen offset:3072 lds\n"
    "v_mfma_f32_16x16x32_bf16 v[64:67], a[0:3], a[96:99], v[64:67]\n"
    "ds_read_b64_tr_b16 a[56:57], v186 offset:256\n"
    "s_add_u32 m0, s69, 81920\n"
    "v_mfma_f32_16x16x32_bf16 v[64:67], a[4:7], a[100:103], v[64:67]\n"
    "ds_read_b64_tr_b16 a[58:59], v186 offset:1280\n"
    "v_mfma_f32_16x16x32_bf16 v[80:83], a[0:3], a[104:107], v[80:83]\n"
    "ds_read_b64_tr_b16 a[60:61], v186 offset:8448\n"
    "buffer_load_dwordx4 v168, s[36:39], s62 offen offset:0 lds\n"
    "v_mfma_f32_16x16x32_bf16 v[80:83], a[4:7], a[108:111], v[80:83]\n"
    "ds_read_b64_tr_b16 a[62:63], v186 offset:9472\n"
    "v_mfma_f32_16x16x32_bf16 v[84:87], a[8:11], a[104:107], v[84:87]\n"
    "ds_read_b128 a[72:75], v180 offset:2048\n"
    "v_mfma_f32_16x16x32_bf16 v[84:87], a[12:15], a[108:111], v[84:87]\n"
    "ds_read_b128 a[76:79], v180 offset:2304\n"
    "buffer_load_dwordx4 v169, s[36:39], s62 offen offset:1024 lds\n"
    "v_mfma_f32_16x16x32_bf16 v[88:91], a[16:19], a[104:107], v[88:91]\n"
    "ds_read_b128 a[80:83], v180 offset:4096\n"
    "v_mfma_f32_16x16x32_bf16 v[88:91], a[20:23], a[108:111], v[88:91]\n"
    "ds_read_b128 a[84:87], v180 offset:4352\n"
    "v_mfma_f32_16x16x32_bf16 v[92:95], a[24:27], a[104:107], v[92:95]\n"
    "ds_read_b128 a[88:91], v180 offset:6144\n"
    "buffer_load_dwordx4 v170, s[36:39], s62 offen offset:2048 lds\n"
    "v_mfma_f32_16x16x32_bf16 v[92:95], a[28:31], a[108:111], v[92:95]\n"
    "ds_read_b128 a[92:95], v180 offset:6400\n"
    "s_waitcnt lgkmcnt(8)\n"
    "v_mfma_f32_16x16x32_bf16 v[0:3], a[32:35], a[64:67], v[0:3]\n"
    "ds_read_b128 a[96:99], v180 offset:8192\n"
    "v_mfma_f32_16x16x32_bf16 v[0:3], a[36:39], a[68:71], v[0:3]\n"
    "ds_read_b128 a[100:103], v180 offset:8448\n"
    "buffer_load_dwordx4 v171, s[36:39], s62 offen offset:3072 lds\n"
    "v_mfma_f32_16x16x32_bf16 v[4:7], a[40:43], a[64:67], v[4:7]\n"
    "ds_read_b128 a[104:107], v180 offset:10240\n"
    "s_add_u32 m0, s69, 86016\n"
    "v_mfma_f32_16x16x32_bf16 v[4:7], a[44:47], a[68:71], v[4:7]\n"
    "ds_read_b128 a[108:111], v180 offset:10496\n"
    "v_mfma_f32_16x16x32_bf16 v[8:11], a[48:51], a[64:67], v[8:11]\n"
    "buffer_load_dwordx4 v172, s[36:39], s62 offen offset:0 lds\n"
    "v_mfma_f32_16x16x32_bf16 v[8:11], a[52:55], a[68:71], v[8:11]\n"
    "v_mfma_f32_16x16x32_bf16 v[12:15], a[56:59], a[64:67], v[12:15]\n"
    "s_waitcnt lgkmcnt(6)\n"
    "v_mfma_f32_16x16x32_bf16 v[12:15], a[60:63], a[68:71], v[12:15]\n"
    "buffer_load_dwordx4 v173, s[36:39], s62 offen offset:1024 lds\n"
    "v_mfma_f32_16x16x32_bf16 v[28:31], a[56:59], a[72:75], v[28:31]\n"
    "s_add_u32 m0, s64, 0\n"
    "v_mfma_f32_16x16x32_bf16 v[28:31], a[60:63], a[76:79], v[28:31]\n"
    "v_mfma_f32_16x16x32_bf16 v[24:27], a[48:51], a[72:75], v[24:27]\n"
    "buffer_load_dwordx4 v253, s[44:47], s74 offen offset:0 nt lds\n"
    "v_mfma_f32_16x16x32_bf16 v[24:27], a[52:55], a[76:79], v[24:27]\n"
    "v_mfma_f32_16x16x32_bf16 v[20:23], a[40:43], a[72:75], v[20:23]\n"
    "v_mfma_f32_16x16x32_bf16 v[20:23], a[44:47], a[76:79], v[20:23]\n"
    "buffer_load_dwordx4 v254, s[44:47], s74 offen offset:1024 nt lds\n"
    "v_mfma_f32_16x16x32_bf16 v[16:19], a[32:35], a[72:75], v[16:19]\n"
    "v_mfma_f32_16x16x32_bf16 v[16:19], a[36:39], a[76:79], v[16:19]\n"
    "v_mfma_f32_16x16x32_bf16 v[32:35], a[32:35], a[80:83], v[32:35]\n"
    "buffer_load_dwordx4 v253, s[44:47], s74 offen offset:2048 nt lds\n"
    "v_mfma_f32_16x16x32_bf16 v[32:35], a[36:39], a[84:87], v[32:35]\n"
    "v_mfma_f32_16x16x32_bf16 v[36:39], a[40:43], a[80:83], v[36:39]\n"
    "v_mfma_f32_16x16x32_bf16 v[36:39], a[44:47], a[84:87], v[36:39]\n"
    "buffer_load_dwordx4 v254, s[44:47], s74 offen offset:3072 nt lds\n"
    "v_mfma_f32_16x16x32_bf16 v[40:43], a[48:51], a[80:83], v[40:43]\n"
    "v_mfma_f32_16x16x32_bf16 v[40:43], a[52:55], a[84:87], v[40:43]\n"
    "v_mfma_f32_16x16x32_bf16 v[44:47], a[56:59], a[80:83], v[44:47]\n"
    "v_mfma_f32_16x16x32_bf16 v[44:47], a[60:63], a[84:87], v[44:47]\n"
    "s_waitcnt lgkmcnt(0)\n"
    "s_waitcnt vmcnt(14)\n"
    "s_barrier\n"
    "s_memtime s[72:73]\n"
    "s_memrealtime s[92:93]\n"
    "v_mfma_f32_16x16x32_bf16 v[48:51], a[32:35], a[88:91], v[48:51]\n"
    "ds_read_b64_tr_b16 a[0:1], v181 offset:0\n"
    "s_lshl_b32 s63, s61, 19\n"
    "v_mfma_f32_16x16x32_bf16 v[48:51], a[36:39], a[92:95], v[48:51]\n"
    "ds_read_b64_tr_b16 a[2:3], v181 offset:1024\n"
    "s_add_u32 m0, s66, 147456\n"
    "v_mfma_f32_16x16x32_bf16 v[52:55], a[40:43], a[88:91], v[52:55]\n"
    "ds_read_b64_tr_b16 a[4:5], v181 offset:8192\n"
    "s_add_u32 s63, s63, s68\n"
    "v_mfma_f32_16x16x32_bf16 v[52:55], a[44:47], a[92:95], v[52:55]\n"
    "ds_read_b64_tr_b16 a[6:7], v181 offset:9216\n"
    "buffer_load_dwordx4 v174, s[40:43], s63 offen offset:0 lds\n"
    "v_mfma_f32_16x16x32_bf16 v[56:59], a[48:51], a[88:91], v[56:59]\n"
    "ds_read_b128 a[64:67], v178 offset:0\n"
    "s_lshl_b32 s62, s61, 7\n"
    "v_mfma_f32_16x16x32_bf16 v[56:59], a[52:55], a[92:95], v[56:59]\n"
    "ds_read_b128 a[68:71], v178 offset:256\n"
    "s_add_u32 s62, s62, s67\n"
    "v_mfma_f32_16x16x32_bf16 v[60:63], a[56:59], a[88:91], v[60:63]\n"
    "ds_read_b64_tr_b16 a[8:9], v184 offset:0\n"
    "s_add_u32 s61, s61, 1\n"
    "v_mfma_f32_16x16x32_bf16 v[60:63], a[60:63], a[92:95], v[60:63]\n"
    "ds_read_b64_tr_b16 a[10:11], v184 offset:1024\n"
    "s_cmp_eq_u32 s61, 10\n"
    "s_cselect_b32 s61, 0, s61\n"
    "v_mfma_f32_16x16x32_bf16 v[76:79], a[56:59], a[96:99], v[76:79]\n"
    "ds_read_b64_tr_b16 a[12:13], v184 offset:8192\n"
    "buffer_load_dwordx4 v175, s[40:43], s63 offen offset:1024 lds\n"
    "v_mfma_f32_16x16x32_bf16 v[76:79], a[60:63], a[100:103], v[76:79]\n"
    "ds_read_b64_tr_b16 a[14:15], v184 offset:9216\n"
    "v_mfma_f32_16x16x32_bf16 v[72:75], a[48:51], a[96:99], v[72:75]\n"
    "ds_read_b64_tr_b16 a[16:17], v181 offset:256\n"
    "v_mfma_f32_16x16x32_bf16 v[72:75], a[52:55], a[100:103], v[72:75]\n"
    "ds_read_b64_tr_b16 a[18:19], v181 offset:1280\n"
    "v_mfma_f32_16x16x32_bf16 v[68:71], a[40:43], a[96:99], v[68:71]\n"
    "ds_read_b64_tr_b16 a[20:21], v181 offset:8448\n"
    "v_mfma_f32_16x16x32_bf16 v[68:71], a[44:47], a[100:103], v[68:71]\n"
    "ds_read_b64_tr_b16 a[22:23], v181 offset:9472\n"
    "buffer_load_dwordx4 v176, s[40:43], s63 offen offset:2048 lds\n"
    "v_mfma_f32_16x16x32_bf16 v[64:67], a[32:35], a[96:99], v[64:67]\n"
    "ds_read_b64_tr_b16 a[24:25], v184 offset:256\n"
    "v_mfma_f32_16x16x32_bf16 v[64:67], a[36:39], a[100:103], v[64:67]\n"
    "ds_read_b64_tr_b16 a[26:27], v184 offset:1280\n"
    "v_mfma_f32_16x16x32_bf16 v[80:83], a[32:35], a[104:107], v[80:83]\n"
    "ds_read_b64_tr_b16 a[28:29], v184 offset:8448\n"
    "v_mfma_f32_16x16x32_bf16 v[80:83], a[36:39], a[108:111], v[80:83]\n"
    "ds_read_b64_tr_b16 a[30:31], v184 offset:9472\n"
    "v_mfma_f32_16x16x32_bf16 v[84:87], a[40:43], a[104:107], v[84:87]\n"
    "ds_read_b128 a[72:75], v178 offset:2048\n"
    "buffer_load_dwordx4 v177, s[40:43], s63 offen offset:3072 lds\n"
    "v_mfma_f32_16x16x32_bf16 v[84:87], a[44:47], a[108:111], v[84:87]\n"
    "ds_read_b128 a[76:79], v178 offset:2304\n"
    "s_add_u32 m0, s69, 122880\n"
    "v_mfma_f32_16x16x32_bf16 v[88:91], a[48:51], a[104:107], v[88:91]\n"
    "ds_read_b128 a[80:83], v178 offset:4096\n"
    "v_mfma_f32_16x16x32_bf16 v[88:91], a[52:55], a[108:111], v[88:91]\n"
    "ds_read_b128 a[84:87], v178 offset:4352\n"
    "v_mfma_f32_16x16x32_bf16 v[92:95], a[56:59], a[104:107], v[92:95]\n"
    "ds_read_b128 a[88:91], v178 offset:6144\n"
    "v_mfma_f32_16x16x32_bf16 v[92:95], a[60:63], a[108:111], v[92:95]\n"
    "ds_read_b128 a[92:95], v178 offset:6400\n"
    "buffer_load_dwordx4 v168, s[36:39], s62 offen offset:0 lds\n"
    "s_waitcnt lgkmcnt(8)\n"
    "v_mfma_f32_16x16x32_bf16 v[0:3], a[0:3], a[64:67], v[0:3]\n"
    "ds_read_b128 a[96:99], v178 offset:8192\n"
    "v_mfma_f32_16x16x32_bf16 v[0:3], a[4:7], a[68:71], v[0:3]\n"
    "ds_read_b128 a[100:103], v178 offset:8448\n"
    "v_mfma_f32_16x16x32_bf16 v[4:7], a[8:11], a[64:67], v[4:7]\n"
    "ds_read_b128 a[104:107], v178 offset:10240\n"
    "v_mfma_f32_16x16x32_bf16 v[4:7], a[12:15], a[68:71], v[4:7]\n"
    "ds_read_b128 a[108:111], v178 offset:10496\n"
    "v_mfma_f32_16x16x32_bf16 v[8:11], a[16:19], a[64:67], v[8:11]\n"
    "buffer_load_dwordx4 v169, s[36:39], s62 offen offset:1024 lds\n"
    "v_mfma_f32_16x16x32_bf16 v[8:11], a[20:23], a[68:71], v[8:11]\n"
    "v_mfma_f32_16x16x32_bf16 v[12:15], a[24:27], a[64:67], v[12:15]\n"
    "s_waitcnt lgkmcnt(7)\n"
    "v_mfma_f32_16x16x32_bf16 v[12:15], a[28:31], a[68:71], v[12:15]\n"
    "v_mfma_f32_16x16x32_bf16 v[28:31], a[24:27], a[72:75], v[28:31]\n"
    "v_mfma_f32_16x16x32_bf16 v[28:31], a[28:31], a[76:79], v[28:31]\n"
    "buffer_load_dwordx4 v170, s[36:39], s62 offen offset:2048 lds\n"
    "v_mfma_f32_16x16x32_bf16 v[24:27], a[16:19], a[72:75], v[24:27]\n"
    "v_mfma_f32_16x16x32_bf16 v[24:27], a[20:23], a[76:79], v[24:27]\n"
    "v_mfma_f32_16x16x32_bf16 v[20:23], a[8:11], a[72:75], v[20:23]\n"
    "v_mfma_f32_16x16x32_bf16 v[20:23], a[12:15], a[76:79], v[20:23]\n"
    "v_mfma_f32_16x16x32_bf16 v[16:19], a[0:3], a[72:75], v[16:19]\n"
    "buffer_load_dwordx4 v171, s[36:39], s62 offen offset:3072 lds\n"
    "s_add_u32 m0, s69, 126976\n"
    "v_mfma_f32_16x16x32_bf16 v[16:19], a[4:7], a[76:79], v[16:19]\n"
    "v_mfma_f32_16x16x32_bf16 v[32:35], a[0:3], a[80:83], v[32:35]\n"
    "s_waitcnt lgkmcnt(1)\n"
    "v_mfma_f32_16x16x32_bf16 v[32:35], a[4:7], a[84:87], v[32:35]\n"
    "v_mfma_f32_16x16x32_bf16 v[36:39], a[8:11], a[80:83], v[36:39]\n"
    "v_mfma_f32_16x16x32_bf16 v[36:39], a[12:15], a[84:87], v[36:39]\n"
    "buffer_load_dwordx4 v172, s[36:39], s62 offen offset:0 lds\n"
    "v_mfma_f32_16x16x32_bf16 v[40:43], a[16:19], a[80:83], v[40:43]\n"
    "v_mfma_f32_16x16x32_bf16 v[40:43], a[20:23], a[84:87], v[40:43]\n"
    "v_mfma_f32_16x16x32_bf16 v[44:47], a[24:27], a[80:83], v[44:47]\n"
    "v_mfma_f32_16x16x32_bf16 v[44:47], a[28:31], a[84:87], v[44:47]\n"
    "buffer_load_dwordx4 v173, s[36:39], s62 offen offset:1024 lds\n"
    "s_waitcnt lgkmcnt(0)\n"
    "s_waitcnt vmcnt(14)\n"
    "s_barrier\n"
    "s_sub_u32 s72, s72, s82\n"
    "s_sub_u32 s92, s92, s83\n"
    "s_lshr_b32 s72, s72, 3\n"
    "s_lshl_b32 s72, s72, 16\n"
    "s_or_b32 s60, s72, s92\n"
    "v_mfma_f32_16x16x32_bf16 v[48:51], a[0:3], a[88:91], v[48:51]\n"
    "ds_read_b64_tr_b16 a[32:33], v182 offset:0\n"
    "s_lshl_b32 s63, s61, 19\n"
    "v_mfma_f32_16x16x32_bf16 v[48:51], a[4:7], a[92:95], v[48:51]\n"
    "ds_read_b64_tr_b16 a[34:35], v182 offset:1024\n"
    "s_add_u32 m0, s66, 65536\n"
    "v_mfma_f32_16x16x32_bf16 v[52:55], a[8:11], a[88:91], v[52:55]\n"
    "ds_read_b64_tr_b16 a[36:37], v182 offset:8192\n"
    "s_add_u32 s63, s63, s68\n"
    "v_mfma_f32_16x16x32_bf16 v[52:55], a[12:15], a[92:95], v[52:55]\n"
    "ds_read_b64_tr_b16 a[38:39], v182 offset:9216\n"
    "buffer_load_dwordx4 v174, s[40:43], s63 offen offset:0 lds\n"
    "v_mfma_f32_16x16x32_bf16 v[56:59], a[16:19], a[88:91], v[56:59]\n"
    "ds_read_b128 a[64:67], v179 offset:0\n"
    "s_lshl_b32 s62, s61, 7\n"
    "v_mfma_f32_16x16x32_bf16 v[56:59], a[20:23], a[92:95], v[56:59]\n"
    "ds_read_b128 a[68:71], v179 offset:256\n"
    "s_add_u32 s62, s62, s67\n"
    "v_mfma_f32_16x16x32_bf16 v[60:63], a[24:27], a[88:91], v[60:63]\n"
    "ds_read_b64_tr_b16 a[40:41], v185 offset:0\n"
    "s_add_u32 s61, s61, 1\n"
    "v_mfma_f32_16x16x32_bf16 v[60:63], a[28:31], a[92:95], v[60:63]\n"
    "ds_read_b64_tr_b16 a[42:43], v185 offset:1024\n"
    "s_cmp_eq_u32 s61, 10\n"
    "s_cselect_b32 s61, 0, s61\n"
    "v_mfma_f32_16x16x32_bf16 v[76:79], a[24:27], a[96:99], v[76:79]\n"
    "ds_read_b64_tr_b16 a[44:45], v185 offset:8192\n"
    "buffer_load_dwordx4 v175, s[40:43], s63 offen offset:1024 lds\n"
    "v_mfma_f32_16x16x32_bf16 v[76:79], a[28:31], a[100:103], v[76:79]\n"
    "ds_read_b64_tr_b16 a[46:47], v185 offset:9216\n"
    "v_mfma_f32_16x16x32_bf16 v[72:75], a[16:19], a[96:99], v[72:75]\n"
    "ds_read_b64_tr_b16 a[48:49], v182 offset:256\n"
    "v_mfma_f32_16x16x32_bf16 v[72:75], a[20:23], a[100:103], v[72:75]\n"
    "ds_read_b64_tr_b16 a[50:51], v182 offset:1280\n"
    "v_mfma_f32_16x16x32_bf16 v[68:71], a[8:11], a[96:99], v[68:71]\n"
    "ds_read_b64_tr_b16 a[52:53], v182 offset:8448\n"
    "v_mfma_f32_16x16x32_bf16 v[68:71], a[12:15], a[100:103], v[68:71]\n"
    "ds_read_b64_tr_b16 a[54:55], v182 offset:9472\n"
    "buffer_load_dwordx4 v176, s[40:43], s63 offen offset:2048 lds\n"
    "v_mfma_f32_16x16x32_bf16 v[64:67], a[0:3], a[96:99], v[64:67]\n"
    "ds_read_b64_tr_b16 a[56:57], v185 offset:256\n"
    "v_mfma_f32_16x16x32_bf16 v[64:67], a[4:7], a[100:103], v[64:67]\n"
    "ds_read_b64_tr_b16 a[58:59], v185 offset:1280\n"
    "v_mfma_f32_16x16x32_bf16 v[80:83], a[0:3], a[104:107], v[80:83]\n"
    "ds_read_b64_tr_b16 a[60:61], v185 offset:8448\n"
    "v_mfma_f32_16x16x32_bf16 v[80:83], a[4:7], a[108:111], v[80:83]\n"
    "ds_read_b64_tr_b16 a[62:63], v185 offset:9472\n"
    "v_mfma_f32_16x16x32_bf16 v[84:87], a[8:11], a[104:107], v[84:87]\n"
    "ds_read_b128 a[72:75], v179 offset:2048\n"
    "buffer_load_dwordx4 v177, s[40:43], s63 offen offset:3072 lds\n"
    "v_mfma_f32_16x16x32_bf16 v[84:87], a[12:15], a[108:111], v[84:87]\n"
    "ds_read_b128 a[76:79], v179 offset:2304\n"
    "s_add_u32 m0, s69, 40960\n"
    "v_mfma_f32_16x16x32_bf16 v[88:91], a[16:19], a[104:107], v[88:91]\n"
    "ds_read_b128 a[80:83], v179 offset:4096\n"
    "v_mfma_f32_16x16x32_bf16 v[88:91], a[20:23], a[108:111], v[88:91]\n"
    "ds_read_b128 a[84:87], v179 offset:4352\n"
    "v_mfma_f32_16x16x32_bf16 v[92:95], a[24:27], a[104:107], v[92:95]\n"
    "ds_read_b128 a[88:91], v179 offset:6144\n"
    "v_mfma_f32_16x16x32_bf16 v[92:95], a[28:31], a[108:111], v[92:95]\n"
    "ds_read_b128 a[92:95], v179 offset:6400\n"
    "buffer_load_dwordx4 v168, s[36:39], s62 offen offset:0 lds\n"
    "s_waitcnt lgkmcnt(8)\n"
    "v_mfma_f32_16x16x32_bf16 v[0:3], a[32:35], a[64:67], v[0:3]\n"
    "ds_read_b128 a[96:99], v179 offset:8192\n"
    "v_mfma_f32_16x16x32_bf16 v[0:3], a[36:39], a[68:71], v[0:3]\n"
    "ds_read_b128 a[100:103], v179 offset:8448\n"
    "v_mfma_f32_16x16x32_bf16 v[4:7], a[40:43], a[64:67], v[4:7]\n"
    "ds_read_b128 a[104:107], v179 offset:10240\n"
    "v_mfma_f32_16x16x32_bf16 v[4:7], a[44:47], a[68:71], v[4:7]\n"
    "ds_read_b128 a[108:111], v179 offset:10496\n"
    "v_mfma_f32_16x16x32_bf16 v[8:11], a[48:51], a[64:67], v[8:11]\n"
    "buffer_load_dwordx4 v169, s[36:39], s62 offen offset:1024 lds\n"
    "v_mfma_f32_16x16x32_bf16 v[8:11], a[52:55], a[68:71], v[8:11]\n"
    "v_mfma_f32_16x16x32_bf16 v[12:15], a[56:59], a[64:67], v[12:15]\n"
    "s_waitcnt lgkmcnt(7)\n"
    "v_mfma_f32_16x16x32_bf16 v[12:15], a[60:63], a[68:71], v[12:15]\n"
    "v_mfma_f32_16x16x32_bf16 v[28:31], a[56:59], a[72:75], v[28:31]\n"
    "v_mfma_f32_16x16x32_bf16 v[28:31], a[60:63], a[76:79], v[28:31]\n"
    "buffer_load_dwordx4 v170, s[36:39], s62 offen offset:2048 lds\n"
    "v_mfma_f32_16x16x32_bf16 v[24:27], a[48:51], a[72:75], v[24:27]\n"
    "v_mfma_f32_16x16x32_bf16 v[24:27], a[52:55], a[76:79], v[24:27]\n"
    "v_mfma_f32_16x16x32_bf16 v[20:23], a[40:43], a[72:75], v[20:23]\n"
    "v_mfma_f32_16x16x32_bf16 v[20:23], a[44:47], a[76:79], v[20:23]\n"
    "v_mfma_f32_16x16x32_bf16 v[16:19], a[32:35], a[72:75], v[16:19]\n"
    "buffer_load_dwordx4 v171, s[36:39], s62 offen offset:3072 lds\n"
    "s_add_u32 m0, s69, 45056\n"
    "v_mfma_f32_16x16x32_bf16 v[16:19], a[36:39], a[76:79], v[16:19]\n"
    "v_mfma_f32_16x16x32_bf16 v[32:35], a[32:35], a[80:83], v[32:35]\n"
    "s_waitcnt lgkmcnt(1)\n"
    "v_mfma_f32_16x16x32_bf16 v[32:35], a[36:39], a[84:87], v[32:35]\n"
    "v_mfma_f32_16x16x32_bf16 v[36:39], a[40:43], a[80:83], v[36:39]\n"
    "v_mfma_f32_16x16x32_bf16 v[36:39], a[44:47], a[84:87], v[36:39]\n"
    "buffer_load_dwordx4 v172, s[36:39], s62 offen offset:0 lds\n"
    "v_mfma_f32_16x16x32_bf16 v[40:43], a[48:51], a[80:83], v[40:43]\n"
    "v_mfma_f32_16x16x32_bf16 v[40:43], a[52:55], a[84:87], v[40:43]\n"
    "v_mfma_f32_16x16x32_bf16 v[44:47], a[56:59], a[80:83], v[44:47]\n"
    "v_mfma_f32_16x16x32_bf16 v[44:47], a[60:63], a[84:87], v[44:47]\n"
    "buffer_load_dwordx4 v173, s[36:39], s62 offen offset:1024 lds\n"
    "s_waitcnt lgkmcnt(0)\n"
    "s_waitcnt vmcnt(10)\n"
    "s_barrier\n"
    "v_mfma_f32_16x16x32_bf16 v[48:51], a[32:35], a[88:91], v[48:51]\n"
    "ds_read_b64_tr_b16 a[0:1], v183 offset:0\n"
    "s_lshl_b32 s63, s61, 19\n"
    "v_mfma_f32_16x16x32_bf16 v[48:51], a[36:39], a[92:95], v[48:51]\n"
    "ds_read_b64_tr_b16 a[2:3], v183 offset:1024\n"
    "s_add_u32 m0, s66, 106496\n"
    "v_mfma_f32_16x16x32_bf16 v[52:55], a[40:43], a[88:91], v[52:55]\n"
    "ds_read_b64_tr_b16 a[4:5], v183 offset:8192\n"
    "s_add_u32 s63, s63, s68\n"
    "v_mfma_f32_16x16x32_bf16 v[52:55], a[44:47], a[92:95], v[52:55]\n"
    "ds_read_b64_tr_b16 a[6:7], v183 offset:9216\n"
    "buffer_load_dwordx4 v174, s[40:43], s63 offen offset:0 lds\n"
    "v_mfma_f32_16x16x32_bf16 v[56:59], a[48:51], a[88:91], v[56:59]\n"
    "ds_read_b128 a[64:67], v180 offset:0\n"
    "s_lshl_b32 s62, s61, 7\n"
    "v_mfma_f32_16x16x32_bf16 v[56:59], a[52:55], a[92:95], v[56:59]\n"
    "ds_read_b128 a[68:71], v180 offset:256\n"
    "s_add_u32 s62, s62, s67\n"
    "v_mfma_f32_16x16x32_bf16 v[60:63], a[56:59], a[88:91], v[60:63]\n"
    "ds_read_b64_tr_b16 a[8:9], v186 offset:0\n"
    "s_add_u32 s61, s61, 1\n"
    "v_mfma_f32_16x16x32_bf16 v[60:63], a[60:63], a[92:95], v[60:63]\n"
    "ds_read_b64_tr_b16 a[10:11], v186 offset:1024\n"
    "buffer_load_dwordx4 v175, s[40:43], s63 offen offset:1024 lds\n"
    "v_mfma_f32_16x16x32_bf16 v[76:79], a[56:59], a[96:99], v[76:79]\n"
    "ds_read_b64_tr_b16 a[12:13], v186 offset:8192\n"
    "s_cmp_eq_u32 s61, 10\n"
    "s_cselect_b32 s61, 0, s61\n"
    "v_mfma_f32_16x16x32_bf16 v[76:79], a[60:63], a[100:103], v[76:79]\n"
    "ds_read_b64_tr_b16 a[14:15], v186 offset:9216\n"
    "s_add_u32 s74, s64, 4096\n"
    "v_mfma_f32_16x16x32_bf16 v[72:75], a[48:51], a[96:99], v[72:75]\n"
    "ds_read_b64_tr_b16 a[16:17], v183 offset:256\n"
    "buffer_load_dwordx4 v176, s[40:43], s63 offen offset:2048 lds\n"
    "v_mfma_f32_16x16x32_bf16 v[72:75], a[52:55], a[100:103], v[72:75]\n"
    "ds_read_b64_tr_b16 a[18:19], v183 offset:1280\n"
    "v_readfirstlane_b32 s76, v188\n"
    "v_mfma_f32_16x16x32_bf16 v[68:71], a[40:43], a[96:99], v[68:71]\n"
    "ds_read_b64_tr_b16 a[20:21], v183 offset:8448\n"
    "v_readfirstlane_b32 s77, v189\n"
    "v_mfma_f32_16x16x32_bf16 v[68:71], a[44:47], a[100:103], v[68:71]\n"
    "ds_read_b64_tr_b16 a[22:23], v183 offset:9472\n"
    "s_nop 1\n"
    "v_mfma_f32_16x16x32_bf16 v[64:67], a[32:35], a[96:99], v[64:67]\n"
    "ds_read_b64_tr_b16 a[24:25], v186 offset:256\n"
    "buffer_load_dwordx4 v177, s[40:43], s63 offen offset:3072 lds\n"
    "v_mfma_f32_16x16x32_bf16 v[64:67], a[36:39], a[100:103], v[64:67]\n"
    "ds_read_b64_tr_b16 a[26:27], v186 offset:1280\n"
    "s_add_u32 m0, s69, 81920\n"
    "v_mfma_f32_16x16x32_bf16 v[80:83], a[32:35], a[104:107], v[80:83]\n"
    "ds_read_b64_tr_b16 a[28:29], v186 offset:8448\n"
    "s_mul_i32 s78, s76, 0x4c957f2d\n"
    "v_mfma_f32_16x16x32_bf16 v[80:83], a[36:39], a[108:111], v[80:83]\n"
    "ds_read_b64_tr_b16 a[30:31], v186 offset:9472\n"
    "s_mul_hi_u32 s79, s76, 0x4c957f2d\n"
    "v_mfma_f32_16x16x32_bf16 v[84:87], a[40:43], a[104:107], v[84:87]\n"
    "ds_read_b128 a[72:75], v180 offset:2048\n"
    "buffer_load_dwordx4 v168, s[36:39], s62 offen offset:0 lds\n"
    "v_mfma_f32_16x16x32_bf16 v[84:87], a[44:47], a[108:111], v[84:87]\n"
    "ds_read_b128 a[76:79], v180 offset:2304\n"
    "s_mul_i32 s76, s76, 0x5851f42d\n"
    "v_mfma_f32_16x16x32_bf16 v[88:91], a[48:51], a[104:107], v[88:91]\n"
    "ds_read_b128 a[80:83], v180 offset:4096\n"
    "s_add_u32 s79, s79, s76\n"
    "v_mfma_f32_16x16x32_bf16 v[88:91], a[52:55], a[108:111], v[88:91]\n"
    "ds_read_b128 a[84:87], v180 offset:4352\n"
    "s_mul_i32 s76, s77, 0x4c957f2d\n"
    "v_mfma_f32_16x16x32_bf16 v[92:95], a[56:59], a[104:107], v[92:95]\n"
    "ds_read_b128 a[88:91], v180 offset:6144\n"
    "buffer_load_dwordx4 v169, s[36:39], s62 offen offset:1024 lds\n"
    "v_mfma_f32_16x16x32_bf16 v[92:95], a[60:63], a[108:111], v[92:95]\n"
    "ds_read_b128 a[92:95], v180 offset:6400\n"
    "s_add_u32 s79, s79, s76\n"
    "s_waitcnt lgkmcnt(6)\n"
    "v_mfma_f32_16x16x32_bf16 v[0:3], a[0:3], a[64:67], v[0:3]\n"
    "ds_read_b128 a[96:99], v180 offset:8192\n"
    "s_add_u32 s78, s78, 0xf767814f\n"
    "s_addc_u32 s79, s79, 0x14057b7e\n"
    "v_mfma_f32_16x16x32_bf16 v[0:3], a[4:7], a[68:71], v[0:3]\n"
    "ds_read_b128 a[100:103], v180 offset:8448\n"
    "buffer_load_dwordx4 v170, s[36:39], s62 offen offset:2048 lds\n"
    "v_mfma_f32_16x16x32_bf16 v[4:7], a[8:11], a[64:67], v[4:7]\n"
    "ds_read_b128 a[104:107], v180 offset:10240\n"
    "v_mfma_f32_16x16x32_bf16 v[4:7], a[12:15], a[68:71], v[4:7]\n"
    "ds_read_b128 a[108:111], v180 offset:10496\n"
    "v_mfma_f32_16x16x32_bf16 v[8:11], a[16:19], a[64:67], v[8:11]\n"
    "buffer_load_dwordx4 v171, s[36:39], s62 offen offset:3072 lds\n"
    "v_mfma_f32_16x16x32_bf16 v[8:11], a[20:23], a[68:71], v[8:11]\n"
    "s_add_u32 m0, s69, 86016\n"
    "v_mfma_f32_16x16x32_bf16 v[12:15], a[24:27], a[64:67], v[12:15]\n"
    "v_mfma_f32_16x16x32_bf16 v[12:15], a[28:31], a[68:71], v[12:15]\n"
    "buffer_load_dwordx4 v172, s[36:39], s62 offen offset:0 lds\n"
    "s_waitcnt lgkmcnt(4)\n"
    "v_mfma_f32_16x16x32_bf16 v[28:31], a[24:27], a[72:75], v[28:31]\n"
    "v_mfma_f32_16x16x32_bf16 v[28:31], a[28:31], a[76:79], v[28:31]\n"
    "v_mfma_f32_16x16x32_bf16 v[24:27], a[16:19], a[72:75], v[24:27]\n"
    "buffer_load_dwordx4 v173, s[36:39], s62 offen offset:1024 lds\n"
    "v_mfma_f32_16x16x32_bf16 v[24:27], a[20:23], a[76:79], v[24:27]\n"
    "s_add_u32 m0, s64, 4096\n"
    "v_mfma_f32_16x16x32_bf16 v[20:23], a[8:11], a[72:75], v[20:23]\n"
    "v_mfma_f32_16x16x32_bf16 v[20:23], a[12:15], a[76:79], v[20:23]\n"
    "buffer_load_dwordx4 v253, s[44:47], s74 offen offset:0 nt lds\n"
    "v_mfma_f32_16x16x32_bf16 v[16:19], a[0:3], a[72:75], v[16:19]\n"
    "v_mfma_f32_16x16x32_bf16 v[16:19], a[4:7], a[76:79], v[16:19]\n"
    "v_mfma_f32_16x16x32_bf16 v[32:35], a[0:3], a[80:83], v[32:35]\n"
    "buffer_load_dwordx4 v254, s[44:47], s74 offen offset:1024 nt lds\n"
    "v_mfma_f32_16x16x32_bf16 v[32:35], a[4:7], a[84:87], v[32:35]\n"
    "v_mfma_f32_16x16x32_bf16 v[36:39], a[8:11], a[80:83], v[36:39]\n"
    "v_mfma_f32_16x16x32_bf16 v[36:39], a[12:15], a[84:87], v[36:39]\n"
    "buffer_load_dwordx4 v253, s[44:47], s74 offen offset:2048 nt lds\n"
    "v_mfma_f32_16x16x32_bf16 v[40:43], a[16:19], a[80:83], v[40:43]\n"
    "v_mfma_f32_16x16x32_bf16 v[40:43], a[20:23], a[84:87], v[40:43]\n"
    "v_mfma_f32_16x16x32_bf16 v[44:47], a[24:27], a[80:83], v[44:47]\n"
    "buffer_load_dwordx4 v254, s[44:47], s74 offen offset:3072 nt lds\n"
    "v_mfma_f32_16x16x32_bf16 v[44:47], a[28:31], a[84:87], v[44:47]\n"
    "s_waitcnt lgkmcnt(0)\n"
    "s_waitcnt vmcnt(14)\n"
    "s_barrier\n"
    "v_mfma_f32_16x16x32_bf16 v[48:51], a[0:3], a[88:91], v[48:51]\n"
    "ds_read_b64_tr_b16 a[32:33], v181 offset:0\n"
    "s_lshl_b32 s63, s61, 19\n"
    "v_mfma_f32_16x16x32_bf16 v[48:51], a[4:7], a[92:95], v[48:51]\n"
    "ds_read_b64_tr_b16 a[34:35], v181 offset:1024\n"
    "s_add_u32 m0, s66, 147456\n"
    "v_mfma_f32_16x16x32_bf16 v[52:55], a[8:11], a[88:91], v[52:55]\n"
    "ds_read_b64_tr_b16 a[36:37], v181 offset:8192\n"
    "s_add_u32 s63, s63, s68\n"
    "v_mfma_f32_16x16x32_bf16 v[52:55], a[12:15], a[92:95], v[52:55]\n"
    "ds_read_b64_tr_b16 a[38:39], v181 offset:9216\n"
    "buffer_load_dwordx4 v174, s[40:43], s63 offen offset:0 lds\n"
    "v_mfma_f32_16x16x32_bf16 v[56:59], a[16:19], a[88:91], v[56:59]\n"
    "ds_read_b128 a[64:67], v178 offset:0\n"
    "s_lshl_b32 s62, s61, 7\n"
    "v_mfma_f32_16x16x32_bf16 v[56:59], a[20:23], a[92:95], v[56:59]\n"
    "ds_read_b128 a[68:71], v178 offset:256\n"
    "s_add_u32 s62, s62, s67\n"
    "v_mfma_f32_16x16x32_bf16 v[60:63], a[24:27], a[88:91], v[60:63]\n"
    "ds_read_b64_tr_b16 a[40:41], v184 offset:0\n"
    "s_add_u32 s61, s61, 1\n"
    "v_mfma_f32_16x16x32_bf16 v[60:63], a[28:31], a[92:95], v[60:63]\n"
    "ds_read_b64_tr_b16 a[42:43], v184 offset:1024\n"
    "s_cmp_eq_u32 s61, 10\n"
    "s_cselect_b32 s61, 0, s61\n"
    "v_mfma_f32_16x16x32_bf16 v[76:79], a[24:27], a[96:99], v[76:79]\n"
    "ds_read_b64_tr_b16 a[44:45], v184 offset:8192\n"
    "buffer_load_dwordx4 v175, s[40:43], s63 offen offset:1024 lds\n"
    "v_mfma_f32_16x16x32_bf16 v[76:79], a[28:31], a[100:103], v[76:79]\n"
    "ds_read_b64_tr_b16 a[46:47], v184 offset:9216\n"
    "v_mfma_f32_16x16x32_bf16 v[72:75], a[16:19], a[96:99], v[72:75]\n"
    "ds_read_b64_tr_b16 a[48:49], v181 offset:256\n"
    "v_mfma_f32_16x16x32_bf16 v[72:75], a[20:23], a[100:103], v[72:75]\n"
    "ds_read_b64_tr_b16 a[50:51], v181 offset:1280\n"
    "v_mfma_f32_16x16x32_bf16 v[68:71], a[8:11], a[96:99], v[68:71]\n"
    "ds_read_b64_tr_b16 a[52:53], v181 offset:8448\n"
    "v_mfma_f32_16x16x32_bf16 v[68:71], a[12:15], a[100:103], v[68:71]\n"
    "ds_read_b64_tr_b16 a[54:55], v181 offset:9472\n"
    "buffer_load_dwordx4 v176, s[40:43], s63 offen offset:2048 lds\n"
    "v_mfma_f32_16x16x32_bf16 v[64:67], a[0:3], a[96:99], v[64:67]\n"
    "ds_read_b64_tr_b16 a[56:57], v184 offset:256\n"
    "v_mfma_f32_16x16x32_bf16 v[64:67], a[4:7], a[100:103], v[64:67]\n"
    "ds_read_b64_tr_b16 a[58:59], v184 offset:1280\n"
    "v_mfma_f32_16x16x32_bf16 v[80:83], a[0:3], a[104:107], v[80:83]\n"
    "ds_read_b64_tr_b16 a[60:61], v184 offset:8448\n"
    "v_mfma_f32_16x16x32_bf16 v[80:83], a[4:7], a[108:111], v[80:83]\n"
    "ds_read_b64_tr_b16 a[62:63], v184 offset:9472\n"
    "v_mfma_f32_16x16x32_bf16 v[84:87], a[8:11], a[104:107], v[84:87]\n"
    "ds_read_b128 a[72:75], v178 offset:2048\n"
    "buffer_load_dwordx4 v177, s[40:43], s63 offen offset:3072 lds\n"
    "v_mfma_f32_16x16x32_bf16 v[84:87], a[12:15], a[108:111], v[84:87]\n"
    "ds_read_b128 a[76:79], v178 offset:2304\n"
    "s_add_u32 m0, s69, 122880\n"
    "v_mfma_f32_16x16x32_bf16 v[88:91], a[16:19], a[104:107], v[88:91]\n"
    "ds_read_b128 a[80:83], v178 offset:4096\n"
    "v_mfma_f32_16x16x32_bf16 v[88:91], a[20:23], a[108:111], v[88:91]\n"
    "ds_read_b128 a[84:87], v178 offset:4352\n"
    "v_mfma_f32_16x16x32_bf16 v[92:95], a[24:27], a[104:107], v[92:95]\n"
    "ds_read_b128 a[88:91], v178 offset:6144\n"
    "v_mfma_f32_16x16x32_bf16 v[92:95], a[28:31], a[108:111], v[92:95]\n"
    "ds_read_b128 a[92:95], v178 offset:6400\n"
    "buffer_load_dwordx4 v168, s[36:39], s62 offen offset:0 lds\n"
    "s_waitcnt lgkmcnt(8)\n"
    "v_mfma_f32_16x16x32_bf16 v[0:3], a[32:35], a[64:67], v[0:3]\n"
    "ds_read_b128 a[96:99], v178 offset:8192\n"
    "v_mfma_f32_16x16x32_bf16 v[0:3], a[36:39], a[68:71], v[0:3]\n"
    "ds_read_b128 a[100:103], v178 offset:8448\n"
    "v_mfma_f32_16x16x32_bf16 v[4:7], a[40:43], a[64:67], v[4:7]\n"
    "ds_read_b128 a[104:107], v178 offset:10240\n"
    "v_mfma_f32_16x16x32_bf16 v[4:7], a[44:47], a[68:71], v[4:7]\n"
    "ds_read_b128 a[108:111], v178 offset:10496\n"
    "v_mfma_f32_16x16x32_bf16 v[8:11], a[48:51], a[64:67], v[8:11]\n"
    "buffer_load_dwordx4 v169, s[36:39], s62 offen offset:1024 lds\n"
    "v_mfma_f32_16x16x32_bf16 v[8:11], a[52:55], a[68:71], v[8:11]\n"
    "v_mfma_f32_16x16x32_bf16 v[12:15], a[56:59], a[64:67], v[12:15]\n"
    "s_waitcnt lgkmcnt(7)\n"
    "v_mfma_f32_16x16x32_bf16 v[12:15], a[60:63], a[68:71], v[12:15]\n"
    "v_mfma_f32_16x16x32_bf16 v[28:31], a[56:59], a[72:75], v[28:31]\n"
    "v_mfma_f32_16x16x32_bf16 v[28:31], a[60:63], a[76:79], v[28:31]\n"
    "buffer_load_dwordx4 v170, s[36:39], s62 offen offset:2048 lds\n"
    "v_mfma_f32_16x16x32_bf16 v[24:27], a[48:51], a[72:75], v[24:27]\n"
    "v_mfma_f32_16x16x32_bf16 v[24:27], a[52:55], a[76:79], v[24:27]\n"
    "v_mfma_f32_16x16x32_bf16 v[20:23], a[40:43], a[72:75], v[20:23]\n"
    "v_mfma_f32_16x16x32_bf16 v[20:23], a[44:47], a[76:79], v[20:23]\n"
    "v_mfma_f32_16x16x32_bf16 v[16:19], a[32:35], a[72:75], v[16:19]\n"
    "buffer_load_dwordx4 v171, s[36:39], s62 offen offset:3072 lds\n"
    "s_add_u32 m0, s69, 126976\n"
    "v_mfma_f32_16x16x32_bf16 v[16:19], a[36:39], a[76:79], v[16:19]\n"
    "v_mfma_f32_16x16x32_bf16 v[32:35], a[32:35], a[80:83], v[32:35]\n"
    "s_waitcnt lgkmcnt(1)\n"
    "v_mfma_f32_16x16x32_bf16 v[32:35], a[36:39], a[84:87], v[32:35]\n"
    "v_mfma_f32_16x16x32_bf16 v[36:39], a[40:43], a[80:83], v[36:39]\n"
    "v_mfma_f32_16x16x32_bf16 v[36:39], a[44:47], a[84:87], v[36:39]\n"
    "buffer_load_dwordx4 v172, s[36:39], s62 offen offset:0 lds\n"
    "v_mfma_f32_16x16x32_bf16 v[40:43], a[48:51], a[80:83], v[40:43]\n"
    "v_mfma_f32_16x16x32_bf16 v[40:43], a[52:55], a[84:87], v[40:43]\n"
    "v_mfma_f32_16x16x32_bf16 v[44:47], a[56:59], a[80:83], v[44:47]\n"
    "v_mfma_f32_16x16x32_bf16 v[44:47], a[60:63], a[84:87], v[44:47]\n"
    "buffer_load_dwordx4 v173, s[36:39], s62 offen offset:1024 lds\n"
    "s_waitcnt lgkmcnt(0)\n"
    "s_waitcnt vmcnt(14)\n"
    "s_barrier\n"
    "v_mfma_f32_16x16x32_bf16 v[48:51], a[32:35], a[88:91], v[48:51]\n"
    "ds_read_b64_tr_b16 a[0:1], v182 offset:0\n"
    "s_add_u32 m0, s64, 32768\n"
    "v_mfma_f32_16x16x32_bf16 v[48:51], a[36:39], a[92:95], v[48:51]\n"
    "ds_read_b64_tr_b16 a[2:3], v182 offset:1024\n"
    "s_add_u32 s74, s64, 0\n"
    "v_mfma_f32_16x16x32_bf16 v[52:55], a[40:43], a[88:91], v[52:55]\n"
    "ds_read_b64_tr_b16 a[4:5], v182 offset:8192\n"
    "buffer_load_dwordx4 v253, s[48:51], s74 offen offset:0 nt lds\n"
    "v_mfma_f32_16x16x32_bf16 v[52:55], a[44:47], a[92:95], v[52:55]\n"
    "ds_read_b64_tr_b16 a[6:7], v182 offset:9216\n"
    "v_mfma_f32_16x16x32_bf16 v[56:59], a[48:51], a[88:91], v[56:59]\n"
    "ds_read_b128 a[64:67], v179 offset:0\n"
    "v_mfma_f32_16x16x32_bf16 v[56:59], a[52:55], a[92:95], v[56:59]\n"
    "ds_read_b128 a[68:71], v179 offset:256\n"
    "v_mfma_f32_16x16x32_bf16 v[60:63], a[56:59], a[88:91], v[60:63]\n"
    "ds_read_b64_tr_b16 a[8:9], v185 offset:0\n"
    "v_mfma_f32_16x16x32_bf16 v[60:63], a[60:63], a[92:95], v[60:63]\n"
    "ds_read_b64_tr_b16 a[10:11], v185 offset:1024\n"
    "v_mfma_f32_16x16x32_bf16 v[76:79], a[56:59], a[96:99], v[76:79]\n"
    "ds_read_b64_tr_b16 a[12:13], v185 offset:8192\n"
    "v_mfma_f32_16x16x32_bf16 v[76:79], a[60:63], a[100:103], v[76:79]\n"
    "ds_read_b64_tr_b16 a[14:15], v185 offset:9216\n"
    "v_mfma_f32_16x16x32_bf16 v[72:75], a[48:51], a[96:99], v[72:75]\n"
    "ds_read_b64_tr_b16 a[16:17], v182 offset:256\n"
    "v_mfma_f32_16x16x32_bf16 v[72:75], a[52:55], a[100:103], v[72:75]\n"
    "ds_read_b64_tr_b16 a[18:19], v182 offset:1280\n"
    "v_mfma_f32_16x16x32_bf16 v[68:71], a[40:43], a[96:99], v[68:71]\n"
    "ds_read_b64_tr_b16 a[20:21], v182 offset:8448\n"
    "v_mfma_f32_16x16x32_bf16 v[68:71], a[44:47], a[100:103], v[68:71]\n"
    "ds_read_b64_tr_b16 a[22:23], v182 offset:9472\n"
    "v_mfma_f32_16x16x32_bf16 v[64:67], a[32:35], a[96:99], v[64:67]\n"
    "ds_read_b64_tr_b16 a[24:25], v185 offset:256\n"
    "buffer_load_dwordx4 v254, s[48:51], s74 offen offset:1024 nt lds\n"
    "v_mfma_f32_16x16x32_bf16 v[64:67], a[36:39], a[100:103], v[64:67]\n"
    "ds_read_b64_tr_b16 a[26:27], v185 offset:1280\n"
    "v_mfma_f32_16x16x32_bf16 v[80:83], a[32:35], a[104:107], v[80:83]\n"
    "ds_read_b64_tr_b16 a[28:29], v185 offset:8448\n"
    "v_mfma_f32_16x16x32_bf16 v[80:83], a[36:39], a[108:111], v[80:83]\n"
    "ds_read_b64_tr_b16 a[30:31], v185 offset:9472\n"
    "v_mfma_f32_16x16x32_bf16 v[84:87], a[40:43], a[104:107], v[84:87]\n"
    "ds_read_b128 a[72:75], v179 offset:2048\n"
    "v_mfma_f32_16x16x32_bf16 v[84:87], a[44:47], a[108:111], v[84:87]\n"
    "ds_read_b128 a[76:79], v179 offset:2304\n"
    "v_mfma_f32_16x16x32_bf16 v[88:91], a[48:51], a[104:107], v[88:91]\n"
    "ds_read_b128 a[80:83], v179 offset:4096\n"
    "v_mfma_f32_16x16x32_bf16 v[88:91], a[52:55], a[108:111], v[88:91]\n"
    "ds_read_b128 a[84:87], v179 offset:4352\n"
    "v_mfma_f32_16x16x32_bf16 v[92:95], a[56:59], a[104:107], v[92:95]\n"
    "ds_read_b128 a[88:91], v179 offset:6144\n"
    "v_mfma_f32_16x16x32_bf16 v[92:95], a[60:63], a[108:111], v[92:95]\n"
    "ds_read_b128 a[92:95], v179 offset:6400\n"
    "s_waitcnt lgkmcnt(9)\n"
    "v_mfma_f32_16x16x32_bf16 v[0:3], a[0:3], a[64:67], v[0:3]\n"
    "ds_read_b128 a[96:99], v179 offset:8192\n"
    "v_mfma_f32_16x16x32_bf16 v[0:3], a[4:7], a[68:71], v[0:3]\n"
    "ds_read_b128 a[100:103], v179 offset:8448\n"
    "v_mfma_f32_16x16x32_bf16 v[4:7], a[8:11], a[64:67], v[4:7]\n"
    "ds_read_b128 a[104:107], v179 offset:10240\n"
    "buffer_load_dwordx4 v253, s[48:51], s74 offen offset:2048 nt lds\n"
    "v_mfma_f32_16x16x32_bf16 v[4:7], a[12:15], a[68:71], v[4:7]\n"
    "ds_read_b128 a[108:111], v179 offset:10496\n"
    "v_mfma_f32_16x16x32_bf16 v[8:11], a[16:19], a[64:67], v[8:11]\n"
    "v_mfma_f32_16x16x32_bf16 v[8:11], a[20:23], a[68:71], v[8:11]\n"
    "s_waitcnt lgkmcnt(8)\n"
    "v_mfma_f32_16x16x32_bf16 v[12:15], a[24:27], a[64:67], v[12:15]\n"
    "v_mfma_f32_16x16x32_bf16 v[12:15], a[28:31], a[68:71], v[12:15]\n"
    "v_mfma_f32_16x16x32_bf16 v[28:31], a[24:27], a[72:75], v[28:31]\n"
    "v_mfma_f32_16x16x32_bf16 v[28:31], a[28:31], a[76:79], v[28:31]\n"
    "v_mfma_f32_16x16x32_bf16 v[24:27], a[16:19], a[72:75], v[24:27]\n"
    "v_mfma_f32_16x16x32_bf16 v[24:27], a[20:23], a[76:79], v[24:27]\n"
    "v_mfma_f32_16x16x32_bf16 v[20:23], a[8:11], a[72:75], v[20:23]\n"
    "v_mfma_f32_16x16x32_bf16 v[20:23], a[12:15], a[76:79], v[20:23]\n"
    "v_mfma_f32_16x16x32_bf16 v[16:19], a[0:3], a[72:75], v[16:19]\n"
    "buffer_load_dwordx4 v254, s[48:51], s74 offen offset:3072 nt lds\n"
    "v_mfma_f32_16x16x32_bf16 v[16:19], a[4:7], a[76:79], v[16:19]\n"
    "s_waitcnt lgkmcnt(2)\n"
    "v_mfma_f32_16x16x32_bf16 v[32:35], a[0:3], a[80:83], v[32:35]\n"
    "v_mfma_f32_16x16x32_bf16 v[32:35], a[4:7], a[84:87], v[32:35]\n"
    "v_mfma_f32_16x16x32_bf16 v[36:39], a[8:11], a[80:83], v[36:39]\n"
    "v_mfma_f32_16x16x32_bf16 v[36:39], a[12:15], a[84:87], v[36:39]\n"
    "v_mfma_f32_16x16x32_bf16 v[40:43], a[16:19], a[80:83], v[40:43]\n"
    "v_mfma_f32_16x16x32_bf16 v[40:43], a[20:23], a[84:87], v[40:43]\n"
    "v_mfma_f32_16x16x32_bf16 v[44:47], a[24:27], a[80:83], v[44:47]\n"
    "v_mfma_f32_16x16x32_bf16 v[44:47], a[28:31], a[84:87], v[44:47]\n"
    "s_waitcnt lgkmcnt(0)\n"
    "s_waitcnt vmcnt(4)\n"
    "s_barrier\n"
    "s_lshr_b32 s75, %[rk], 2\n"
    "s_or_b32 s75, s75, %[w]\n"
    "s_cbranch_scc1 NOCLK_%=\n"
    "s_memtime s[72:73]\n"
    "s_memrealtime s[92:93]\n"
    "s_waitcnt lgkmcnt(0)\n"
    "s_sub_u32 s72, s72, s82\n"
    "s_sub_u32 s92, s92, s83\n"
    "s_add_u32 s72, s72, 4\n"
    "s_lshr_b32 s72, s72, 3\n"
    "s_lshl_b32 s72, s72, 16\n"
    "s_or_b32 s72, s72, s92\n"
    "v_mov_b32 v164, s78\n"
    "v_mov_b32 v165, s72\n"
    "v_mov_b32 v166, s60\n"
    "s_lshl_b32 s75, %[rk], 3\n"
    "s_add_u32 s75, s75, %[xcd]\n"
    "s_lshl_b32 s75, s75, 4\n"
    "s_add_u32 s75, s75, 40894464\n"
    "s_mov_b64 exec, 1\n"
    "buffer_store_dwordx3 v[164:166], off, s[52:55], s75 sc1\n"
    "s_mov_b64 exec, -1\n"
    "s_nop 3\n"
    "NOCLK_%=:\n"
    "s_mov_b32 s82, 0x3e0293ee\n"
    "s_mov_b32 s83, 0x3e0293ee\n"
    "v_mfma_f32_16x16x32_bf16 v[48:51], a[0:3], a[88:91], v[48:51]\n"
    "ds_read_b64_tr_b16 a[32:33], v183 offset:0\n"
    "s_add_u32 m0, s64, 36864\n"
    "v_mfma_f32_16x16x32_bf16 v[48:51], a[4:7], a[92:95], v[48:51]\n"
    "ds_read_b64_tr_b16 a[34:35], v183 offset:1024\n"
    "s_add_u32 s74, s64, 4096\n"
    "v_mfma_f32_16x16x32_bf16 v[52:55], a[8:11], a[88:91], v[52:55]\n"
    "ds_read_b64_tr_b16 a[36:37], v183 offset:8192\n"
    "buffer_load_dwordx4 v253, s[48:51], s74 offen offset:0 nt lds\n"
    "v_mfma_f32_16x16x32_bf16 v[52:55], a[12:15], a[92:95], v[52:55]\n"
    "ds_read_b64_tr_b16 a[38:39], v183 offset:9216\n"
    "v_mfma_f32_16x16x32_bf16 v[56:59], a[16:19], a[88:91], v[56:59]\n"
    "ds_read_b128 a[64:67], v180 offset:0\n"
    "v_mfma_f32_16x16x32_bf16 v[56:59], a[20:23], a[92:95], v[56:59]\n"
    "ds_read_b128 a[68:71], v180 offset:256\n"
    "v_mfma_f32_16x16x32_bf16 v[60:63], a[24:27], a[88:91], v[60:63]\n"
    "ds_read_b64_tr_b16 a[40:41], v186 offset:0\n"
    "v_mfma_f32_16x16x32_bf16 v[60:63], a[28:31], a[92:95], v[60:63]\n"
    "ds_read_b64_tr_b16 a[42:43], v186 offset:1024\n"
    "v_mfma_f32_16x16x32_bf16 v[76:79], a[24:27], a[96:99], v[76:79]\n"
    "ds_read_b64_tr_b16 a[44:45], v186 offset:8192\n"
    "buffer_load_dwordx4 v254, s[48:51], s74 offen offset:1024 nt lds\n"
    "v_mfma_f32_16x16x32_bf16 v[76:79], a[28:31], a[100:103], v[76:79]\n"
    "ds_read_b64_tr_b16 a[46:47], v186 offset:9216\n"
    "v_mfma_f32_16x16x32_bf16 v[72:75], a[16:19], a[96:99], v[72:75]\n"
    "ds_read_b64_tr_b16 a[48:49], v183 offset:256\n"
    "v_mfma_f32_16x16x32_bf16 v[72:75], a[20:23], a[100:103], v[72:75]\n"
    "ds_read_b64_tr_b16 a[50:51], v183 offset:1280\n"
    "v_mfma_f32_16x16x32_bf16 v[68:71], a[8:11], a[96:99], v[68:71]\n"
    "ds_read_b64_tr_b16 a[52:53], v183 offset:8448\n"
    "v_mfma_f32_16x16x32_bf16 v[68:71], a[12:15], a[100:103], v[68:71]\n"
    "ds_read_b64_tr_b16 a[54:55], v183 offset:9472\n"
    "v_mfma_f32_16x16x32_bf16 v[64:67], a[0:3], a[96:99], v[64:67]\n"
    "ds_read_b64_tr_b16 a[56:57], v186 offset:256\n"
    "buffer_load_dwordx4 v253, s[48:51], s74 offen offset:2048 nt lds\n"
    "v_mfma_f32_16x16x32_bf16 v[64:67], a[4:7], a[100:103], v[64:67]\n"
    "ds_read_b64_tr_b16 a[58:59], v186 offset:1280\n"
    "v_mfma_f32_16x16x32_bf16 v[80:83], a[0:3], a[104:107], v[80:83]\n"
    "ds_read_b64_tr_b16 a[60:61], v186 offset:8448\n"
    "v_mfma_f32_16x16x32_bf16 v[80:83], a[4:7], a[108:111], v[80:83]\n"
    "ds_read_b64_tr_b16 a[62:63], v186 offset:9472\n"
    "v_mfma_f32_16x16x32_bf16 v[84:87], a[8:11], a[104:107], v[84:87]\n"
    "ds_read_b128 a[72:75], v180 offset:2048\n"
    "v_mfma_f32_16x16x32_bf16 v[84:87], a[12:15], a[108:111], v[84:87]\n"
    "ds_read_b128 a[76:79], v180 offset:2304\n"
    "v_mfma_f32_16x16x32_bf16 v[88:91], a[16:19], a[104:107], v[88:91]\n"
    "ds_read_b128 a[80:83], v180 offset:4096\n"
    "buffer_load_dwordx4 v254, s[48:51], s74 offen offset:3072 nt lds\n"
    "v_mfma_f32_16x16x32_bf16 v[88:91], a[20:23], a[108:111], v[88:91]\n"
    "ds_read_b128 a[84:87], v180 offset:4352\n"
    "v_mfma_f32_16x16x32_bf16 v[92:95], a[24:27], a[104:107], v[92:95]\n"
    "v_mfma_f32_16x16x32_bf16 v[92:95], a[28:31], a[108:111], v[92:95]\n"
    "s_mov_b32 s37, 40894464\n"
    "s_waitcnt lgkmcnt(7)\n"
    "v_mfma_f32_16x16x32_bf16 v[0:3], a[32:35], a[64:67], v[0:3]\n"
    "ds_read_b128 a[88:91], v180 offset:6144\n"
    "v_mfma_f32_16x16x32_bf16 v[0:3], a[36:39], a[68:71], v[0:3]\n"
    "ds_read_b128 a[92:95], v180 offset:6400\n"
    "v_mfma_f32_16x16x32_bf16 v[4:7], a[40:43], a[64:67], v[4:7]\n"
    "ds_read_b128 a[96:99], v180 offset:8192\n"
    "v_mfma_f32_16x16x32_bf16 v[4:7], a[44:47], a[68:71], v[4:7]\n"
    "ds_read_b128 a[100:103], v180 offset:8448\n"
    "s_nop 2\n"
    "v_mul_f32_e64 v0, s70, v0\n"
    "v_mfma_f32_16x16x32_bf16 v[8:11], a[48:51], a[64:67], v[8:11]\n"
    "ds_read_b128 a[104:107], v180 offset:10240\n"
    "v_mul_f32_e64 v1, s70, v1\n"
    "v_mfma_f32_16x16x32_bf16 v[8:11], a[52:55], a[68:71], v[8:11]\n"
    "ds_read_b128 a[108:111], v180 offset:10496\n"
    "v_mul_f32_e64 v2, s70, v2\n"
    "s_waitcnt lgkmcnt(6)\n"
    "v_mfma_f32_16x16x32_bf16 v[12:15], a[56:59], a[64:67], v[12:15]\n"
    "ds_read_b128 a[192:195], v250 offset:0\n"
    "v_mul_f32_e64 v3, s70, v3\n"
    "v_mfma_f32_16x16x32_bf16 v[12:15], a[60:63], a[68:71], v[12:15]\n"
    "ds_read_b128 a[196:199], v250 offset:256\n"
    "v_cvt_pk_bf16_f32 v0, v0, v1\n"
    "v_mfma_f32_16x16x32_bf16 v[28:31], a[56:59], a[72:75], v[28:31]\n"
    "ds_read_b128 a[200:203], v250 offset:512\n"
    "v_cvt_pk_bf16_f32 v1, v2, v3\n"
    "v_mfma_f32_16x16x32_bf16 v[28:31], a[60:63], a[76:79], v[28:31]\n"
    "ds_read_b128 a[204:207], v250 offset:768\n"
    "v_mul_f32_e64 v4, s70, v4\n"
    "v_mfma_f32_16x16x32_bf16 v[24:27], a[48:51], a[72:75], v[24:27]\n"
    "ds_read_b128 a[208:211], v250 offset:4096\n"
    "v_mul_f32_e64 v5, s70, v5\n"
    "v_mfma_f32_16x16x32_bf16 v[24:27], a[52:55], a[76:79], v[24:27]\n"
    "ds_read_b128 a[212:215], v250 offset:4352\n"
    "v_mul_f32_e64 v6, s70, v6\n"
    "v_mfma_f32_16x16x32_bf16 v[20:23], a[40:43], a[72:75], v[20:23]\n"
    "ds_read_b128 a[216:219], v250 offset:4608\n"
    "v_mul_f32_e64 v7, s70, v7\n"
    "v_mfma_f32_16x16x32_bf16 v[20:23], a[44:47], a[76:79], v[20:23]\n"
    "ds_read_b128 a[220:223], v250 offset:4864\n"
    "v_cvt_pk_bf16_f32 v4, v4, v5\n"
    "v_mfma_f32_16x16x32_bf16 v[16:19], a[32:35], a[72:75], v[16:19]\n"
    "ds_read_b128 a[224:227], v250 offset:8192\n"
    "v_cvt_pk_bf16_f32 v5, v6, v7\n"
    "v_mfma_f32_16x16x32_bf16 v[16:19], a[36:39], a[76:79], v[16:19]\n"
    "ds_read_b128 a[228:231], v250 offset:8448\n"
    "v_mul_f32_e64 v8, s70, v8\n"
    "v_mfma_f32_16x16x32_bf16 v[32:35], a[32:35], a[80:83], v[32:35]\n"
    "ds_read_b128 a[232:235], v250 offset:8704\n"
    "v_mul_f32_e64 v9, s70, v9\n"
    "v_mfma_f32_16x16x32_bf16 v[32:35], a[36:39], a[84:87], v[32:35]\n"
    "ds_read_b128 a[236:239], v250 offset:8960\n"
    "v_mul_f32_e64 v10, s70, v10\n"
    "v_mfma_f32_16x16x32_bf16 v[36:39], a[40:43], a[80:83], v[36:39]\n"
    "ds_read_b128 a[240:243], v250 offset:12288\n"
    "v_mul_f32_e64 v11, s70, v11\n"
    "v_mfma_f32_16x16x32_bf16 v[36:39], a[44:47], a[84:87], v[36:39]\n"
    "ds_read_b128 a[244:247], v250 offset:12544\n"
    "v_cvt_pk_bf16_f32 v8, v8, v9\n"
    "v_mfma_f32_16x16x32_bf16 v[40:43], a[48:51], a[80:83], v[40:43]\n"
    "ds_read_b128 a[248:251], v250 offset:12800\n"
    "v_cvt_pk_bf16_f32 v9, v10, v11\n"
    "v_mfma_f32_16x16x32_bf16 v[40:43], a[52:55], a[84:87], v[40:43]\n"
    "ds_read_b128 a[252:255], v250 offset:13056\n"
    "v_mul_f32_e64 v12, s70, v12\n"
    "v_mfma_f32_16x16x32_bf16 v[44:47], a[56:59], a[80:83], v[44:47]\n"
    "ds_read_b128 v[128:131], v190 offset:0\n"
    "v_mul_f32_e64 v13, s70, v13\n"
    "v_mfma_f32_16x16x32_bf16 v[44:47], a[60:63], a[84:87], v[44:47]\n"
    "ds_read_b128 v[132:135], v190 offset:256\n"
    "v_mul_f32_e64 v14, s70, v14\n"
    "s_waitcnt lgkmcnt(6)\n"
    "v_mfma_f32_16x16x32_bf16 v[48:51], a[32:35], a[88:91], v[48:51]\n"
    "ds_read_b128 v[136:139], v191 offset:0\n"
    "v_mul_f32_e64 v15, s70, v15\n"
    "v_mfma_f32_16x16x32_bf16 v[48:51], a[36:39], a[92:95], v[48:51]\n"
    "ds_read_b128 v[140:143], v191 offset:256\n"
    "v_cvt_pk_bf16_f32 v12, v12, v13\n"
    "v_mfma_f32_16x16x32_bf16 v[52:55], a[40:43], a[88:91], v[52:55]\n"
    "v_cvt_pk_bf16_f32 v13, v14, v15\n"
    "v_mul_f32_e64 v16, s70, v16\n"
    "v_mfma_f32_16x16x32_bf16 v[52:55], a[44:47], a[92:95], v[52:55]\n"
    "v_mul_f32_e64 v17, s70, v17\n"
    "v_mul_f32_e64 v18, s70, v18\n"
    "v_mfma_f32_16x16x32_bf16 v[56:59], a[48:51], a[88:91], v[56:59]\n"
    "v_mul_f32_e64 v19, s70, v19\n"
    "v_cvt_pk_bf16_f32 v16, v16, v17\n"
    "v_mfma_f32_16x16x32_bf16 v[56:59], a[52:55], a[92:95], v[56:59]\n"
    "v_cvt_pk_bf16_f32 v17, v18, v19\n"
    "v_mul_f32_e64 v20, s70, v20\n"
    "v_mfma_f32_16x16x32_bf16 v[60:63], a[56:59], a[88:91], v[60:63]\n"
    "v_mul_f32_e64 v21, s70, v21\n"
    "s_waitcnt lgkmcnt(0)\n"
    "s_barrier\n"
    "v_mfma_f32_16x16x32_bf16 v[60:63], a[60:63], a[92:95], v[60:63]\n"
    "ds_write_b64 v204, v[0:1] offset:0\n"
    "v_mfma_f32_16x16x32_bf16 v[76:79], a[56:59], a[96:99], v[76:79]\n"
    "v_mul_f32_e64 v22, s70, v22\n"
    "v_mul_f32_e64 v23, s70, v23\n"
    "v_mfma_f32_16x16x32_bf16 v[76:79], a[60:63], a[100:103], v[76:79]\n"
    "ds_write_b64 v204, v[4:5] offset:512\n"
    "v_mfma_f32_16x16x32_bf16 v[72:75], a[48:51], a[96:99], v[72:75]\n"
    "v_cvt_pk_bf16_f32 v20, v20, v21\n"
    "v_cvt_pk_bf16_f32 v21, v22, v23\n"
    "v_mfma_f32_16x16x32_bf16 v[72:75], a[52:55], a[100:103], v[72:75]\n"
    "ds_write_b64 v204, v[8:9] offset:1024\n"
    "v_mfma_f32_16x16x32_bf16 v[68:71], a[40:43], a[96:99], v[68:71]\n"
    "v_mul_f32_e64 v24, s70, v24\n"
    "v_mul_f32_e64 v25, s70, v25\n"
    "s_add_u32 m0, s64, 65536\n"
    "s_add_u32 s74, s64, 32768\n"
    "buffer_load_dwordx4 v253, s[44:47], s74 offen offset:0 nt lds\n"
    "v_mfma_f32_16x16x32_bf16 v[68:71], a[44:47], a[100:103], v[68:71]\n"
    "ds_write_b64 v204, v[12:13] offset:1536\n"
    "v_mfma_f32_16x16x32_bf16 v[64:67], a[32:35], a[96:99], v[64:67]\n"
    "v_mul_f32_e64 v26, s70, v26\n"
    "v_mul_f32_e64 v27, s70, v27\n"
    "v_mfma_f32_16x16x32_bf16 v[64:67], a[36:39], a[100:103], v[64:67]\n"
    "ds_write_b64 v204, v[16:17] offset:4096\n"
    "v_mfma_f32_16x16x32_bf16 v[80:83], a[32:35], a[104:107], v[80:83]\n"
    "v_cvt_pk_bf16_f32 v24, v24, v25\n"
    "v_cvt_pk_bf16_f32 v25, v26, v27\n"
    "v_mfma_f32_16x16x32_bf16 v[80:83], a[36:39], a[108:111], v[80:83]\n"
    "ds_write_b64 v204, v[20:21] offset:4608\n"
    "v_mfma_f32_16x16x32_bf16 v[84:87], a[40:43], a[104:107], v[84:87]\n"
    "v_mul_f32_e64 v28, s70, v28\n"
    "v_mul_f32_e64 v29, s70, v29\n"
    "v_mfma_f32_16x16x32_bf16 v[84:87], a[44:47], a[108:111], v[84:87]\n"
    "ds_write_b64 v204, v[24:25] offset:5120\n"
    "v_mfma_f32_16x16x32_bf16 v[88:91], a[48:51], a[104:107], v[88:91]\n"
    "v_mul_f32_e64 v30, s70, v30\n"
    "v_mul_f32_e64 v31, s70, v31\n"
    "v_mfma_f32_16x16x32_bf16 v[88:91], a[52:55], a[108:111], v[88:91]\n"
    "v_cvt_pk_bf16_f32 v28, v28, v29\n"
    "v_cvt_pk_bf16_f32 v29, v30, v31\n"
    "v_mfma_f32_16x16x32_bf16 v[92:95], a[56:59], a[104:107], v[92:95]\n"
    "v_mul_f32_e64 v32, s70, v32\n"
    "ds_write_b64 v204, v[28:29] offset:5632\n"
    "v_mfma_f32_16x16x32_bf16 v[92:95], a[60:63], a[108:111], v[92:95]\n"
    "v_mul_f32_e64 v33, s70, v33\n"
    "v_mul_f32_e64 v34, s70, v34\n"
    "v_mfma_f32_32x32x16_bf16 a[80:95], v[200:203], v[200:203], 0\n"
    "buffer_load_dwordx4 v254, s[44:47], s74 offen offset:1024 nt lds\n"
    "v_mul_f32_e64 v35, s70, v35\n"
    "v_cvt_pk_bf16_f32 v32, v32, v33\n"
    "v_cvt_pk_bf16_f32 v33, v34, v35\n"
    "v_mul_f32_e64 v36, s70, v36\n"
    "v_mul_f32_e64 v37, s70, v37\n"
    "v_mul_f32_e64 v38, s70, v38\n"
    "ds_write_b64 v204, v[32:33] offset:8192\n"
    "v_mul_f32_e64 v39, s70, v39\n"
    "v_cvt_pk_bf16_f32 v36, v36, v37\n"
    "v_cvt_pk_bf16_f32 v37, v38, v39\n"
    "v_mul_f32_e64 v40, s70, v40\n"
    "v_mul_f32_e64 v41, s70, v41\n"
    "v_mul_f32_e64 v42, s70, v42\n"
    "ds_write_b64 v204, v[36:37] offset:8704\n"
    "v_mul_f32_e64 v43, s70, v43\n"
    "v_cvt_pk_bf16_f32 v40, v40, v41\n"
    "v_cvt_pk_bf16_f32 v41, v42, v43\n"
    "v_mul_f32_e64 v44, s70, v44\n"
    "v_mul_f32_e64 v45, s70, v45\n"
    "v_mul_f32_e64 v46, s70, v46\n"
    "ds_write_b64 v204, v[40:41] offset:9216\n"
    "v_mul_f32_e64 v47, s70, v47\n"
    "v_cvt_pk_bf16_f32 v44, v44, v45\n"
    "v_cvt_pk_bf16_f32 v45, v46, v47\n"
    "ds_write_b64 v204, v[44:45] offset:9728\n"
    "v_pk_mul_f32 v[48:49], v[48:49], s[82:83]\n"
    "v_pk_mul_f32 v[50:51], v[50:51], s[82:83]\n"
    "v_cvt_pk_bf16_f32 v48, v48, v49\n"
    "v_cvt_pk_bf16_f32 v49, v50, v51\n"
    "buffer_load_dwordx4 v253, s[44:47], s74 offen offset:2048 nt lds\n"
    "v_pk_mul_f32 v[52:53], v[52:53], s[82:83]\n"
    "v_pk_mul_f32 v[54:55], v[54:55], s[82:83]\n"
    "v_cvt_pk_bf16_f32 v52, v52, v53\n"
    "v_cvt_pk_bf16_f32 v53, v54, v55\n"
    "v_pk_mul_f32 v[56:57], v[56:57], s[82:83]\n"
    "v_pk_mul_f32 v[58:59], v[58:59], s[82:83]\n"
    "v_cvt_pk_bf16_f32 v56, v56, v57\n"
    "v_cvt_pk_bf16_f32 v57, v58, v59\n"
    "v_pk_mul_f32 v[60:61], v[60:61], s[82:83]\n"
    "v_pk_mul_f32 v[62:63], v[62:63], s[82:83]\n"
    "v_cvt_pk_bf16_f32 v60, v60, v61\n"
    "v_cvt_pk_bf16_f32 v61, v62, v63\n"
    "v_pk_mul_f32 v[64:65], v[64:65], s[82:83]\n"
    "v_pk_mul_f32 v[66:67], v[66:67], s[82:83]\n"
    "v_cvt_pk_bf16_f32 v64, v64, v65\n"
    "v_cvt_pk_bf16_f32 v65, v66, v67\n"
    "v_pk_mul_f32 v[68:69], v[68:69], s[82:83]\n"
    "v_pk_mul_f32 v[70:71], v[70:71], s[82:83]\n"
    "v_cvt_pk_bf16_f32 v68, v68, v69\n"
    "v_cvt_pk_bf16_f32 v69, v70, v71\n"
    "v_pk_mul_f32 v[72:73], v[72:73], s[82:83]\n"
    "v_pk_mul_f32 v[74:75], v[74:75], s[82:83]\n"
    "v_cvt_pk_bf16_f32 v72, v72, v73\n"
    "v_cvt_pk_bf16_f32 v73, v74, v75\n"
    "v_pk_mul_f32 v[76:77], v[76:77], s[82:83]\n"
    "v_pk_mul_f32 v[78:79], v[78:79], s[82:83]\n"
    "v_cvt_pk_bf16_f32 v76, v76, v77\n"
    "v_cvt_pk_bf16_f32 v77, v78, v79\n"
    "v_pk_mul_f32 v[80:81], v[80:81], s[82:83]\n"
    "buffer_load_dwordx4 v254, s[44:47], s74 offen offset:3072 nt lds\n"
    "v_pk_mul_f32 v[82:83], v[82:83], s[82:83]\n"
    "v_cvt_pk_bf16_f32 v80, v80, v81\n"
    "v_cvt_pk_bf16_f32 v81, v82, v83\n"
    "v_pk_mul_f32 v[84:85], v[84:85], s[82:83]\n"
    "v_pk_mul_f32 v[86:87], v[86:87], s[82:83]\n"
    "v_cvt_pk_bf16_f32 v84, v84, v85\n"
    "v_cvt_pk_bf16_f32 v85, v86, v87\n"
    "v_pk_mul_f32 v[88:89], v[88:89], s[82:83]\n"
    "v_pk_mul_f32 v[90:91], v[90:91], s[82:83]\n"
    "v_cvt_pk_bf16_f32 v88, v88, v89\n"
    "v_cvt_pk_bf16_f32 v89, v90, v91\n"
    "v_pk_mul_f32 v[92:93], v[92:93], s[82:83]\n"
    "v_pk_mul_f32 v[94:95], v[94:95], s[82:83]\n"
    "v_cvt_pk_bf16_f32 v92, v92, v93\n"
    "v_cvt_pk_bf16_f32 v93, v94, v95\n"
    "s_waitcnt vmcnt(63) lgkmcnt(0)\n"
    "s_barrier\n"
    "ds_read_b128 a[112:115], v205 offset:4096\n"
    "ds_read_b128 a[116:119], v205 offset:5120\n"
    "ds_read_b128 a[120:123], v206 offset:4096\n"
    "ds_read_b128 a[124:127], v206 offset:5120\n"
    "ds_read_b128 a[128:131], v205 offset:8192\n"
    "ds_read_b128 a[132:135], v205 offset:9216\n"
    "ds_read_b128 a[136:139], v206 offset:8192\n"
    "s_waitcnt lgkmcnt(6)\n"
    "v_mfma_f32_16x16x32_bf16 v[100:103], v[128:131], a[112:115], 0\n"
    "ds_read_b128 a[140:143], v206 offset:9216\n"
    "s_waitcnt lgkmcnt(6)\n"
    "v_mfma_f32_16x16x32_bf16 v[100:103], v[132:135], a[116:119], v[100:103]\n"
    "ds_read_b128 a[96:99], v205 offset:0\n"
    "s_add_u32 m0, s64, 69632\n"
    "s_add_u32 s74, s64, 36864\n"
    "buffer_load_dwordx4 v253, s[44:47], s74 offen offset:0 nt lds\n"
    "s_waitcnt lgkmcnt(6)\n"
    "v_mfma_f32_16x16x32_bf16 v[100:103], v[136:139], a[120:123], v[100:103]\n"
    "ds_read_b128 a[100:103], v205 offset:1024\n"
    "v_add_u32 v250, 65536, v250\n"
    "s_waitcnt lgkmcnt(6)\n"
    "v_mfma_f32_16x16x32_bf16 v[100:103], v[140:143], a[124:127], v[100:103]\n"
    "ds_read_b128 a[104:107], v206 offset:0\n"
    "s_waitcnt lgkmcnt(6)\n"
    "v_mfma_f32_16x16x32_bf16 v[104:107], v[128:131], a[128:131], 0\n"
    "ds_read_b128 a[108:111], v206 offset:1024\n"
    "s_waitcnt lgkmcnt(5)\n"
    "v_mfma_f32_16x16x32_bf16 v[104:107], v[132:135], a[132:135], v[104:107]\n"
    "ds_write_b64 v204, v[48:49] offset:12288\n"
    "v_mfma_f32_16x16x32_bf16 v[104:107], v[136:139], a[136:139], v[104:107]\n"
    "v_max3_f32 v212, v100, v101, v102\n"
    "s_waitcnt lgkmcnt(5)\n"
    "v_mfma_f32_16x16x32_bf16 v[104:107], v[140:143], a[140:143], v[104:107]\n"
    "ds_write_b64 v204, v[52:53] offset:12800\n"
    "s_waitcnt lgkmcnt(5)\n"
    "v_mfma_f32_16x16x32_bf16 v[96:99], v[128:131], a[96:99], 0\n"
    "v_max_f32 v212, v212, v103\n"
    "s_waitcnt lgkmcnt(4)\n"
    "v_mfma_f32_16x16x32_bf16 v[96:99], v[132:135], a[100:103], v[96:99]\n"
    "ds_write_b64 v204, v[56:57] offset:13312\n"
    "s_waitcnt lgkmcnt(4)\n"
    "v_mfma_f32_16x16x32_bf16 v[96:99], v[136:139], a[104:107], v[96:99]\n"
    "v_mov_b32 v213, v212\n"
    "v_max3_f32 v216, v104, v105, v106\n"
    "s_waitcnt lgkmcnt(3)\n"
    "v_mfma_f32_16x16x32_bf16 v[96:99], v[140:143], a[108:111], v[96:99]\n"
    "ds_write_b64 v204, v[60:61] offset:13824\n"
    "v_permlane32_swap_b32 v212, v213\n"
    "buffer_load_dwordx4 v254, s[44:47], s74 offen offset:1024 nt lds\n"
    "v_max_f32 v212, v212, v213\n"
    "v_max_f32 v216, v216, v107\n"
    "v_mov_b32 v213, v212\n"
    "v_mov_b32 v217, v216\n"
    "ds_write_b64 v204, v[64:65] offset:16384\n"
    "v_max3_f32 v208, v96, v97, v98\n"
    "v_permlane16_swap_b32 v212, v213\n"
    "v_max_f32 v208, v208, v99\n"
    "v_max_f32 v215, v212, v213\n"
    "v_mov_b32 v209, v208\n"
    "ds_write_b64 v204, v[68:69] offset:16896\n"
    "s_nop 0\n"
    "v_permlane32_swap_b32 v208, v209\n"
    "v_max_f32 v208, v208, v209\n"
    "v_sub_f32 v212, 0, v215\n"
    "v_mov_b32 v209, v208\n"
    "v_sub_f32 v213, 0, v215\n"
    "ds_write_b64 v204, v[72:73] offset:17408\n"
    "v_permlane16_swap_b32 v208, v209\n"
    "v_max_f32 v211, v208, v209\n"
    "v_sub_f32 v214, 0, v215\n"
    "v_sub_f32 v208, 0, v211\n"
    "v_sub_f32 v209, 0, v211\n"
    "buffer_load_dwordx4 v253, s[44:47], s74 offen offset:2048 nt lds\n"
    "ds_write_b64 v204, v[76:77] offset:17920\n"
    "v_sub_f32 v210, 0, v211\n"
    "v_sub_f32 v211, 0, v211\n"
    "v_sub_f32 v215, 0, v215\n"
    "s_nop 0\n"
    "v_mfma_f32_16x16x32_bf16 v[160:163], a[192:195], a[96:99], v[208:211]\n"
    "ds_write_b64 v204, v[80:81] offset:20480\n"
    "v_mfma_f32_16x16x32_bf16 v[160:163], a[196:199], a[100:103], v[160:163]\n"
    "v_permlane32_swap_b32 v216, v217\n"
    "v_mfma_f32_16x16x32_bf16 v[160:163], a[200:203], a[104:107], v[160:163]\n"
    "ds_write_b64 v204, v[84:85] offset:20992\n"
    "v_mfma_f32_16x16x32_bf16 v[160:163], a[204:207], a[108:111], v[160:163]\n"
    "v_max_f32 v216, v216, v217\n"
    "v_mfma_f32_16x16x32_bf16 v[164:167], a[220:223], a[108:111], v[208:211]\n"
    "ds_write_b64 v204, v[88:89] offset:21504\n"
    "v_mfma_f32_16x16x32_bf16 v[164:167], a[216:219], a[104:107], v[164:167]\n"
    "v_mov_b32 v217, v216\n"
    "s_nop 2\n"
    "v_exp_f32 v160, v160\n"
    "v_mfma_f32_16x16x32_bf16 v[164:167], a[212:215], a[100:103], v[164:167]\n"
    "ds_write_b64 v204, v[92:93] offset:22016\n"
    "v_mfma_f32_32x32x16_bf16 v[80:95], v[200:203], v[200:203], 0\n"
    "v_permlane16_swap_b32 v216, v217\n"
    "buffer_load_dwordx4 v254, s[44:47], s74 offen offset:3072 nt lds\n"
    "v_max_f32 v219, v216, v217\n"
    "v_exp_f32 v161, v161\n"
    "v_sub_f32 v216, 0, v219\n"
    "v_mfma_f32_16x16x32_bf16 v[164:167], a[208:211], a[96:99], v[164:167]\n"
    "v_sub_f32 v217, 0, v219\n"
    "v_sub_f32 v218, 0, v219\n"
    "v_mfma_f32_16x16x32_bf16 v[168:171], a[224:227], a[96:99], v[208:211]\n"
    "v_sub_f32 v219, 0, v219\n"
    "v_cvt_pk_bf16_f32 v192, v160, v161\n"
    "v_mfma_f32_16x16x32_bf16 v[168:171], a[228:231], a[100:103], v[168:171]\n"
    "v_exp_f32 v162, v162\n"
    "v_mfma_f32_16x16x32_bf16 v[168:171], a[232:235], a[104:107], v[168:171]\n"
    "s_waitcnt vmcnt(63) lgkmcnt(0)\n"
    "s_barrier\n"
    "v_mfma_f32_16x16x32_bf16 v[168:171], a[236:239], a[108:111], v[168:171]\n"
    "ds_read_b128 a[144:147], v205 offset:12288\n"
    "v_exp_f32 v163, v163\n"
    "v_mfma_f32_16x16x32_bf16 v[172:175], a[252:255], a[108:111], v[208:211]\n"
    "ds_read_b128 a[148:151], v205 offset:13312\n"
    "v_cvt_pk_bf16_f32 v193, v162, v163\n"
    "v_mfma_f32_16x16x32_bf16 v[172:175], a[248:251], a[104:107], v[172:175]\n"
    "ds_read_b128 a[152:155], v206 offset:12288\n"
    "v_exp_f32 v164, v164\n"
    "v_mfma_f32_16x16x32_bf16 v[172:175], a[244:247], a[100:103], v[172:175]\n"
    "ds_read_b128 a[156:159], v206 offset:13312\n"
    "v_exp_f32 v165, v165\n"
    "v_mfma_f32_16x16x32_bf16 v[172:175], a[240:243], a[96:99], v[172:175]\n"
    "ds_read_b128 a[160:163], v205 offset:16384\n"
    "v_cvt_pk_bf16_f32 v194, v164, v165\n"
    "v_mfma_f32_16x16x32_bf16 v[176:179], a[192:195], a[112:115], v[212:215]\n"
    "ds_read_b128 a[164:167], v205 offset:17408\n"
    "v_exp_f32 v166, v166\n"
    "s_waitcnt lgkmcnt(5)\n"
    "v_mfma_f32_16x16x32_bf16 v[108:111], v[128:131], a[144:147], 0\n"
    "ds_read_b128 a[168:171], v206 offset:16384\n"
    "v_exp_f32 v167, v167\n"
    "s_waitcnt lgkmcnt(5)\n"
    "v_mfma_f32_16x16x32_bf16 v[108:111], v[132:135], a[148:151], v[108:111]\n"
    "ds_read_b128 a[172:175], v206 offset:17408\n"
    "v_cvt_pk_bf16_f32 v195, v166, v167\n"
    "s_waitcnt lgkmcnt(5)\n"
    "v_mfma_f32_16x16x32_bf16 v[108:111], v[136:139], a[152:155], v[108:111]\n"
    "ds_read_b128 a[176:179], v205 offset:20480\n"
    "v_exp_f32 v168, v168\n"
    "s_waitcnt lgkmcnt(5)\n"
    "v_mfma_f32_16x16x32_bf16 v[108:111], v[140:143], a[156:159], v[108:111]\n"
    "ds_read_b128 a[180:183], v205 offset:21504\n"
    "v_exp_f32 v169, v169\n"
    "s_waitcnt lgkmcnt(5)\n"
    "v_mfma_f32_16x16x32_bf16 v[112:115], v[128:131], a[160:163], 0\n"
    "ds_read_b128 a[184:187], v206 offset:20480\n"
    "v_cvt_pk_bf16_f32 v196, v168, v169\n"
    "s_waitcnt lgkmcnt(5)\n"
    "v_mfma_f32_16x16x32_bf16 v[112:115], v[132:135], a[164:167], v[112:115]\n"
    "ds_read_b128 a[188:191], v206 offset:21504\n"
    "v_max3_f32 v220, v108, v109, v110\n"
    "s_waitcnt lgkmcnt(5)\n"
    "v_mfma_f32_16x16x32_bf16 v[112:115], v[136:139], a[168:171], v[112:115]\n"
    "v_max_f32 v220, v220, v111\n"
    "v_exp_f32 v170, v170\n"
    "s_waitcnt lgkmcnt(4)\n"
    "v_mfma_f32_16x16x32_bf16 v[112:115], v[140:143], a[172:175], v[112:115]\n"
    "v_mov_b32 v221, v220\n"
    "v_exp_f32 v171, v171\n"
    "s_waitcnt lgkmcnt(3)\n"
    "v_mfma_f32_16x16x32_bf16 v[116:119], v[128:131], a[176:179], 0\n"
    "v_permlane32_swap_b32 v220, v221\n"
    "s_waitcnt lgkmcnt(2)\n"
    "v_mfma_f32_16x16x32_bf16 v[116:119], v[132:135], a[180:183], v[116:119]\n"
    "v_max_f32 v220, v220, v221\n"
    "v_max3_f32 v224, v112, v113, v114\n"
    "v_mfma_f32_16x16x32_bf16 v[176:179], a[196:199], a[116:119], v[176:179]\n"
    "s_waitcnt vmcnt(8) lgkmcnt(0)\n"
    "s_barrier\n"
    "s_lshl_b32 s36, %[rk], 3\n"
    "s_add_u32 s36, s36, %[xcd]\n"
    "s_lshl_b32 s36, s36, 2\n"
    "s_add_u32 s36, s36, %[w]\n"
    "s_cmp_lg_u32 s36, 1\n"
    "s_cbranch_scc1 NOAGG_%=\n"
    "v_mbcnt_lo_u32_b32 v144, -1, 0\n"
    "v_and_b32 v144, 31, v144\n"
    "v_lshlrev_b32 v144, 4, v144\n"
    "buffer_load_dwordx3 v[146:148], v144, s[52:55], s37 offen sc1\n"
    "s_waitcnt vmcnt(0)\n"
    "v_cmp_eq_u32 vcc, s78, v146\n"
    "s_nop 1\n"
    "v_cndmask_b32 v147, 0, v147, vcc\n"
    "v_cndmask_b32 v148, 0, v148, vcc\n"
    "s_bcnt1_i32_b32 s38, vcc_lo\n"
    "s_cmp_lt_u32 s38, 26\n"
    "s_cbranch_scc1 NOAGG_%=\n"
    "s_mov_b32 s38, 0\n"
    "s_mov_b32 s39, 0\n"
    "v_readlane_b32 s40, v147, 0\n"
    "v_readlane_b32 s41, v148, 0\n"
    "s_add_u32 s38, s38, s40\n"
    "s_add_u32 s39, s39, s41\n"
    "v_readlane_b32 s40, v147, 1\n"
    "v_readlane_b32 s41, v148, 1\n"
    "s_add_u32 s38, s38, s40\n"
    "s_add_u32 s39, s39, s41\n"
    "v_readlane_b32 s40, v147, 2\n"
    "v_readlane_b32 s41, v148, 2\n"
    "s_add_u32 s38, s38, s40\n"
    "s_add_u32 s39, s39, s41\n"
    "v_readlane_b32 s40, v147, 3\n"
    "v_readlane_b32 s41, v148, 3\n"
    "s_add_u32 s38, s38, s40\n"
    "s_add_u32 s39, s39, s41\n"
    "v_readlane_b32 s40, v147, 4\n"
    "v_readlane_b32 s41, v148, 4\n"
    "s_add_u32 s38, s38, s40\n"
    "s_add_u32 s39, s39, s41\n"
    "v_readlane_b32 s40, v147, 5\n"
    "v_readlane_b32 s41, v148, 5\n"
    "s_add_u32 s38, s38, s40\n"
    "s_add_u32 s39, s39, s41\n"
    "v_readlane_b32 s40, v147, 6\n"
    "v_readlane_b32 s41, v148, 6\n"
    "s_add_u32 s38, s38, s40\n"
    "s_add_u32 s39, s39, s41\n"
    "v_readlane_b32 s40, v147, 7\n"
    "v_readlane_b32 s41, v148, 7\n"
    "s_add_u32 s38, s38, s40\n"
    "s_add_u32 s39, s39, s41\n"
    "v_readlane_b32 s40, v147, 8\n"
    "v_readlane_b32 s41, v148, 8\n"
    "s_add_u32 s38, s38, s40\n"
    "s_add_u32 s39, s39, s41\n"
    "v_readlane_b32 s40, v147, 9\n"
    "v_readlane_b32 s41, v148, 9\n"
    "s_add_u32 s38, s38, s40\n"
    "s_add_u32 s39, s39, s41\n"
    "v_readlane_b32 s40, v147, 10\n"
    "v_readlane_b32 s41, v148, 10\n"
    "s_add_u32 s38, s38, s40\n"
    "s_add_u32 s39, s39, s41\n"
    "v_readlane_b32 s40, v147, 11\n"
    "v_readlane_b32 s41, v148, 11\n"
    "s_add_u32 s38, s38, s40\n"
    "s_add_u32 s39, s39, s41\n"
    "v_readlane_b32 s40, v147, 12\n"
    "v_readlane_b32 s41, v148, 12\n"
    "s_add_u32 s38, s38, s40\n"
    "s_add_u32 s39, s39, s41\n"
    "v_readlane_b32 s40, v147, 13\n"
    "v_readlane_b32 s41, v148, 13\n"
    "s_add_u32 s38, s38, s40\n"
    "s_add_u32 s39, s39, s41\n"
    "v_readlane_b32 s40, v147, 14\n"
    "v_readlane_b32 s41, v148, 14\n"
    "s_add_u32 s38, s38, s40\n"
    "s_add_u32 s39, s39, s41\n"
    "v_readlane_b32 s40, v147, 15\n"
    "v_readlane_b32 s41, v148, 15\n"
    "s_add_u32 s38, s38, s40\n"
    "s_add_u32 s39, s39, s41\n"
    "v_readlane_b32 s40, v147, 16\n"
    "v_readlane_b32 s41, v148, 16\n"
    "s_add_u32 s38, s38, s40\n"
    "s_add_u32 s39, s39, s41\n"
    "v_readlane_b32 s40, v147, 17\n"
    "v_readlane_b32 s41, v148, 17\n"
    "s_add_u32 s38, s38, s40\n"
    "s_add_u32 s39, s39, s41\n"
    "v_readlane_b32 s40, v147, 18\n"
    "v_readlane_b32 s41, v148, 18\n"
    "s_add_u32 s38, s38, s40\n"
    "s_add_u32 s39, s39, s41\n"
    "v_readlane_b32 s40, v147, 19\n"
    "v_readlane_b32 s41, v148, 19\n"
    "s_add_u32 s38, s38, s40\n"
    "s_add_u32 s39, s39, s41\n"
    "v_readlane_b32 s40, v147, 20\n"
    "v_readlane_b32 s41, v148, 20\n"
    "s_add_u32 s38, s38, s40\n"
    "s_add_u32 s39, s39, s41\n"
    "v_readlane_b32 s40, v147, 21\n"
    "v_readlane_b32 s41, v148, 21\n"
    "s_add_u32 s38, s38, s40\n"
    "s_add_u32 s39, s39, s41\n"
    "v_readlane_b32 s40, v147, 22\n"
    "v_readlane_b32 s41, v148, 22\n"
    "s_add_u32 s38, s38, s40\n"
    "s_add_u32 s39, s39, s41\n"
    "v_readlane_b32 s40, v147, 23\n"
    "v_readlane_b32 s41, v148, 23\n"
    "s_add_u32 s38, s38, s40\n"
    "s_add_u32 s39, s39, s41\n"
    "v_readlane_b32 s40, v147, 24\n"
    "v_readlane_b32 s41, v148, 24\n"
    "s_add_u32 s38, s38, s40\n"
    "s_add_u32 s39, s39, s41\n"
    "v_readlane_b32 s40, v147, 25\n"
    "v_readlane_b32 s41, v148, 25\n"
    "s_add_u32 s38, s38, s40\n"
    "s_add_u32 s39, s39, s41\n"
    "v_readlane_b32 s40, v147, 26\n"
    "v_readlane_b32 s41, v148, 26\n"
    "s_add_u32 s38, s38, s40\n"
    "s_add_u32 s39, s39, s41\n"
    "v_readlane_b32 s40, v147, 27\n"
    "v_readlane_b32 s41, v148, 27\n"
    "s_add_u32 s38, s38, s40\n"
    "s_add_u32 s39, s39, s41\n"
    "v_readlane_b32 s40, v147, 28\n"
    "v_readlane_b32 s41, v148, 28\n"
    "s_add_u32 s38, s38, s40\n"
    "s_add_u32 s39, s39, s41\n"
    "v_readlane_b32 s40, v147, 29\n"
    "v_readlane_b32 s41, v148, 29\n"
    "s_add_u32 s38, s38, s40\n"
    "s_add_u32 s39, s39, s41\n"
    "v_readlane_b32 s40, v147, 30\n"
    "v_readlane_b32 s41, v148, 30\n"
    "s_add_u32 s38, s38, s40\n"
    "s_add_u32 s39, s39, s41\n"
    "v_readlane_b32 s40, v147, 31\n"
    "v_readlane_b32 s41, v148, 31\n"
    "s_add_u32 s38, s38, s40\n"
    "s_add_u32 s39, s39, s41\n"
    "s_lshr_b32 s40, s91, 16\n"
    "s_and_b32 s41, s91, 0xffff\n"
    "s_mul_i32 s40, s40, 100\n"
    "s_mul_i32 s42, s41, 2250\n"
    "s_cmp_lt_u32 s40, s42\n"
    "s_cbranch_scc1 NOAGG_%=\n"
    "s_lshr_b32 s40, s38, 16\n"
    "s_and_b32 s41, s38, 0xffff\n"
    "s_lshr_b32 s42, s39, 16\n"
    "s_and_b32 s39, s39, 0xffff\n"
    "s_mul_i32 s39, s39, s40\n"
    "s_mul_i32 s42, s42, s41\n"
    "s_lshr_b32 s43, s42, 5\n"
    "s_sub_u32 s42, s42, s43\n"
    "s_lshr_b32 s43, s43, 1\n"
    "s_sub_u32 s42, s42, s43\n"
    "s_mul_i32 s40, s40, 800\n"
    "s_mov_b32 s43, 384\n"
    "s_cmp_lt_u32 s39, s42\n"
    "s_cbranch_scc1 AGGP_%=\n"
    "s_mov_b32 s43, 0\n"
    "s_mul_i32 s42, s41, 1728\n"
    "s_cmp_lt_u32 s40, s42\n"
    "s_cselect_b32 s43, 40, s43\n"
    "s_mul_i32 s42, s41, 1703\n"
    "s_cmp_lt_u32 s40, s42\n"
    "s_cselect_b32 s43, 128, s43\n"
    "s_mul_i32 s42, s41, 1698\n"
    "s_cmp_lt_u32 s40, s42\n"
    "s_cselect_b32 s43, 208, s43\n"
    "s_mul_i32 s42, s41, 1688\n"
    "s_cmp_lt_u32 s40, s42\n"
    "s_cselect_b32 s43, 288, s43\n"
    "s_mul_i32 s42, s41, 1670\n"
    "s_cmp_lt_u32 s40, s42\n"
    "s_cselect_b32 s43, 384, s43\n"
    "s_mul_i32 s42, s41, 1638\n"
    "s_cmp_lt_u32 s40, s42\n"
    "s_cselect_b32 s43, 40, s43\n"
    "AGGP_%=:\n"
    "s_xor_b32 s43, s43, s79\n"
    "v_mov_b32 v144, s43\n"
    "s_mov_b64 exec, 1\n"
    "buffer_store_dword v144, off, s[52:55], s37 offset:1024 sc1\n"
    "s_mov_b64 exec, -1\n"
    "s_nop 3\n"
    "NOAGG_%=:\n"
    "v_mfma_f32_16x16x32_bf16 v[116:119], v[136:139], a[184:187], v[116:119]\n"
    "ds_read_b64_tr_b16 v[96:97], v251 offset:0\n"
    "v_mov_b32 v221, v220\n"
    "v_mfma_f32_16x16x32_bf16 v[116:119], v[140:143], a[188:191], v[116:119]\n"
    "ds_read_b64_tr_b16 v[98:99], v251 offset:4096\n"
    "v_permlane16_swap_b32 v220, v221\n"
    "v_mfma_f32_16x16x32_bf16 v[176:179], a[200:203], a[120:123], v[176:179]\n"
    "ds_read_b64_tr_b16 v[100:101], v251 offset:8192\n"
    "v_max_f32 v223, v220, v221\n"
    "v_mfma_f32_16x16x32_bf16 v[176:179], a[204:207], a[124:127], v[176:179]\n"
    "ds_read_b64_tr_b16 v[102:103], v251 offset:12288\n"
    "v_sub_f32 v220, 0, v223\n"
    "s_add_u32 m0, s64, 98304\n"
    "s_add_u32 s74, s64, 32768\n"
    "buffer_load_dwordx4 v253, s[48:51], s74 offen offset:0 nt lds\n"
    "v_mfma_f32_16x16x32_bf16 v[180:183], a[220:223], a[124:127], v[212:215]\n"
    "ds_read_b64_tr_b16 v[104:105], v252 offset:0\n"
    "v_sub_f32 v221, 0, v223\n"
    "v_mfma_f32_16x16x32_bf16 v[180:183], a[216:219], a[120:123], v[180:183]\n"
    "ds_read_b64_tr_b16 v[106:107], v252 offset:4096\n"
    "v_sub_f32 v222, 0, v223\n"
    "v_mfma_f32_16x16x32_bf16 v[180:183], a[212:215], a[116:119], v[180:183]\n"
    "ds_read_b64_tr_b16 v[108:109], v252 offset:8192\n"
    "v_sub_f32 v223, 0, v223\n"
    "v_mfma_f32_16x16x32_bf16 v[180:183], a[208:211], a[112:115], v[180:183]\n"
    "ds_read_b64_tr_b16 v[110:111], v252 offset:12288\n"
    "v_max_f32 v224, v224, v115\n"
    "v_mfma_f32_16x16x32_bf16 v[184:187], a[224:227], a[112:115], v[212:215]\n"
    "ds_read_b64_tr_b16 v[112:113], v251 offset:256\n"
    "v_mov_b32 v225, v224\n"
    "v_mfma_f32_16x16x32_bf16 v[184:187], a[228:231], a[116:119], v[184:187]\n"
    "ds_read_b64_tr_b16 v[114:115], v251 offset:4352\n"
    "v_permlane32_swap_b32 v224, v225\n"
    "v_mfma_f32_16x16x32_bf16 v[184:187], a[232:235], a[120:123], v[184:187]\n"
    "ds_read_b64_tr_b16 v[120:121], v252 offset:256\n"
    "v_max_f32 v224, v224, v225\n"
    "v_mfma_f32_16x16x32_bf16 v[184:187], a[236:239], a[124:127], v[184:187]\n"
    "ds_read_b64_tr_b16 v[122:123], v252 offset:4352\n"
    "v_mov_b32 v225, v224\n"
    "buffer_load_dwordx4 v254, s[48:51], s74 offen offset:1024 nt lds\n"
    "v_mfma_f32_16x16x32_bf16 v[188:191], a[252:255], a[124:127], v[212:215]\n"
    "ds_read_b64_tr_b16 v[124:125], v252 offset:8448\n"
    "v_permlane16_swap_b32 v224, v225\n"
    "v_mfma_f32_16x16x32_bf16 v[188:191], a[248:251], a[120:123], v[188:191]\n"
    "ds_read_b64_tr_b16 v[126:127], v252 offset:12544\n"
    "v_max_f32 v227, v224, v225\n"
    "v_mfma_f32_16x16x32_bf16 v[188:191], a[244:247], a[116:119], v[188:191]\n"
    "ds_read_b64_tr_b16 v[128:129], v251 offset:512\n"
    "v_sub_f32 v224, 0, v227\n"
    "v_mfma_f32_16x16x32_bf16 v[188:191], a[240:243], a[112:115], v[188:191]\n"
    "ds_read_b64_tr_b16 v[130:131], v251 offset:4608\n"
    "v_sub_f32 v225, 0, v227\n"
    "v_mfma_f32_16x16x32_bf16 v[160:163], a[192:195], a[128:131], v[216:219]\n"
    "ds_read_b64_tr_b16 v[132:133], v251 offset:8704\n"
    "v_sub_f32 v226, 0, v227\n"
    "v_mfma_f32_16x16x32_bf16 v[160:163], a[196:199], a[132:135], v[160:163]\n"
    "ds_read_b64_tr_b16 v[134:135], v251 offset:12800\n"
    "v_sub_f32 v227, 0, v227\n"
    "v_mfma_f32_16x16x32_bf16 v[160:163], a[200:203], a[136:139], v[160:163]\n"
    "ds_read_b64_tr_b16 v[136:137], v252 offset:512\n"
    "v_max3_f32 v228, v116, v117, v118\n"
    "v_mfma_f32_16x16x32_bf16 v[160:163], a[204:207], a[140:143], v[160:163]\n"
    "ds_read_b64_tr_b16 v[116:117], v251 offset:8448\n"
    "v_max_f32 v228, v228, v119\n"
    "buffer_load_dwordx4 v253, s[48:51], s74 offen offset:2048 nt lds\n"
    "v_mfma_f32_16x16x32_bf16 v[164:167], a[220:223], a[140:143], v[216:219]\n"
    "ds_read_b64_tr_b16 v[118:119], v251 offset:12544\n"
    "v_mov_b32 v229, v228\n"
    "v_mfma_f32_16x16x32_bf16 v[164:167], a[216:219], a[136:139], v[164:167]\n"
    "ds_read_b64_tr_b16 v[138:139], v252 offset:4608\n"
    "v_permlane32_swap_b32 v228, v229\n"
    "v_mfma_f32_16x16x32_bf16 v[164:167], a[212:215], a[132:135], v[164:167]\n"
    "ds_read_b64_tr_b16 v[140:141], v252 offset:8704\n"
    "v_max_f32 v228, v228, v229\n"
    "v_mfma_f32_16x16x32_bf16 v[164:167], a[208:211], a[128:131], v[164:167]\n"
    "ds_read_b64_tr_b16 v[142:143], v252 offset:12800\n"
    "v_mov_b32 v229, v228\n"
    "s_waitcnt lgkmcnt(6)\n"
    "v_mfma_f32_16x16x32_bf16 v[0:3], v[192:195], v[96:99], 0\n"
    "ds_read_b64_tr_b16 v[144:145], v251 offset:768\n"
    "v_permlane16_swap_b32 v228, v229\n"
    "v_mfma_f32_16x16x32_bf16 v[4:7], v[192:195], v[104:107], 0\n"
    "ds_read_b64_tr_b16 v[146:147], v251 offset:4864\n"
    "v_max_f32 v231, v228, v229\n"
    "v_mfma_f32_16x16x32_bf16 v[8:11], v[192:195], v[112:115], 0\n"
    "ds_read_b64_tr_b16 v[148:149], v251 offset:8960\n"
    "v_sub_f32 v228, 0, v231\n"
    "v_mfma_f32_16x16x32_bf16 v[12:15], v[192:195], v[120:123], 0\n"
    "ds_read_b64_tr_b16 v[150:151], v251 offset:13056\n"
    "v_sub_f32 v229, 0, v231\n"
    "buffer_load_dwordx4 v254, s[48:51], s74 offen offset:3072 nt lds\n"
    "v_mfma_f32_16x16x32_bf16 a[0:3], v[192:195], v[128:131], 0\n"
    "ds_read_b64_tr_b16 v[152:153], v252 offset:768\n"
    "v_sub_f32 v230, 0, v231\n"
    "s_waitcnt lgkmcnt(6)\n"
    "v_mfma_f32_16x16x32_bf16 a[4:7], v[192:195], v[136:139], 0\n"
    "ds_read_b64_tr_b16 v[154:155], v252 offset:4864\n"
    "v_sub_f32 v231, 0, v231\n"
    "v_mfma_f32_16x16x32_bf16 v[232:235], v[246:249], v[192:195], v[232:235]\n"
    "ds_read_b64_tr_b16 v[156:157], v252 offset:8960\n"
    "s_waitcnt lgkmcnt(5)\n"
    "v_mfma_f32_16x16x32_bf16 a[8:11], v[192:195], v[144:147], 0\n"
    "ds_read_b64_tr_b16 v[158:159], v252 offset:13056\n"
    "v_cvt_pk_bf16_f32 v197, v170, v171\n"
    "v_mfma_f32_16x16x32_bf16 v[168:171], a[224:227], a[128:131], v[216:219]\n"
    "v_exp_f32 v172, v172\n"
    "v_mfma_f32_16x16x32_bf16 v[168:171], a[228:231], a[132:135], v[168:171]\n"
    "v_exp_f32 v173, v173\n"
    "v_mfma_f32_16x16x32_bf16 v[168:171], a[232:235], a[136:139], v[168:171]\n"
    "v_cvt_pk_bf16_f32 v198, v172, v173\n"
    "v_exp_f32 v174, v174\n"
    "v_mfma_f32_16x16x32_bf16 v[168:171], a[236:239], a[140:143], v[168:171]\n"
    "v_exp_f32 v175, v175\n"
    "s_waitcnt lgkmcnt(2)\n"
    "v_mfma_f32_16x16x32_bf16 a[12:15], v[192:195], v[152:155], 0\n"
    "v_cvt_pk_bf16_f32 v199, v174, v175\n"
    "v_exp_f32 v176, v176\n"
    "v_mfma_f32_16x16x32_bf16 v[172:175], a[252:255], a[140:143], v[216:219]\n"
    "s_add_u32 m0, s64, 102400\n"
    "s_add_u32 s74, s64, 36864\n"
    "buffer_load_dwordx4 v253, s[48:51], s74 offen offset:0 nt lds\n"
    "v_exp_f32 v177, v177\n"
    "v_mfma_f32_16x16x32_bf16 v[172:175], a[248:251], a[136:139], v[172:175]\n"
    "v_cvt_pk_bf16_f32 v200, v176, v177\n"
    "v_exp_f32 v178, v178\n"
    "v_mfma_f32_16x16x32_bf16 v[172:175], a[244:247], a[132:135], v[172:175]\n"
    "v_exp_f32 v179, v179\n"
    "v_mfma_f32_16x16x32_bf16 v[172:175], a[240:243], a[128:131], v[172:175]\n"
    "v_cvt_pk_bf16_f32 v201, v178, v179\n"
    "v_mfma_f32_16x16x32_bf16 v[0:3], v[196:199], v[100:103], v[0:3]\n"
    "v_exp_f32 v180, v180\n"
    "v_mfma_f32_16x16x32_bf16 v[4:7], v[196:199], v[108:111], v[4:7]\n"
    "v_exp_f32 v181, v181\n"
    "v_mfma_f32_16x16x32_bf16 v[8:11], v[196:199], v[116:119], v[8:11]\n"
    "v_cvt_pk_bf16_f32 v202, v180, v181\n"
    "v_exp_f32 v182, v182\n"
    "v_mfma_f32_16x16x32_bf16 v[12:15], v[196:199], v[124:127], v[12:15]\n"
    "v_exp_f32 v183, v183\n"
    "v_mfma_f32_16x16x32_bf16 a[0:3], v[196:199], v[132:135], a[0:3]\n"
    "v_cvt_pk_bf16_f32 v203, v182, v183\n"
    "v_mfma_f32_16x16x32_bf16 a[4:7], v[196:199], v[140:143], a[4:7]\n"
    "v_exp_f32 v184, v184\n"
    "v_mfma_f32_16x16x32_bf16 a[8:11], v[196:199], v[148:151], a[8:11]\n"
    "v_exp_f32 v185, v185\n"
    "buffer_load_dwordx4 v254, s[48:51], s74 offen offset:1024 nt lds\n"
    "s_waitcnt lgkmcnt(0)\n"
    "v_mfma_f32_16x16x32_bf16 a[12:15], v[196:199], v[156:159], a[12:15]\n"
    "v_cvt_pk_bf16_f32 v204, v184, v185\n"
    "v_exp_f32 v186, v186\n"
    "v_mfma_f32_16x16x32_bf16 v[232:235], v[246:249], v[196:199], v[232:235]\n"
    "v_exp_f32 v187, v187\n"
    "v_mfma_f32_16x16x32_bf16 v[176:179], a[192:195], a[144:147], v[220:223]\n"
    "v_mfma_f32_16x16x32_bf16 v[176:179], a[196:199], a[148:151], v[176:179]\n"
    "v_cvt_pk_bf16_f32 v205, v186, v187\n"
    "v_exp_f32 v188, v188\n"
    "v_mfma_f32_16x16x32_bf16 v[176:179], a[200:203], a[152:155], v[176:179]\n"
    "v_exp_f32 v189, v189\n"
    "v_mfma_f32_16x16x32_bf16 v[176:179], a[204:207], a[156:159], v[176:179]\n"
    "v_cvt_pk_bf16_f32 v206, v188, v189\n"
    "v_exp_f32 v190, v190\n"
    "v_mfma_f32_16x16x32_bf16 v[180:183], a[220:223], a[156:159], v[220:223]\n"
    "v_exp_f32 v191, v191\n"
    "v_mfma_f32_16x16x32_bf16 v[180:183], a[216:219], a[152:155], v[180:183]\n"
    "v_cvt_pk_bf16_f32 v207, v190, v191\n"
    "v_mfma_f32_16x16x32_bf16 v[180:183], a[212:215], a[148:151], v[180:183]\n"
    "v_exp_f32 v160, v160\n"
    "v_mfma_f32_16x16x32_bf16 v[180:183], a[208:211], a[144:147], v[180:183]\n"
    "v_exp_f32 v161, v161\n"
    "v_mfma_f32_16x16x32_bf16 v[184:187], a[224:227], a[144:147], v[220:223]\n"
    "buffer_load_dwordx4 v253, s[48:51], s74 offen offset:2048 nt lds\n"
    "v_cvt_pk_bf16_f32 v192, v160, v161\n"
    "v_exp_f32 v162, v162\n"
    "v_mfma_f32_16x16x32_bf16 v[184:187], a[228:231], a[148:151], v[184:187]\n"
    "v_exp_f32 v163, v163\n"
    "v_mfma_f32_16x16x32_bf16 v[184:187], a[232:235], a[152:155], v[184:187]\n"
    "v_cvt_pk_bf16_f32 v193, v162, v163\n"
    "v_mfma_f32_16x16x32_bf16 v[184:187], a[236:239], a[156:159], v[184:187]\n"
    "v_exp_f32 v164, v164\n"
    "v_mfma_f32_16x16x32_bf16 v[188:191], a[252:255], a[156:159], v[220:223]\n"
    "v_exp_f32 v165, v165\n"
    "v_mfma_f32_16x16x32_bf16 v[188:191], a[248:251], a[152:155], v[188:191]\n"
    "v_cvt_pk_bf16_f32 v194, v164, v165\n"
    "v_exp_f32 v166, v166\n"
    "v_mfma_f32_16x16x32_bf16 v[188:191], a[244:247], a[148:151], v[188:191]\n"
    "v_exp_f32 v167, v167\n"
    "v_mfma_f32_16x16x32_bf16 v[188:191], a[240:243], a[144:147], v[188:191]\n"
    "v_cvt_pk_bf16_f32 v195, v166, v167\n"
    "v_mfma_f32_16x16x32_bf16 v[16:19], v[200:203], v[96:99], 0\n"
    "v_exp_f32 v168, v168\n"
    "v_mfma_f32_16x16x32_bf16 v[16:19], v[204:207], v[100:103], v[16:19]\n"
    "v_exp_f32 v169, v169\n"
    "v_mfma_f32_16x16x32_bf16 v[20:23], v[200:203], v[104:107], 0\n"
    "v_cvt_pk_bf16_f32 v196, v168, v169\n"
    "v_exp_f32 v170, v170\n"
    "buffer_load_dwordx4 v254, s[48:51], s74 offen offset:3072 nt lds\n"
    "v_mfma_f32_16x16x32_bf16 v[20:23], v[204:207], v[108:111], v[20:23]\n"
    "v_exp_f32 v171, v171\n"
    "v_mfma_f32_16x16x32_bf16 v[24:27], v[200:203], v[112:115], 0\n"
    "v_mfma_f32_16x16x32_bf16 v[24:27], v[204:207], v[116:119], v[24:27]\n"
    "v_cvt_pk_bf16_f32 v197, v170, v171\n"
    "v_exp_f32 v172, v172\n"
    "v_mfma_f32_16x16x32_bf16 v[28:31], v[200:203], v[120:123], 0\n"
    "v_exp_f32 v173, v173\n"
    "v_mfma_f32_16x16x32_bf16 v[28:31], v[204:207], v[124:127], v[28:31]\n"
    "v_cvt_pk_bf16_f32 v198, v172, v173\n"
    "v_exp_f32 v174, v174\n"
    "v_mfma_f32_16x16x32_bf16 a[16:19], v[200:203], v[128:131], 0\n"
    "v_exp_f32 v175, v175\n"
    "v_mfma_f32_16x16x32_bf16 a[16:19], v[204:207], v[132:135], a[16:19]\n"
    "v_cvt_pk_bf16_f32 v199, v174, v175\n"
    "v_mfma_f32_16x16x32_bf16 a[20:23], v[200:203], v[136:139], 0\n"
    "v_exp_f32 v176, v176\n"
    "v_mfma_f32_16x16x32_bf16 a[20:23], v[204:207], v[140:143], a[20:23]\n"
    "v_exp_f32 v177, v177\n"
    "v_mfma_f32_16x16x32_bf16 a[24:27], v[200:203], v[144:147], 0\n"
    "v_exp_f32 v178, v178\n"
    "v_mfma_f32_16x16x32_bf16 a[24:27], v[204:207], v[148:151], a[24:27]\n"
    "v_exp_f32 v179, v179\n"
    "s_add_u32 m0, s64, 0\n"
    "s_add_u32 s74, s64, 65536\n"
    "buffer_load_dwordx4 v253, s[44:47], s74 offen offset:0 nt lds\n"
    "v_mfma_f32_16x16x32_bf16 a[28:31], v[200:203], v[152:155], 0\n"
    "v_exp_f32 v180, v180\n"
    "v_mfma_f32_16x16x32_bf16 a[28:31], v[204:207], v[156:159], a[28:31]\n"
    "v_exp_f32 v181, v181\n"
    "v_mfma_f32_16x16x32_bf16 v[234:237], v[246:249], v[200:203], v[234:237]\n"
    "v_cvt_pk_bf16_f32 v200, v176, v177\n"
    "v_cvt_pk_bf16_f32 v201, v178, v179\n"
    "v_mfma_f32_16x16x32_bf16 v[234:237], v[246:249], v[204:207], v[234:237]\n"
    "v_cvt_pk_bf16_f32 v202, v180, v181\n"
    "v_exp_f32 v182, v182\n"
    "v_mfma_f32_16x16x32_bf16 v[160:163], a[192:195], a[160:163], v[224:227]\n"
    "v_exp_f32 v183, v183\n"
    "v_mfma_f32_16x16x32_bf16 v[160:163], a[196:199], a[164:167], v[160:163]\n"
    "v_cvt_pk_bf16_f32 v203, v182, v183\n"
    "v_exp_f32 v184, v184\n"
    "v_mfma_f32_16x16x32_bf16 v[160:163], a[200:203], a[168:171], v[160:163]\n"
    "v_exp_f32 v185, v185\n"
    "v_mfma_f32_16x16x32_bf16 v[160:163], a[204:207], a[172:175], v[160:163]\n"
    "v_cvt_pk_bf16_f32 v204, v184, v185\n"
    "v_exp_f32 v186, v186\n"
    "v_mfma_f32_16x16x32_bf16 v[164:167], a[220:223], a[172:175], v[224:227]\n"
    "v_exp_f32 v187, v187\n"
    "v_mfma_f32_16x16x32_bf16 v[164:167], a[216:219], a[168:171], v[164:167]\n"
    "buffer_load_dwordx4 v254, s[44:47], s74 offen offset:1024 nt lds\n"
    "v_cvt_pk_bf16_f32 v205, v186, v187\n"
    "v_exp_f32 v188, v188\n"
    "v_mfma_f32_16x16x32_bf16 v[164:167], a[212:215], a[164:167], v[164:167]\n"
    "v_exp_f32 v189, v189\n"
    "v_mfma_f32_16x16x32_bf16 v[164:167], a[208:211], a[160:163], v[164:167]\n"
    "v_cvt_pk_bf16_f32 v206, v188, v189\n"
    "v_exp_f32 v190, v190\n"
    "v_mfma_f32_16x16x32_bf16 v[168:171], a[224:227], a[160:163], v[224:227]\n"
    "v_exp_f32 v191, v191\n"
    "v_mfma_f32_16x16x32_bf16 v[168:171], a[228:231], a[164:167], v[168:171]\n"
    "v_cvt_pk_bf16_f32 v207, v190, v191\n"
    "v_xor_b32 v251, 0x10000, v251\n"
    "v_mfma_f32_16x16x32_bf16 v[168:171], a[232:235], a[168:171], v[168:171]\n"
    "v_xor_b32 v252, 0x10000, v252\n"
    "v_exp_f32 v160, v160\n"
    "v_mfma_f32_16x16x32_bf16 v[168:171], a[236:239], a[172:175], v[168:171]\n"
    "v_exp_f32 v161, v161\n"
    "v_mfma_f32_16x16x32_bf16 v[172:175], a[252:255], a[172:175], v[224:227]\n"
    "v_exp_f32 v162, v162\n"
    "v_mfma_f32_16x16x32_bf16 v[172:175], a[248:251], a[168:171], v[172:175]\n"
    "v_exp_f32 v163, v163\n"
    "v_mfma_f32_16x16x32_bf16 v[172:175], a[244:247], a[164:167], v[172:175]\n"
    "buffer_load_dwordx4 v253, s[44:47], s74 offen offset:2048 nt lds\n"
    "v_exp_f32 v164, v164\n"
    "v_mfma_f32_16x16x32_bf16 v[172:175], a[240:243], a[160:163], v[172:175]\n"
    "v_exp_f32 v165, v165\n"
    "v_mfma_f32_16x16x32_bf16 v[32:35], v[192:195], v[96:99], 0\n"
    "v_exp_f32 v166, v166\n"
    "v_mfma_f32_16x16x32_bf16 v[32:35], v[196:199], v[100:103], v[32:35]\n"
    "v_exp_f32 v167, v167\n"
    "v_mfma_f32_16x16x32_bf16 v[36:39], v[192:195], v[104:107], 0\n"
    "v_exp_f32 v168, v168\n"
    "v_mfma_f32_16x16x32_bf16 v[36:39], v[196:199], v[108:111], v[36:39]\n"
    "v_exp_f32 v169, v169\n"
    "v_mfma_f32_16x16x32_bf16 v[40:43], v[192:195], v[112:115], 0\n"
    "v_exp_f32 v170, v170\n"
    "v_mfma_f32_16x16x32_bf16 v[40:43], v[196:199], v[116:119], v[40:43]\n"
    "v_exp_f32 v171, v171\n"
    "v_mfma_f32_16x16x32_bf16 v[44:47], v[192:195], v[120:123], 0\n"
    "v_exp_f32 v172, v172\n"
    "v_mfma_f32_16x16x32_bf16 v[44:47], v[196:199], v[124:127], v[44:47]\n"
    "v_exp_f32 v173, v173\n"
    "v_mfma_f32_16x16x32_bf16 a[32:35], v[192:195], v[128:131], 0\n"
    "v_exp_f32 v174, v174\n"
    "v_mfma_f32_16x16x32_bf16 a[32:35], v[196:199], v[132:135], a[32:35]\n"
    "v_exp_f32 v175, v175\n"
    "buffer_load_dwordx4 v254, s[44:47], s74 offen offset:3072 nt lds\n"
    "v_mfma_f32_16x16x32_bf16 a[36:39], v[192:195], v[136:139], 0\n"
    "v_mfma_f32_16x16x32_bf16 a[36:39], v[196:199], v[140:143], a[36:39]\n"
    "v_mfma_f32_16x16x32_bf16 a[40:43], v[192:195], v[144:147], 0\n"
    "v_mfma_f32_16x16x32_bf16 a[40:43], v[196:199], v[148:151], a[40:43]\n"
    "v_mfma_f32_16x16x32_bf16 a[44:47], v[192:195], v[152:155], 0\n"
    "v_mfma_f32_16x16x32_bf16 a[44:47], v[196:199], v[156:159], a[44:47]\n"
    "v_mfma_f32_16x16x32_bf16 v[236:239], v[246:249], v[192:195], v[236:239]\n"
    "v_cvt_pk_bf16_f32 v192, v160, v161\n"
    "v_cvt_pk_bf16_f32 v193, v162, v163\n"
    "v_mfma_f32_16x16x32_bf16 v[236:239], v[246:249], v[196:199], v[236:239]\n"
    "v_cvt_pk_bf16_f32 v194, v164, v165\n"
    "v_cvt_pk_bf16_f32 v195, v166, v167\n"
    "v_mfma_f32_16x16x32_bf16 v[176:179], a[192:195], a[176:179], v[228:231]\n"
    "s_waitcnt vmcnt(12) lgkmcnt(0)\n"
    "s_barrier\n"
    "v_mfma_f32_16x16x32_bf16 v[176:179], a[196:199], a[180:183], v[176:179]\n"
    "ds_read_b128 a[192:195], v250 offset:0\n"
    "v_mfma_f32_16x16x32_bf16 v[176:179], a[200:203], a[184:187], v[176:179]\n"
    "ds_read_b128 a[196:199], v250 offset:256\n"
    "v_cvt_pk_bf16_f32 v196, v168, v169\n"
    "v_mfma_f32_16x16x32_bf16 v[176:179], a[204:207], a[188:191], v[176:179]\n"
    "ds_read_b128 a[200:203], v250 offset:512\n"
    "v_cvt_pk_bf16_f32 v197, v170, v171\n"
    "v_mfma_f32_16x16x32_bf16 v[180:183], a[220:223], a[188:191], v[228:231]\n"
    "ds_read_b128 a[204:207], v250 offset:768\n"
    "v_cvt_pk_bf16_f32 v198, v172, v173\n"
    "v_mfma_f32_16x16x32_bf16 v[180:183], a[216:219], a[184:187], v[180:183]\n"
    "ds_read_b128 a[220:223], v250 offset:4864\n"
    "v_cvt_pk_bf16_f32 v199, v174, v175\n"
    "s_add_u32 m0, s64, 4096\n"
    "s_add_u32 s74, s64, 69632\n"
    "buffer_load_dwordx4 v253, s[44:47], s74 offen offset:0 nt lds\n"
    "v_mfma_f32_16x16x32_bf16 v[180:183], a[212:215], a[180:183], v[180:183]\n"
    "ds_read_b128 a[216:219], v250 offset:4608\n"
    "v_mfma_f32_16x16x32_bf16 v[180:183], a[208:211], a[176:179], v[180:183]\n"
    "ds_read_b128 a[212:215], v250 offset:4352\n"
    "v_exp_f32 v176, v176\n"
    "v_mfma_f32_16x16x32_bf16 v[184:187], a[224:227], a[176:179], v[228:231]\n"
    "ds_read_b128 a[208:211], v250 offset:4096\n"
    "v_exp_f32 v177, v177\n"
    "v_mfma_f32_16x16x32_bf16 v[184:187], a[228:231], a[180:183], v[184:187]\n"
    "ds_read_b128 a[224:227], v250 offset:8192\n"
    "v_exp_f32 v178, v178\n"
    "v_mfma_f32_16x16x32_bf16 v[184:187], a[232:235], a[184:187], v[184:187]\n"
    "ds_read_b128 a[228:231], v250 offset:8448\n"
    "v_exp_f32 v179, v179\n"
    "buffer_load_dword v245, off, s[52:55], s37 offset:1024 sc1\n"
    "v_mfma_f32_16x16x32_bf16 v[184:187], a[236:239], a[188:191], v[184:187]\n"
    "ds_read_b128 a[232:235], v250 offset:8704\n"
    "v_mfma_f32_16x16x32_bf16 v[188:191], a[252:255], a[188:191], v[228:231]\n"
    "ds_read_b128 a[236:239], v250 offset:8960\n"
    "v_exp_f32 v180, v180\n"
    "v_mfma_f32_16x16x32_bf16 v[188:191], a[248:251], a[184:187], v[188:191]\n"
    "ds_read_b128 a[252:255], v250 offset:13056\n"
    "v_exp_f32 v181, v181\n"
    "v_mfma_f32_16x16x32_bf16 v[188:191], a[244:247], a[180:183], v[188:191]\n"
    "ds_read_b128 a[248:251], v250 offset:12800\n"
    "v_exp_f32 v182, v182\n"
    "v_mfma_f32_16x16x32_bf16 v[188:191], a[240:243], a[176:179], v[188:191]\n"
    "ds_read_b128 a[244:247], v250 offset:12544\n"
    "v_exp_f32 v183, v183\n"
    "buffer_load_dwordx4 v254, s[44:47], s74 offen offset:1024 nt lds\n"
    "v_mfma_f32_16x16x32_bf16 v[48:51], v[200:203], v[96:99], 0\n"
    "ds_read_b128 a[240:243], v250 offset:12288\n"
    "v_mfma_f32_16x16x32_bf16 v[48:51], v[204:207], v[100:103], v[48:51]\n"
    "v_exp_f32 v184, v184\n"
    "v_mfma_f32_16x16x32_bf16 v[52:55], v[200:203], v[104:107], 0\n"
    "v_exp_f32 v185, v185\n"
    "v_mfma_f32_16x16x32_bf16 v[52:55], v[204:207], v[108:111], v[52:55]\n"
    "v_exp_f32 v186, v186\n"
    "v_mfma_f32_16x16x32_bf16 v[56:59], v[200:203], v[112:115], 0\n"
    "v_exp_f32 v187, v187\n"
    "v_mfma_f32_16x16x32_bf16 v[56:59], v[204:207], v[116:119], v[56:59]\n"
    "v_exp_f32 v188, v188\n"
    "v_mfma_f32_16x16x32_bf16 v[60:63], v[200:203], v[120:123], 0\n"
    "v_exp_f32 v189, v189\n"
    "v_mfma_f32_16x16x32_bf16 v[60:63], v[204:207], v[124:127], v[60:63]\n"
    "v_exp_f32 v190, v190\n"
    "v_mfma_f32_16x16x32_bf16 a[48:51], v[200:203], v[128:131], 0\n"
    "v_exp_f32 v191, v191\n"
    "v_mfma_f32_16x16x32_bf16 a[48:51], v[204:207], v[132:135], a[48:51]\n"
    "v_xor_b32 v250, 0x10000, v250\n"
    "v_mfma_f32_16x16x32_bf16 a[52:55], v[200:203], v[136:139], 0\n"
    "v_mfma_f32_16x16x32_bf16 a[52:55], v[204:207], v[140:143], a[52:55]\n"
    "v_mfma_f32_16x16x32_bf16 a[56:59], v[200:203], v[144:147], 0\n"
    "v_mfma_f32_16x16x32_bf16 a[56:59], v[204:207], v[148:151], a[56:59]\n"
    "v_mfma_f32_16x16x32_bf16 a[60:63], v[200:203], v[152:155], 0\n"
    "v_mfma_f32_16x16x32_bf16 a[60:63], v[204:207], v[156:159], a[60:63]\n"
    "v_mfma_f32_16x16x32_bf16 v[238:241], v[246:249], v[200:203], v[238:241]\n"
    "v_cvt_pk_bf16_f32 v200, v176, v177\n"
    "buffer_load_dwordx4 v253, s[44:47], s74 offen offset:2048 nt lds\n"
    "v_cvt_pk_bf16_f32 v201, v178, v179\n"
    "v_mfma_f32_16x16x32_bf16 v[238:241], v[246:249], v[204:207], v[238:241]\n"
    "v_cvt_pk_bf16_f32 v202, v180, v181\n"
    "v_cvt_pk_bf16_f32 v203, v182, v183\n"
    "s_waitcnt lgkmcnt(0)\n"
    "v_mfma_f32_16x16x32_bf16 v[160:163], a[192:195], a[96:99], v[208:211]\n"
    "v_cvt_pk_bf16_f32 v204, v184, v185\n"
    "v_cvt_pk_bf16_f32 v205, v186, v187\n"
    "v_mfma_f32_16x16x32_bf16 v[160:163], a[196:199], a[100:103], v[160:163]\n"
    "v_cvt_pk_bf16_f32 v206, v188, v189\n"
    "v_cvt_pk_bf16_f32 v207, v190, v191\n"
    "v_mfma_f32_16x16x32_bf16 v[160:163], a[200:203], a[104:107], v[160:163]\n"
    "v_mfma_f32_16x16x32_bf16 v[160:163], a[204:207], a[108:111], v[160:163]\n"
    "v_mfma_f32_16x16x32_bf16 v[164:167], a[220:223], a[108:111], v[208:211]\n"
    "v_mfma_f32_16x16x32_bf16 v[164:167], a[216:219], a[104:107], v[164:167]\n"
    "v_mfma_f32_16x16x32_bf16 v[164:167], a[212:215], a[100:103], v[164:167]\n"
    "v_mfma_f32_16x16x32_bf16 v[164:167], a[208:211], a[96:99], v[164:167]\n"
    "v_mfma_f32_16x16x32_bf16 v[168:171], a[224:227], a[96:99], v[208:211]\n"
    "v_mfma_f32_16x16x32_bf16 v[168:171], a[228:231], a[100:103], v[168:171]\n"
    "v_mfma_f32_16x16x32_bf16 v[168:171], a[232:235], a[104:107], v[168:171]\n"
    "v_mfma_f32_16x16x32_bf16 v[168:171], a[236:239], a[108:111], v[168:171]\n"
    "v_mfma_f32_16x16x32_bf16 v[172:175], a[252:255], a[108:111], v[208:211]\n"
    "v_mfma_f32_16x16x32_bf16 v[172:175], a[248:251], a[104:107], v[172:175]\n"
    "v_mfma_f32_16x16x32_bf16 v[172:175], a[244:247], a[100:103], v[172:175]\n"
    "v_mfma_f32_16x16x32_bf16 v[172:175], a[240:243], a[96:99], v[172:175]\n"
    "v_mfma_f32_16x16x32_bf16 v[64:67], v[192:195], v[96:99], 0\n"
    "v_mfma_f32_16x16x32_bf16 v[64:67], v[196:199], v[100:103], v[64:67]\n"
    "v_mfma_f32_16x16x32_bf16 v[68:71], v[192:195], v[104:107], 0\n"
    "buffer_load_dwordx4 v254, s[44:47], s74 offen offset:3072 nt lds\n"
    "v_mfma_f32_16x16x32_bf16 v[68:71], v[196:199], v[108:111], v[68:71]\n"
    "v_mfma_f32_16x16x32_bf16 v[72:75], v[192:195], v[112:115], 0\n"
    "v_mfma_f32_16x16x32_bf16 v[72:75], v[196:199], v[116:119], v[72:75]\n"
    "v_mfma_f32_16x16x32_bf16 v[76:79], v[192:195], v[120:123], 0\n"
    "v_mfma_f32_16x16x32_bf16 v[76:79], v[196:199], v[124:127], v[76:79]\n"
    "v_mfma_f32_16x16x32_bf16 a[64:67], v[192:195], v[128:131], 0\n"
    "v_mfma_f32_16x16x32_bf16 a[64:67], v[196:199], v[132:135], a[64:67]\n"
    "v_mfma_f32_16x16x32_bf16 a[68:71], v[192:195], v[136:139], 0\n"
    "v_mfma_f32_16x16x32_bf16 a[68:71], v[196:199], v[140:143], a[68:71]\n"
    "v_mfma_f32_16x16x32_bf16 a[72:75], v[192:195], v[144:147], 0\n"
    "v_mfma_f32_16x16x32_bf16 a[72:75], v[196:199], v[148:151], a[72:75]\n"
    "v_mfma_f32_16x16x32_bf16 a[76:79], v[192:195], v[152:155], 0\n"
    "v_mfma_f32_16x16x32_bf16 a[76:79], v[196:199], v[156:159], a[76:79]\n"
    "v_mfma_f32_16x16x32_bf16 v[240:243], v[246:249], v[192:195], v[240:243]\n"
    "v_mfma_f32_16x16x32_bf16 v[240:243], v[246:249], v[196:199], v[240:243]\n"
    "s_add_u32 s60, s64, 65536\n"
    "s_mov_b32 s59, 0\n"
    "s_mov_b32 s91, s64\n"
    "s_mul_i32 s16, s90, 6\n"
    "s_sub_u32 s22, %[rk], s16\n"
    "s_and_b32 s16, %[xcd], 1\n"
    "s_mul_i32 s16, s16, 6\n"
    "s_add_u32 s22, s22, s16\n"
    "s_mul_i32 s23, s22, 5\n"
    "s_add_u32 s23, s23, s90\n"
    "s_lshl_b32 s16, s22, 2\n"
    "s_add_u32 s16, s16, %[w]\n"
    "s_mul_i32 s43, s16, 40960\n"
    "s_mul_i32 s18, s16, 43\n"
    "s_lshr_b32 s18, s18, 8\n"
    "s_lshl_b32 s18, s18, 2\n"
    "s_add_u32 s18, s18, s89\n"
    "s_lshl_b32 s18, s18, 7\n"
    "s_add_u32 s18, s18, 41943040\n"
    "s_lshl_b32 s40, s16, 18\n"
    "s_lshl_b32 s17, s89, 15\n"
    "s_add_u32 s40, s40, s17\n"
    "s_lshl_b32 s16, s90, 8\n"
    "s_add_u32 s43, s43, s16\n"
    "s_lshl_b32 s16, s89, 6\n"
    "s_add_u32 s43, s43, s16\n"
    "s_lshl_b32 s22, s23, 17\n"
    "s_add_u32 s22, s22, 16777216\n"
    "s_lshl_b32 s17, %[w], 11\n"
    "s_add_u32 s22, s22, s17\n"
    "s_add_u32 s16, s89, 1\n"
    "s_and_b32 s16, s16, 3\n"
    "s_lshl_b32 s61, s16, 2\n"
    "s_add_u32 s61, s61, s89\n"
    "s_lshl_b32 s61, s61, 13\n"
    "s_add_u32 s61, s61, s22\n"
    "s_lshl_b32 s67, s89, 2\n"
    "s_add_u32 s67, s67, s16\n"
    "s_lshl_b32 s67, s67, 13\n"
    "s_add_u32 s67, s67, s22\n"
    "s_add_u32 s16, s89, 2\n"
    "s_and_b32 s16, s16, 3\n"
    "s_lshl_b32 s62, s16, 2\n"
    "s_add_u32 s62, s62, s89\n"
    "s_lshl_b32 s62, s62, 13\n"
    "s_add_u32 s62, s62, s22\n"
    "s_lshl_b32 s68, s89, 2\n"
    "s_add_u32 s68, s68, s16\n"
    "s_lshl_b32 s68, s68, 13\n"
    "s_add_u32 s68, s68, s22\n"
    "s_add_u32 s16, s89, 3\n"
    "s_and_b32 s16, s16, 3\n"
    "s_lshl_b32 s63, s16, 2\n"
    "s_add_u32 s63, s63, s89\n"
    "s_lshl_b32 s63, s63, 13\n"
    "s_add_u32 s63, s63, s22\n"
    "s_lshl_b32 s69, s89, 2\n"
    "s_add_u32 s69, s69, s16\n"
    "s_lshl_b32 s69, s69, 13\n"
    "s_add_u32 s69, s69, s22\n"
    "s_lshl_b32 s19, s23, 7\n"
    "s_add_u32 s19, s19, 41951232\n"
    "s_lshl_b32 s20, s89, 3\n"
    "s_add_u32 s20, s20, s19\n"
    "s_lshl_b32 s21, %[b], 2\n"
    "s_add_u32 s21, s21, s89\n"
    "s_lshl_b32 s21, s21, 7\n"
    "s_and_b32 s16, %[h], 7\n"
    "s_lshl_b32 s16, s16, 3\n"
    "s_add_u32 s21, s21, s16\n"
    "s_add_u32 s21, s21, 41943040\n"
    "s_add_u32 s17, s89, 1\n"
    "s_mov_b64 s[36:37], s[26:27]\n"
    "s_and_b32 s37, s37, 0xffff\n"
    "s_mov_b32 s38, 1966080\n"
    "s_mov_b32 s39, 0x20000\n"
    "s_mul_i32 s72, s90, 6\n"
    "s_sub_u32 s72, %[rk], s72\n"
    "s_lshl_b32 s72, s72, 2\n"
    "s_add_u32 s72, s72, s56\n"
    "s_mul_i32 s16, s72, 55040\n"
    "s_lshl_b32 s22, s56, 11\n"
    "s_add_u32 s22, s22, 49152\n"
    "s_lshl_b32 s23, s57, 12\n"
    "s_add_u32 s23, s23, 49152\n"
    "s_mov_b32 s81, 107\n"
    "s_add_u32 s80, s80, 4045\n"
    "s_memrealtime s[92:93]\n"
    "s_waitcnt lgkmcnt(0)\n"
    "s_sub_i32 s80, s80, s92\n"
    "s_mul_i32 s80, s80, 315\n"
    "s_ashr_i32 s80, s80, 8\n"
    "s_lshl_b32 s81, s81, 5\n"
    "s_max_i32 s81, s81, s80\n"
    "s_mov_b32 s82, 40894464\n"
    "v_readfirstlane_b32 s93, v245\n"
    "s_mov_b32 s83, 1024\n"
    "s_xor_b32 s93, s93, s79\n"
    "s_cmp_lt_u32 s93, s83\n"
    "s_cselect_b32 s93, s93, 0\n"
    "s_cselect_b32 s83, 0, s83\n"
    "s_add_u32 s81, s81, s93\n"
    "s_lshl_b32 s80, s92, 5\n"
    "s_mov_b32 s92, 0\n"
    "s_mov_b32 s70, 0\n"
    ".p2align 6\n"
    "LOOP_%=:\n"
    "s_cmp_gt_i32 s92, 448\n"
    "s_cbranch_scc1 SPIN0_%=\n"
    "BACK0_%=:\n"
    "v_mfma_f32_16x16x32_bf16 v[176:179], a[192:195], a[112:115], v[212:215]\n"
    "global_load_dword v233, v255, s[94:95]\n"
    "v_mfma_f32_16x16x32_bf16 v[176:179], a[196:199], a[116:119], v[176:179]\n"
    "v_exp_f32 v160, v160\n"
    "v_mfma_f32_16x16x32_bf16 v[176:179], a[200:203], a[120:123], v[176:179]\n"
    "v_exp_f32 v161, v161\n"
    "v_mfma_f32_16x16x32_bf16 v[176:179], a[204:207], a[124:127], v[176:179]\n"
    "s_add_u32 m0, s91, 32768\n"
    "v_cvt_pk_bf16_f32 v192, v160, v161\n"
    "v_mfma_f32_16x16x32_bf16 v[180:183], a[220:223], a[124:127], v[212:215]\n"
    "v_exp_f32 v162, v162\n"
    "v_mfma_f32_16x16x32_bf16 v[180:183], a[216:219], a[120:123], v[180:183]\n"
    "buffer_load_dwordx4 v253, s[48:51], s60 offen offset:0 lds\n"
    "v_mfma_f32_16x16x32_bf16 v[180:183], a[212:215], a[116:119], v[180:183]\n"
    "v_exp_f32 v163, v163\n"
    "v_mfma_f32_16x16x32_bf16 v[180:183], a[208:211], a[112:115], v[180:183]\n"
    "v_exp_f32 v164, v164\n"
    "v_mfma_f32_16x16x32_bf16 v[184:187], a[224:227], a[112:115], v[212:215]\n"
    "v_exp_f32 v165, v165\n"
    "v_mfma_f32_16x16x32_bf16 v[184:187], a[228:231], a[116:119], v[184:187]\n"
    "v_cvt_pk_bf16_f32 v193, v162, v163\n"
    "v_cvt_pk_bf16_f32 v194, v164, v165\n"
    "v_mfma_f32_16x16x32_bf16 v[184:187], a[232:235], a[120:123], v[184:187]\n"
    "v_exp_f32 v166, v166\n"
    "v_mfma_f32_16x16x32_bf16 v[184:187], a[236:239], a[124:127], v[184:187]\n"
    "v_exp_f32 v167, v167\n"
    "v_mfma_f32_16x16x32_bf16 v[188:191], a[252:255], a[124:127], v[212:215]\n"
    "v_exp_f32 v168, v168\n"
    "v_mfma_f32_16x16x32_bf16 v[188:191], a[248:251], a[120:123], v[188:191]\n"
    "v_exp_f32 v169, v169\n"
    "v_mfma_f32_16x16x32_bf16 v[188:191], a[244:247], a[116:119], v[188:191]\n"
    "v_cvt_pk_bf16_f32 v195, v166, v167\n"
    "v_cvt_pk_bf16_f32 v196, v168, v169\n"
    "v_mfma_f32_16x16x32_bf16 v[188:191], a[240:243], a[112:115], v[188:191]\n"
    "s_waitcnt vmcnt(10)\n"
    "s_barrier\n"
    "v_mfma_f32_16x16x32_bf16 v[80:83], v[200:203], v[96:99], v[80:83]\n"
    "ds_read_b64_tr_b16 v[96:97], v251 offset:0\n"
    "ds_read_b64_tr_b16 v[98:99], v251 offset:4096\n"
    "v_mfma_f32_16x16x32_bf16 v[80:83], v[204:207], v[100:103], v[80:83]\n"
    "buffer_load_dwordx4 v254, s[48:51], s60 offen offset:1024 nt lds\n"
    "ds_read_b64_tr_b16 v[100:101], v251 offset:8192\n"
    "v_mfma_f32_16x16x32_bf16 v[84:87], v[200:203], v[104:107], v[84:87]\n"
    "ds_read_b64_tr_b16 v[102:103], v251 offset:12288\n"
    "ds_read_b64_tr_b16 v[104:105], v252 offset:0\n"
    "v_mfma_f32_16x16x32_bf16 v[84:87], v[204:207], v[108:111], v[84:87]\n"
    "ds_read_b64_tr_b16 v[106:107], v252 offset:4096\n"
    "ds_read_b64_tr_b16 v[108:109], v252 offset:8192\n"
    "v_mfma_f32_16x16x32_bf16 v[88:91], v[200:203], v[112:115], v[88:91]\n"
    "ds_read_b64_tr_b16 v[110:111], v252 offset:12288\n"
    "v_mfma_f32_16x16x32_bf16 v[88:91], v[204:207], v[116:119], v[88:91]\n"
    "ds_read_b64_tr_b16 v[112:113], v251 offset:256\n"
    "ds_read_b64_tr_b16 v[114:115], v251 offset:4352\n"
    "v_mfma_f32_16x16x32_bf16 v[92:95], v[200:203], v[120:123], v[92:95]\n"
    "ds_read_b64_tr_b16 v[116:117], v251 offset:8448\n"
    "ds_read_b64_tr_b16 v[118:119], v251 offset:12544\n"
    "v_mfma_f32_16x16x32_bf16 v[92:95], v[204:207], v[124:127], v[92:95]\n"
    "ds_read_b64_tr_b16 v[120:121], v252 offset:256\n"
    "v_mfma_f32_16x16x32_bf16 a[80:83], v[200:203], v[128:131], a[80:83]\n"
    "ds_read_b64_tr_b16 v[122:123], v252 offset:4352\n"
    "ds_read_b64_tr_b16 v[124:125], v252 offset:8448\n"
    "v_mfma_f32_16x16x32_bf16 a[80:83], v[204:207], v[132:135], a[80:83]\n"
    "ds_read_b64_tr_b16 v[126:127], v252 offset:12544\n"
    "ds_read_b64_tr_b16 v[128:129], v251 offset:512\n"
    "v_mfma_f32_16x16x32_bf16 a[84:87], v[200:203], v[136:139], a[84:87]\n"
    "ds_read_b64_tr_b16 v[130:131], v251 offset:4608\n"
    "v_mfma_f32_16x16x32_bf16 a[84:87], v[204:207], v[140:143], a[84:87]\n"
    "ds_read_b64_tr_b16 v[132:133], v251 offset:8704\n"
    "ds_read_b64_tr_b16 v[134:135], v251 offset:12800\n"
    "v_mfma_f32_16x16x32_bf16 a[88:91], v[200:203], v[144:147], a[88:91]\n"
    "ds_read_b64_tr_b16 v[136:137], v252 offset:512\n"
    "ds_read_b64_tr_b16 v[138:139], v252 offset:4608\n"
    "v_mfma_f32_16x16x32_bf16 a[88:91], v[204:207], v[148:151], a[88:91]\n"
    "ds_read_b64_tr_b16 v[140:141], v252 offset:8704\n"
    "v_mfma_f32_16x16x32_bf16 a[92:95], v[200:203], v[152:155], a[92:95]\n"
    "buffer_load_dwordx4 v253, s[48:51], s60 offen offset:2048 nt lds\n"
    "ds_read_b64_tr_b16 v[142:143], v252 offset:12800\n"
    "v_mfma_f32_16x16x32_bf16 a[92:95], v[204:207], v[156:159], a[92:95]\n"
    "ds_read_b64_tr_b16 v[144:145], v251 offset:768\n"
    "ds_read_b64_tr_b16 v[146:147], v251 offset:4864\n"
    "v_mfma_f32_16x16x32_bf16 v[242:245], v[246:249], v[200:203], v[242:245]\n"
    "ds_read_b64_tr_b16 v[148:149], v251 offset:8960\n"
    "ds_read_b64_tr_b16 v[150:151], v251 offset:13056\n"
    "v_mfma_f32_16x16x32_bf16 v[242:245], v[246:249], v[204:207], v[242:245]\n"
    "ds_read_b64_tr_b16 v[152:153], v252 offset:768\n"
    "v_xor_b32 v251, 0x10000, v251\n"
    "v_mfma_f32_16x16x32_bf16 v[160:163], a[192:195], a[128:131], v[216:219]\n"
    "ds_read_b64_tr_b16 v[154:155], v252 offset:4864\n"
    "ds_read_b64_tr_b16 v[156:157], v252 offset:8960\n"
    "v_mfma_f32_16x16x32_bf16 v[160:163], a[196:199], a[132:135], v[160:163]\n"
    "v_exp_f32 v170, v170\n"
    "v_mfma_f32_16x16x32_bf16 v[160:163], a[200:203], a[136:139], v[160:163]\n"
    "v_exp_f32 v171, v171\n"
    "v_mfma_f32_16x16x32_bf16 v[160:163], a[204:207], a[140:143], v[160:163]\n"
    "ds_read_b64_tr_b16 v[158:159], v252 offset:13056\n"
    "v_cvt_pk_bf16_f32 v197, v170, v171\n"
    "v_mfma_f32_16x16x32_bf16 v[164:167], a[220:223], a[140:143], v[216:219]\n"
    "v_exp_f32 v172, v172\n"
    "v_mfma_f32_16x16x32_bf16 v[164:167], a[216:219], a[136:139], v[164:167]\n"
    "v_exp_f32 v173, v173\n"
    "v_mfma_f32_16x16x32_bf16 v[164:167], a[212:215], a[132:135], v[164:167]\n"
    "buffer_load_dword v245, off, s[52:55], s82 offset:1024 sc1\n"
    "v_cvt_pk_bf16_f32 v198, v172, v173\n"
    "v_mfma_f32_16x16x32_bf16 v[164:167], a[208:211], a[128:131], v[164:167]\n"
    "v_exp_f32 v174, v174\n"
    "v_mfma_f32_16x16x32_bf16 v[168:171], a[224:227], a[128:131], v[216:219]\n"
    "v_exp_f32 v175, v175\n"
    "v_mfma_f32_16x16x32_bf16 v[168:171], a[228:231], a[132:135], v[168:171]\n"
    "buffer_load_dwordx4 v254, s[48:51], s60 offen offset:3072 lds\n"
    "v_cvt_pk_bf16_f32 v199, v174, v175\n"
    "v_mfma_f32_16x16x32_bf16 v[168:171], a[232:235], a[136:139], v[168:171]\n"
    "v_exp_f32 v176, v176\n"
    "v_mfma_f32_16x16x32_bf16 v[168:171], a[236:239], a[140:143], v[168:171]\n"
    "v_exp_f32 v177, v177\n"
    "v_mfma_f32_16x16x32_bf16 v[172:175], a[252:255], a[140:143], v[216:219]\n"
    "v_xor_b32 v252, 0x10000, v252\n"
    "v_cvt_pk_bf16_f32 v200, v176, v177\n"
    "v_mfma_f32_16x16x32_bf16 v[172:175], a[248:251], a[136:139], v[172:175]\n"
    "v_exp_f32 v178, v178\n"
    "v_mfma_f32_16x16x32_bf16 v[172:175], a[244:247], a[132:135], v[172:175]\n"
    "v_exp_f32 v179, v179\n"
    "v_mfma_f32_16x16x32_bf16 v[172:175], a[240:243], a[128:131], v[172:175]\n"
    "v_exp_f32 v180, v180\n"
    "s_waitcnt lgkmcnt(0)\n"
    "v_mfma_f32_16x16x32_bf16 v[0:3], v[192:195], v[96:99], v[0:3]\n"
    "v_exp_f32 v181, v181\n"
    "v_mfma_f32_16x16x32_bf16 v[0:3], v[196:199], v[100:103], v[0:3]\n"
    "v_cvt_pk_bf16_f32 v201, v178, v179\n"
    "v_cvt_pk_bf16_f32 v202, v180, v181\n"
    "v_mfma_f32_16x16x32_bf16 v[4:7], v[192:195], v[104:107], v[4:7]\n"
    "v_exp_f32 v182, v182\n"
    "v_mfma_f32_16x16x32_bf16 v[4:7], v[196:199], v[108:111], v[4:7]\n"
    "s_add_u32 s74, s60, 4096\n"
    "v_mfma_f32_16x16x32_bf16 v[8:11], v[192:195], v[112:115], v[8:11]\n"
    "s_add_u32 m0, s91, 36864\n"
    "v_mfma_f32_16x16x32_bf16 v[8:11], v[196:199], v[116:119], v[8:11]\n"
    "v_exp_f32 v183, v183\n"
    "v_mfma_f32_16x16x32_bf16 v[12:15], v[192:195], v[120:123], v[12:15]\n"
    "buffer_load_dwordx4 v253, s[48:51], s74 offen offset:0 nt lds\n"
    "v_cvt_pk_bf16_f32 v203, v182, v183\n"
    "v_mfma_f32_16x16x32_bf16 v[12:15], v[196:199], v[124:127], v[12:15]\n"
    "v_exp_f32 v184, v184\n"
    "v_mfma_f32_16x16x32_bf16 a[0:3], v[192:195], v[128:131], a[0:3]\n"
    "v_exp_f32 v185, v185\n"
    "v_mfma_f32_16x16x32_bf16 a[0:3], v[196:199], v[132:135], a[0:3]\n"
    "v_exp_f32 v186, v186\n"
    "v_mfma_f32_16x16x32_bf16 a[4:7], v[192:195], v[136:139], a[4:7]\n"
    "v_exp_f32 v187, v187\n"
    "v_mfma_f32_16x16x32_bf16 a[4:7], v[196:199], v[140:143], a[4:7]\n"
    "v_cvt_pk_bf16_f32 v204, v184, v185\n"
    "v_cvt_pk_bf16_f32 v205, v186, v187\n"
    "v_mfma_f32_16x16x32_bf16 a[8:11], v[192:195], v[144:147], a[8:11]\n"
    "s_lshl_b32 s93, s70, 1\n"
    "v_mfma_f32_16x16x32_bf16 a[8:11], v[196:199], v[148:151], a[8:11]\n"
    "s_sub_i32 s93, 1920, s93\n"
    "v_mfma_f32_16x16x32_bf16 a[12:15], v[192:195], v[152:155], a[12:15]\n"
    "s_add_u32 s59, s59, 1\n"
    "s_max_i32 s93, s93, 0\n"
    "v_mfma_f32_16x16x32_bf16 a[12:15], v[196:199], v[156:159], a[12:15]\n"
    "s_add_i32 s92, s92, s93\n"
    "v_mfma_f32_16x16x32_bf16 v[232:235], v[246:249], v[192:195], v[232:235]\n"
    "s_min_i32 s92, s92, 0\n"
    "v_mfma_f32_16x16x32_bf16 v[232:235], v[246:249], v[196:199], v[232:235]\n"
    "s_sub_u32 s80, s80, s92\n"
    "v_mfma_f32_16x16x32_bf16 v[176:179], a[192:195], a[144:147], v[220:223]\n"
    "s_lshr_b32 s93, s70, 2\n"
    "v_mfma_f32_16x16x32_bf16 v[176:179], a[196:199], a[148:151], v[176:179]\n"
    "buffer_load_dwordx4 v254, s[48:51], s74 offen offset:1024 nt lds\n"
    "s_sub_u32 s70, s70, s93\n"
    "v_mfma_f32_16x16x32_bf16 v[176:179], a[200:203], a[152:155], v[176:179]\n"
    "s_ashr_i32 s92, s92, 1\n"
    "v_mfma_f32_16x16x32_bf16 v[176:179], a[204:207], a[156:159], v[176:179]\n"
    "s_sub_u32 s70, s70, s92\n"
    "v_mfma_f32_16x16x32_bf16 v[180:183], a[220:223], a[156:159], v[220:223]\n"
    "s_min_u32 s70, s70, 2560\n"
    "v_mfma_f32_16x16x32_bf16 v[180:183], a[216:219], a[152:155], v[180:183]\n"
    "s_add_u32 s80, s80, s70\n"
    "v_mfma_f32_16x16x32_bf16 v[180:183], a[212:215], a[148:151], v[180:183]\n"
    "v_exp_f32 v188, v188\n"
    "v_mfma_f32_16x16x32_bf16 v[180:183], a[208:211], a[144:147], v[180:183]\n"
    "v_exp_f32 v189, v189\n"
    "v_mfma_f32_16x16x32_bf16 v[184:187], a[224:227], a[144:147], v[220:223]\n"
    "s_add_u32 s80, s80, s81\n"
    "v_cvt_pk_bf16_f32 v206, v188, v189\n"
    "v_mfma_f32_16x16x32_bf16 v[184:187], a[228:231], a[148:151], v[184:187]\n"
    "v_exp_f32 v190, v190\n"
    "v_mfma_f32_16x16x32_bf16 v[184:187], a[232:235], a[152:155], v[184:187]\n"
    "v_exp_f32 v191, v191\n"
    "v_mfma_f32_16x16x32_bf16 v[184:187], a[236:239], a[156:159], v[184:187]\n"
    "v_cvt_pk_bf16_f32 v207, v190, v191\n"
    "v_mfma_f32_16x16x32_bf16 v[188:191], a[252:255], a[156:159], v[220:223]\n"
    "v_exp_f32 v160, v160\n"
    "v_mfma_f32_16x16x32_bf16 v[188:191], a[248:251], a[152:155], v[188:191]\n"
    "v_exp_f32 v161, v161\n"
    "v_mfma_f32_16x16x32_bf16 v[188:191], a[244:247], a[148:151], v[188:191]\n"
    "buffer_load_dwordx4 v253, s[48:51], s74 offen offset:2048 lds\n"
    "v_cvt_pk_bf16_f32 v192, v160, v161\n"
    "v_mfma_f32_16x16x32_bf16 v[188:191], a[240:243], a[144:147], v[188:191]\n"
    "v_exp_f32 v162, v162\n"
    "v_mfma_f32_16x16x32_bf16 v[16:19], v[200:203], v[96:99], v[16:19]\n"
    "v_exp_f32 v163, v163\n"
    "v_mfma_f32_16x16x32_bf16 v[16:19], v[204:207], v[100:103], v[16:19]\n"
    "v_exp_f32 v164, v164\n"
    "v_mfma_f32_16x16x32_bf16 v[20:23], v[200:203], v[104:107], v[20:23]\n"
    "v_exp_f32 v165, v165\n"
    "v_mfma_f32_16x16x32_bf16 v[20:23], v[204:207], v[108:111], v[20:23]\n"
    "s_memrealtime s[92:93]\n"
    "v_cvt_pk_bf16_f32 v193, v162, v163\n"
    "v_mfma_f32_16x16x32_bf16 v[24:27], v[200:203], v[112:115], v[24:27]\n"
    "v_exp_f32 v166, v166\n"
    "v_mfma_f32_16x16x32_bf16 v[24:27], v[204:207], v[116:119], v[24:27]\n"
    "v_exp_f32 v167, v167\n"
    "v_mfma_f32_16x16x32_bf16 v[28:31], v[200:203], v[120:123], v[28:31]\n"
    "v_cvt_pk_bf16_f32 v194, v164, v165\n"
    "v_cvt_pk_bf16_f32 v195, v166, v167\n"
    "v_mfma_f32_16x16x32_bf16 v[28:31], v[204:207], v[124:127], v[28:31]\n"
    "v_exp_f32 v168, v168\n"
    "v_mfma_f32_16x16x32_bf16 a[16:19], v[200:203], v[128:131], a[16:19]\n"
    "v_exp_f32 v169, v169\n"
    "v_mfma_f32_16x16x32_bf16 a[16:19], v[204:207], v[132:135], a[16:19]\n"
    "v_exp_f32 v170, v170\n"
    "v_mfma_f32_16x16x32_bf16 a[20:23], v[200:203], v[136:139], a[20:23]\n"
    "v_exp_f32 v171, v171\n"
    "v_mfma_f32_16x16x32_bf16 a[20:23], v[204:207], v[140:143], a[20:23]\n"
    "buffer_load_dwordx4 v254, s[48:51], s74 offen offset:3072 nt lds\n"
    "v_cvt_pk_bf16_f32 v196, v168, v169\n"
    "v_mfma_f32_16x16x32_bf16 a[24:27], v[200:203], v[144:147], a[24:27]\n"
    "v_exp_f32 v172, v172\n"
    "v_mfma_f32_16x16x32_bf16 a[24:27], v[204:207], v[148:151], a[24:27]\n"
    "v_exp_f32 v173, v173\n"
    "v_mfma_f32_16x16x32_bf16 a[28:31], v[200:203], v[152:155], a[28:31]\n"
    "s_waitcnt lgkmcnt(0)\n"
    "s_lshl_b32 s92, s92, 5\n"
    "v_mfma_f32_16x16x32_bf16 a[28:31], v[204:207], v[156:159], a[28:31]\n"
    "s_sub_u32 s92, s80, s92\n"
    "v_cvt_pk_bf16_f32 v197, v170, v171\n"
    "v_mfma_f32_16x16x32_bf16 v[234:237], v[246:249], v[200:203], v[234:237]\n"
    "v_exp_f32 v174, v174\n"
    "v_mfma_f32_16x16x32_bf16 v[234:237], v[246:249], v[204:207], v[234:237]\n"
    "v_cvt_pk_bf16_f32 v198, v172, v173\n"
    "v_exp_f32 v175, v175\n"
    "s_nop 0\n"
    "v_cvt_pk_bf16_f32 v199, v174, v175\n"
    "s_cmp_gt_i32 s92, 448\n"
    "s_cbranch_scc1 SPIN1_%=\n"
    "BACK1_%=:\n"
    "v_mfma_f32_16x16x32_bf16 v[160:163], a[192:195], a[160:163], v[224:227]\n"
    "v_exp_f32 v176, v176\n"
    "v_mfma_f32_16x16x32_bf16 v[160:163], a[196:199], a[164:167], v[160:163]\n"
    "v_exp_f32 v177, v177\n"
    "v_mfma_f32_16x16x32_bf16 v[160:163], a[200:203], a[168:171], v[160:163]\n"
    "s_xor_b32 s73, s91, 0x10000\n"
    "v_cvt_pk_bf16_f32 v200, v176, v177\n"
    "v_mfma_f32_16x16x32_bf16 v[160:163], a[204:207], a[172:175], v[160:163]\n"
    "s_add_u32 s74, s60, 32768\n"
    "v_mfma_f32_16x16x32_bf16 v[164:167], a[220:223], a[172:175], v[224:227]\n"
    "s_add_u32 m0, s73, 0\n"
    "v_mfma_f32_16x16x32_bf16 v[164:167], a[216:219], a[168:171], v[164:167]\n"
    "v_exp_f32 v178, v178\n"
    "v_mfma_f32_16x16x32_bf16 v[164:167], a[212:215], a[164:167], v[164:167]\n"
    "buffer_load_dwordx4 v253, s[44:47], s74 offen offset:0 lds\n"
    "v_mfma_f32_16x16x32_bf16 v[164:167], a[208:211], a[160:163], v[164:167]\n"
    "v_exp_f32 v179, v179\n"
    "v_mfma_f32_16x16x32_bf16 v[168:171], a[224:227], a[160:163], v[224:227]\n"
    "s_lshl_b32 s93, s70, 1\n"
    "v_cvt_pk_bf16_f32 v201, v178, v179\n"
    "v_mfma_f32_16x16x32_bf16 v[168:171], a[228:231], a[164:167], v[168:171]\n"
    "s_sub_i32 s93, 1920, s93\n"
    "v_mfma_f32_16x16x32_bf16 v[168:171], a[232:235], a[168:171], v[168:171]\n"
    "s_max_i32 s93, s93, 0\n"
    "v_mfma_f32_16x16x32_bf16 v[168:171], a[236:239], a[172:175], v[168:171]\n"
    "s_add_i32 s92, s92, s93\n"
    "v_mfma_f32_16x16x32_bf16 v[172:175], a[252:255], a[172:175], v[224:227]\n"
    "s_min_i32 s92, s92, 0\n"
    "v_mfma_f32_16x16x32_bf16 v[172:175], a[248:251], a[168:171], v[172:175]\n"
    "s_sub_u32 s80, s80, s92\n"
    "v_mfma_f32_16x16x32_bf16 v[172:175], a[244:247], a[164:167], v[172:175]\n"
    "s_lshr_b32 s93, s70, 2\n"
    "v_mfma_f32_16x16x32_bf16 v[172:175], a[240:243], a[160:163], v[172:175]\n"
    "s_sub_u32 s70, s70, s93\n"
    "v_mfma_f32_16x16x32_bf16 v[32:35], v[192:195], v[96:99], v[32:35]\n"
    "s_ashr_i32 s92, s92, 1\n"
    "v_mfma_f32_16x16x32_bf16 v[32:35], v[196:199], v[100:103], v[32:35]\n"
    "s_sub_u32 s70, s70, s92\n"
    "v_mfma_f32_16x16x32_bf16 v[36:39], v[192:195], v[104:107], v[36:39]\n"
    "buffer_load_dwordx4 v254, s[44:47], s74 offen offset:1024 nt lds\n"
    "s_min_u32 s70, s70, 2560\n"
    "v_mfma_f32_16x16x32_bf16 v[36:39], v[196:199], v[108:111], v[36:39]\n"
    "s_add_u32 s80, s80, s70\n"
    "v_mfma_f32_16x16x32_bf16 v[40:43], v[192:195], v[112:115], v[40:43]\n"
    "v_exp_f32 v180, v180\n"
    "v_mfma_f32_16x16x32_bf16 v[40:43], v[196:199], v[116:119], v[40:43]\n"
    "v_exp_f32 v181, v181\n"
    "v_mfma_f32_16x16x32_bf16 v[44:47], v[192:195], v[120:123], v[44:47]\n"
    "v_exp_f32 v182, v182\n"
    "v_mfma_f32_16x16x32_bf16 v[44:47], v[196:199], v[124:127], v[44:47]\n"
    "v_exp_f32 v183, v183\n"
    "v_mfma_f32_16x16x32_bf16 a[32:35], v[192:195], v[128:131], a[32:35]\n"
    "v_cvt_pk_bf16_f32 v202, v180, v181\n"
    "v_cvt_pk_bf16_f32 v203, v182, v183\n"
    "v_mfma_f32_16x16x32_bf16 a[32:35], v[196:199], v[132:135], a[32:35]\n"
    "v_exp_f32 v184, v184\n"
    "v_mfma_f32_16x16x32_bf16 a[36:39], v[192:195], v[136:139], a[36:39]\n"
    "v_exp_f32 v185, v185\n"
    "v_mfma_f32_16x16x32_bf16 a[36:39], v[196:199], v[140:143], a[36:39]\n"
    "v_exp_f32 v186, v186\n"
    "v_mfma_f32_16x16x32_bf16 a[40:43], v[192:195], v[144:147], a[40:43]\n"
    "v_exp_f32 v187, v187\n"
    "v_mfma_f32_16x16x32_bf16 a[40:43], v[196:199], v[148:151], a[40:43]\n"
    "v_cvt_pk_bf16_f32 v204, v184, v185\n"
    "v_cvt_pk_bf16_f32 v205, v186, v187\n"
    "v_mfma_f32_16x16x32_bf16 a[44:47], v[192:195], v[152:155], a[44:47]\n"
    "v_exp_f32 v188, v188\n"
    "v_mfma_f32_16x16x32_bf16 a[44:47], v[196:199], v[156:159], a[44:47]\n"
    "buffer_load_dwordx4 v253, s[44:47], s74 offen offset:2048 nt lds\n"
    "v_mfma_f32_16x16x32_bf16 v[236:239], v[246:249], v[192:195], v[236:239]\n"
    "v_exp_f32 v189, v189\n"
    "v_mfma_f32_16x16x32_bf16 v[236:239], v[246:249], v[196:199], v[236:239]\n"
    "s_waitcnt vmcnt(13)\n"
    "s_barrier\n"
    "v_mfma_f32_16x16x32_bf16 v[176:179], a[192:195], a[176:179], v[228:231]\n"
    "ds_read_b128 a[192:195], v250 offset:0\n"
    "v_cvt_pk_bf16_f32 v206, v188, v189\n"
    "v_mfma_f32_16x16x32_bf16 v[176:179], a[196:199], a[180:183], v[176:179]\n"
    "v_exp_f32 v190, v190\n"
    "v_mfma_f32_16x16x32_bf16 v[176:179], a[200:203], a[184:187], v[176:179]\n"
    "ds_read_b128 a[196:199], v250 offset:256\n"
    "v_mfma_f32_16x16x32_bf16 v[176:179], a[204:207], a[188:191], v[176:179]\n"
    "ds_read_b128 a[200:203], v250 offset:512\n"
    "v_mfma_f32_16x16x32_bf16 v[180:183], a[220:223], a[188:191], v[228:231]\n"
    "ds_read_b128 a[204:207], v250 offset:768\n"
    "v_mfma_f32_16x16x32_bf16 v[180:183], a[216:219], a[184:187], v[180:183]\n"
    "ds_read_b128 a[220:223], v250 offset:4864\n"
    "v_mfma_f32_16x16x32_bf16 v[180:183], a[212:215], a[180:183], v[180:183]\n"
    "ds_read_b128 a[216:219], v250 offset:4608\n"
    "v_mfma_f32_16x16x32_bf16 v[180:183], a[208:211], a[176:179], v[180:183]\n"
    "ds_read_b128 a[212:215], v250 offset:4352\n"
    "v_mfma_f32_16x16x32_bf16 v[184:187], a[224:227], a[176:179], v[228:231]\n"
    "ds_read_b128 a[208:211], v250 offset:4096\n"
    "v_mfma_f32_16x16x32_bf16 v[184:187], a[228:231], a[180:183], v[184:187]\n"
    "ds_read_b128 a[224:227], v250 offset:8192\n"
    "v_mfma_f32_16x16x32_bf16 v[184:187], a[232:235], a[184:187], v[184:187]\n"
    "buffer_load_dwordx4 v254, s[44:47], s74 offen offset:3072 lds\n"
    "ds_read_b128 a[228:231], v250 offset:8448\n"
    "v_mfma_f32_16x16x32_bf16 v[184:187], a[236:239], a[188:191], v[184:187]\n"
    "ds_read_b128 a[232:235], v250 offset:8704\n"
    "v_exp_f32 v191, v191\n"
    "v_exp_f32 v160, v160\n"
    "ds_read_b128 a[236:239], v250 offset:8960\n"
    "v_cvt_pk_bf16_f32 v207, v190, v191\n"
    "v_mfma_f32_16x16x32_bf16 v[188:191], a[252:255], a[188:191], v[228:231]\n"
    "v_exp_f32 v161, v161\n"
    "v_mfma_f32_16x16x32_bf16 v[188:191], a[248:251], a[184:187], v[188:191]\n"
    "ds_read_b128 a[252:255], v250 offset:13056\n"
    "v_cvt_pk_bf16_f32 v192, v160, v161\n"
    "v_mfma_f32_16x16x32_bf16 v[188:191], a[244:247], a[180:183], v[188:191]\n"
    "ds_read_b128 a[248:251], v250 offset:12800\n"
    "v_mfma_f32_16x16x32_bf16 v[188:191], a[240:243], a[176:179], v[188:191]\n"
    "ds_read_b128 a[244:247], v250 offset:12544\n"
    "v_mfma_f32_16x16x32_bf16 v[48:51], v[200:203], v[96:99], v[48:51]\n"
    "v_exp_f32 v162, v162\n"
    "v_mfma_f32_16x16x32_bf16 v[48:51], v[204:207], v[100:103], v[48:51]\n"
    "v_exp_f32 v163, v163\n"
    "v_mfma_f32_16x16x32_bf16 v[52:55], v[200:203], v[104:107], v[52:55]\n"
    "s_xor_b32 s73, s91, 0x10000\n"
    "ds_read_b128 a[240:243], v250 offset:12288\n"
    "v_mfma_f32_16x16x32_bf16 v[52:55], v[204:207], v[108:111], v[52:55]\n"
    "s_add_u32 s74, s60, 36864\n"
    "v_xor_b32 v250, 0x10000, v250\n"
    "v_mfma_f32_16x16x32_bf16 v[56:59], v[200:203], v[112:115], v[56:59]\n"
    "s_add_u32 m0, s73, 4096\n"
    "v_cvt_pk_bf16_f32 v193, v162, v163\n"
    "v_mfma_f32_16x16x32_bf16 v[56:59], v[204:207], v[116:119], v[56:59]\n"
    "v_exp_f32 v164, v164\n"
    "v_mfma_f32_16x16x32_bf16 v[60:63], v[200:203], v[120:123], v[60:63]\n"
    "buffer_load_dwordx4 v253, s[44:47], s74 offen offset:0 nt lds\n"
    "v_mfma_f32_16x16x32_bf16 v[60:63], v[204:207], v[124:127], v[60:63]\n"
    "v_exp_f32 v165, v165\n"
    "v_mfma_f32_16x16x32_bf16 a[48:51], v[200:203], v[128:131], a[48:51]\n"
    "v_exp_f32 v166, v166\n"
    "v_mfma_f32_16x16x32_bf16 a[48:51], v[204:207], v[132:135], a[48:51]\n"
    "v_exp_f32 v167, v167\n"
    "v_mfma_f32_16x16x32_bf16 a[52:55], v[200:203], v[136:139], a[52:55]\n"
    "v_cvt_pk_bf16_f32 v194, v164, v165\n"
    "v_cvt_pk_bf16_f32 v195, v166, v167\n"
    "v_mfma_f32_16x16x32_bf16 a[52:55], v[204:207], v[140:143], a[52:55]\n"
    "v_exp_f32 v168, v168\n"
    "v_mfma_f32_16x16x32_bf16 a[56:59], v[200:203], v[144:147], a[56:59]\n"
    "v_exp_f32 v169, v169\n"
    "v_mfma_f32_16x16x32_bf16 a[56:59], v[204:207], v[148:151], a[56:59]\n"
    "v_exp_f32 v170, v170\n"
    "v_mfma_f32_16x16x32_bf16 a[60:63], v[200:203], v[152:155], a[60:63]\n"
    "v_exp_f32 v171, v171\n"
    "v_mfma_f32_16x16x32_bf16 a[60:63], v[204:207], v[156:159], a[60:63]\n"
    "v_cvt_pk_bf16_f32 v196, v168, v169\n"
    "v_cvt_pk_bf16_f32 v197, v170, v171\n"
    "v_mfma_f32_16x16x32_bf16 v[238:241], v[246:249], v[200:203], v[238:241]\n"
    "v_exp_f32 v172, v172\n"
    "v_mfma_f32_16x16x32_bf16 v[238:241], v[246:249], v[204:207], v[238:241]\n"
    "v_exp_f32 v173, v173\n"
    "s_waitcnt lgkmcnt(0)\n"
    "v_mfma_f32_16x16x32_bf16 v[160:163], a[192:195], a[96:99], v[208:211]\n"
    "v_exp_f32 v174, v174\n"
    "v_mfma_f32_16x16x32_bf16 v[160:163], a[196:199], a[100:103], v[160:163]\n"
    "buffer_load_dwordx4 v254, s[44:47], s74 offen offset:1024 nt lds\n"
    "v_cvt_pk_bf16_f32 v198, v172, v173\n"
    "v_mfma_f32_16x16x32_bf16 v[160:163], a[200:203], a[104:107], v[160:163]\n"
    "v_readfirstlane_b32 s93, v245\n"
    "v_mfma_f32_16x16x32_bf16 v[160:163], a[204:207], a[108:111], v[160:163]\n"
    "v_exp_f32 v175, v175\n"
    "v_mfma_f32_16x16x32_bf16 v[164:167], a[220:223], a[108:111], v[208:211]\n"
    "s_xor_b32 s93, s93, s79\n"
    "v_cvt_pk_bf16_f32 v199, v174, v175\n"
    "v_mfma_f32_16x16x32_bf16 v[164:167], a[216:219], a[104:107], v[164:167]\n"
    "s_cmp_lt_u32 s93, s83\n"
    "s_cselect_b32 s93, s93, 0\n"
    "s_cselect_b32 s83, 0, s83\n"
    "v_mfma_f32_16x16x32_bf16 v[164:167], a[212:215], a[100:103], v[164:167]\n"
    "s_add_u32 s81, s81, s93\n"
    "v_mfma_f32_16x16x32_bf16 v[164:167], a[208:211], a[96:99], v[164:167]\n"
    "v_exp_f32 v176, v176\n"
    "v_mfma_f32_16x16x32_bf16 v[168:171], a[224:227], a[96:99], v[208:211]\n"
    "s_add_u32 s80, s80, s81\n"
    "v_mfma_f32_16x16x32_bf16 v[168:171], a[228:231], a[100:103], v[168:171]\n"
    "v_exp_f32 v177, v177\n"
    "v_mfma_f32_16x16x32_bf16 v[168:171], a[232:235], a[104:107], v[168:171]\n"
    "v_exp_f32 v178, v178\n"
    "v_mfma_f32_16x16x32_bf16 v[168:171], a[236:239], a[108:111], v[168:171]\n"
    "v_exp_f32 v179, v179\n"
    "v_mfma_f32_16x16x32_bf16 v[172:175], a[252:255], a[108:111], v[208:211]\n"
    "v_cvt_pk_bf16_f32 v200, v176, v177\n"
    "v_cvt_pk_bf16_f32 v201, v178, v179\n"
    "v_mfma_f32_16x16x32_bf16 v[172:175], a[248:251], a[104:107], v[172:175]\n"
    "v_exp_f32 v180, v180\n"
    "v_mfma_f32_16x16x32_bf16 v[172:175], a[244:247], a[100:103], v[172:175]\n"
    "buffer_load_dwordx4 v253, s[44:47], s74 offen offset:2048 lds\n"
    "v_mfma_f32_16x16x32_bf16 v[172:175], a[240:243], a[96:99], v[172:175]\n"
    "v_exp_f32 v181, v181\n"
    "v_mfma_f32_16x16x32_bf16 v[64:67], v[192:195], v[96:99], v[64:67]\n"
    "v_exp_f32 v182, v182\n"
    "v_mfma_f32_16x16x32_bf16 v[64:67], v[196:199], v[100:103], v[64:67]\n"
    "v_exp_f32 v183, v183\n"
    "v_mfma_f32_16x16x32_bf16 v[68:71], v[192:195], v[104:107], v[68:71]\n"
    "v_cvt_pk_bf16_f32 v202, v180, v181\n"
    "v_cvt_pk_bf16_f32 v203, v182, v183\n"
    "v_mfma_f32_16x16x32_bf16 v[68:71], v[196:199], v[108:111], v[68:71]\n"
    "s_memrealtime s[92:93]\n"
    "v_mfma_f32_16x16x32_bf16 v[72:75], v[192:195], v[112:115], v[72:75]\n"
    "v_exp_f32 v184, v184\n"
    "v_mfma_f32_16x16x32_bf16 v[72:75], v[196:199], v[116:119], v[72:75]\n"
    "v_exp_f32 v185, v185\n"
    "v_mfma_f32_16x16x32_bf16 v[76:79], v[192:195], v[120:123], v[76:79]\n"
    "v_exp_f32 v186, v186\n"
    "v_mfma_f32_16x16x32_bf16 v[76:79], v[196:199], v[124:127], v[76:79]\n"
    "v_exp_f32 v187, v187\n"
    "v_mfma_f32_16x16x32_bf16 a[64:67], v[192:195], v[128:131], a[64:67]\n"
    "v_cvt_pk_bf16_f32 v204, v184, v185\n"
    "v_cvt_pk_bf16_f32 v205, v186, v187\n"
    "v_mfma_f32_16x16x32_bf16 a[64:67], v[196:199], v[132:135], a[64:67]\n"
    "v_exp_f32 v188, v188\n"
    "v_mfma_f32_16x16x32_bf16 a[68:71], v[192:195], v[136:139], a[68:71]\n"
    "v_exp_f32 v189, v189\n"
    "v_mfma_f32_16x16x32_bf16 a[68:71], v[196:199], v[140:143], a[68:71]\n"
    "buffer_load_dwordx4 v254, s[44:47], s74 offen offset:3072 nt lds\n"
    "v_cvt_pk_bf16_f32 v206, v188, v189\n"
    "v_mfma_f32_16x16x32_bf16 a[72:75], v[192:195], v[144:147], a[72:75]\n"
    "v_exp_f32 v190, v190\n"
    "v_mfma_f32_16x16x32_bf16 a[72:75], v[196:199], v[148:151], a[72:75]\n"
    "v_exp_f32 v191, v191\n"
    "v_mfma_f32_16x16x32_bf16 a[76:79], v[192:195], v[152:155], a[76:79]\n"
    "s_waitcnt lgkmcnt(0)\n"
    "s_lshl_b32 s92, s92, 5\n"
    "v_mfma_f32_16x16x32_bf16 a[76:79], v[196:199], v[156:159], a[76:79]\n"
    "s_sub_u32 s92, s80, s92\n"
    "v_cvt_pk_bf16_f32 v207, v190, v191\n"
    "v_mfma_f32_16x16x32_bf16 v[240:243], v[246:249], v[192:195], v[240:243]\n"
    "s_xor_b32 s91, s91, 0x10000\n"
    "s_add_u32 s60, s60, 32768\n"
    "v_mfma_f32_16x16x32_bf16 v[240:243], v[246:249], v[196:199], v[240:243]\n"
    "s_cmp_lt_u32 s59, 13\n"
    "s_cbranch_scc1 LOOP_%=\n"
    "s_cmp_gt_i32 s92, 448\n"
    "s_cbranch_scc1 SPIN2_%=\n"
    "BACK2_%=:\n"
    "v_mfma_f32_16x16x32_bf16 v[176:179], a[192:195], a[112:115], v[212:215]\n"
    "v_exp_f32 v160, v160\n"
    "v_mfma_f32_16x16x32_bf16 v[176:179], a[196:199], a[116:119], v[176:179]\n"
    "v_exp_f32 v161, v161\n"
    "v_mfma_f32_16x16x32_bf16 v[176:179], a[200:203], a[120:123], v[176:179]\n"
    "v_exp_f32 v162, v162\n"
    "v_mfma_f32_16x16x32_bf16 v[176:179], a[204:207], a[124:127], v[176:179]\n"
    "s_add_u32 m0, s91, 32768\n"
    "v_cvt_pk_bf16_f32 v192, v160, v161\n"
    "v_mfma_f32_16x16x32_bf16 v[180:183], a[220:223], a[124:127], v[212:215]\n"
    "v_exp_f32 v163, v163\n"
    "v_mfma_f32_16x16x32_bf16 v[180:183], a[216:219], a[120:123], v[180:183]\n"
    "buffer_load_dwordx4 v253, s[48:51], s60 offen offset:0 nt lds\n"
    "v_cvt_pk_bf16_f32 v193, v162, v163\n"
    "v_mfma_f32_16x16x32_bf16 v[180:183], a[212:215], a[116:119], v[180:183]\n"
    "v_exp_f32 v164, v164\n"
    "v_mfma_f32_16x16x32_bf16 v[180:183], a[208:211], a[112:115], v[180:183]\n"
    "v_exp_f32 v165, v165\n"
    "v_mfma_f32_16x16x32_bf16 v[184:187], a[224:227], a[112:115], v[212:215]\n"
    "v_exp_f32 v166, v166\n"
    "v_mfma_f32_16x16x32_bf16 v[184:187], a[228:231], a[116:119], v[184:187]\n"
    "v_exp_f32 v167, v167\n"
    "v_mfma_f32_16x16x32_bf16 v[184:187], a[232:235], a[120:123], v[184:187]\n"
    "v_cvt_pk_bf16_f32 v194, v164, v165\n"
    "v_cvt_pk_bf16_f32 v195, v166, v167\n"
    "v_mfma_f32_16x16x32_bf16 v[184:187], a[236:239], a[124:127], v[184:187]\n"
    "v_exp_f32 v168, v168\n"
    "v_mfma_f32_16x16x32_bf16 v[188:191], a[252:255], a[124:127], v[212:215]\n"
    "v_exp_f32 v169, v169\n"
    "v_mfma_f32_16x16x32_bf16 v[188:191], a[248:251], a[120:123], v[188:191]\n"
    "v_exp_f32 v170, v170\n"
    "v_mfma_f32_16x16x32_bf16 v[188:191], a[244:247], a[116:119], v[188:191]\n"
    "v_exp_f32 v171, v171\n"
    "v_mfma_f32_16x16x32_bf16 v[188:191], a[240:243], a[112:115], v[188:191]\n"
    "s_waitcnt vmcnt(9)\n"
    "s_barrier\n"
    "v_mfma_f32_16x16x32_bf16 v[80:83], v[200:203], v[96:99], v[80:83]\n"
    "ds_read_b64_tr_b16 v[96:97], v251 offset:0\n"
    "ds_read_b64_tr_b16 v[98:99], v251 offset:4096\n"
    "v_mfma_f32_16x16x32_bf16 v[80:83], v[204:207], v[100:103], v[80:83]\n"
    "buffer_load_dwordx4 v254, s[48:51], s60 offen offset:1024 nt lds\n"
    "ds_read_b64_tr_b16 v[100:101], v251 offset:8192\n"
    "v_mfma_f32_16x16x32_bf16 v[84:87], v[200:203], v[104:107], v[84:87]\n"
    "ds_read_b64_tr_b16 v[102:103], v251 offset:12288\n"
    "ds_read_b64_tr_b16 v[104:105], v252 offset:0\n"
    "v_mfma_f32_16x16x32_bf16 v[84:87], v[204:207], v[108:111], v[84:87]\n"
    "ds_read_b64_tr_b16 v[106:107], v252 offset:4096\n"
    "ds_read_b64_tr_b16 v[108:109], v252 offset:8192\n"
    "v_mfma_f32_16x16x32_bf16 v[88:91], v[200:203], v[112:115], v[88:91]\n"
    "ds_read_b64_tr_b16 v[110:111], v252 offset:12288\n"
    "v_cvt_pk_bf16_f32 v196, v168, v169\n"
    "v_mfma_f32_16x16x32_bf16 v[88:91], v[204:207], v[116:119], v[88:91]\n"
    "ds_read_b64_tr_b16 v[112:113], v251 offset:256\n"
    "ds_read_b64_tr_b16 v[114:115], v251 offset:4352\n"
    "v_mfma_f32_16x16x32_bf16 v[92:95], v[200:203], v[120:123], v[92:95]\n"
    "ds_read_b64_tr_b16 v[116:117], v251 offset:8448\n"
    "ds_read_b64_tr_b16 v[118:119], v251 offset:12544\n"
    "v_mfma_f32_16x16x32_bf16 v[92:95], v[204:207], v[124:127], v[92:95]\n"
    "ds_read_b64_tr_b16 v[120:121], v252 offset:256\n"
    "v_cvt_pk_bf16_f32 v197, v170, v171\n"
    "v_mfma_f32_16x16x32_bf16 a[80:83], v[200:203], v[128:131], a[80:83]\n"
    "ds_read_b64_tr_b16 v[122:123], v252 offset:4352\n"
    "ds_read_b64_tr_b16 v[124:125], v252 offset:8448\n"
    "v_mfma_f32_16x16x32_bf16 a[80:83], v[204:207], v[132:135], a[80:83]\n"
    "ds_read_b64_tr_b16 v[126:127], v252 offset:12544\n"
    "ds_read_b64_tr_b16 v[128:129], v251 offset:512\n"
    "v_mfma_f32_16x16x32_bf16 a[84:87], v[200:203], v[136:139], a[84:87]\n"
    "ds_read_b64_tr_b16 v[130:131], v251 offset:4608\n"
    "v_mfma_f32_16x16x32_bf16 a[84:87], v[204:207], v[140:143], a[84:87]\n"
    "ds_read_b64_tr_b16 v[132:133], v251 offset:8704\n"
    "ds_read_b64_tr_b16 v[134:135], v251 offset:12800\n"
    "v_mfma_f32_16x16x32_bf16 a[88:91], v[200:203], v[144:147], a[88:91]\n"
    "ds_read_b64_tr_b16 v[136:137], v252 offset:512\n"
    "ds_read_b64_tr_b16 v[138:139], v252 offset:4608\n"
    "v_mfma_f32_16x16x32_bf16 a[88:91], v[204:207], v[148:151], a[88:91]\n"
    "ds_read_b64_tr_b16 v[140:141], v252 offset:8704\n"
    "v_mfma_f32_16x16x32_bf16 a[92:95], v[200:203], v[152:155], a[92:95]\n"
    "buffer_load_dwordx4 v253, s[48:51], s60 offen offset:2048 nt lds\n"
    "ds_read_b64_tr_b16 v[142:143], v252 offset:12800\n"
    "v_mfma_f32_16x16x32_bf16 a[92:95], v[204:207], v[156:159], a[92:95]\n"
    "ds_read_b64_tr_b16 v[144:145], v251 offset:768\n"
    "ds_read_b64_tr_b16 v[146:147], v251 offset:4864\n"
    "v_mfma_f32_16x16x32_bf16 v[242:245], v[246:249], v[200:203], v[242:245]\n"
    "ds_read_b64_tr_b16 v[148:149], v251 offset:8960\n"
    "ds_read_b64_tr_b16 v[150:151], v251 offset:13056\n"
    "v_mfma_f32_16x16x32_bf16 v[242:245], v[246:249], v[204:207], v[242:245]\n"
    "ds_read_b64_tr_b16 v[152:153], v252 offset:768\n"
    "v_xor_b32 v251, 0x10000, v251\n"
    "v_mfma_f32_16x16x32_bf16 v[160:163], a[192:195], a[128:131], v[216:219]\n"
    "ds_read_b64_tr_b16 v[154:155], v252 offset:4864\n"
    "ds_read_b64_tr_b16 v[156:157], v252 offset:8960\n"
    "v_mfma_f32_16x16x32_bf16 v[160:163], a[196:199], a[132:135], v[160:163]\n"
    "v_exp_f32 v172, v172\n"
    "v_mfma_f32_16x16x32_bf16 v[160:163], a[200:203], a[136:139], v[160:163]\n"
    "v_exp_f32 v173, v173\n"
    "v_mfma_f32_16x16x32_bf16 v[160:163], a[204:207], a[140:143], v[160:163]\n"
    "ds_read_b64_tr_b16 v[158:159], v252 offset:13056\n"
    "v_cvt_pk_bf16_f32 v198, v172, v173\n"
    "v_mfma_f32_16x16x32_bf16 v[164:167], a[220:223], a[140:143], v[216:219]\n"
    "v_exp_f32 v174, v174\n"
    "v_mfma_f32_16x16x32_bf16 v[164:167], a[216:219], a[136:139], v[164:167]\n"
    "v_exp_f32 v175, v175\n"
    "v_mfma_f32_16x16x32_bf16 v[164:167], a[212:215], a[132:135], v[164:167]\n"
    "v_cvt_pk_bf16_f32 v199, v174, v175\n"
    "v_xor_b32 v252, 0x10000, v252\n"
    "v_mfma_f32_16x16x32_bf16 v[164:167], a[208:211], a[128:131], v[164:167]\n"
    "v_exp_f32 v176, v176\n"
    "v_mfma_f32_16x16x32_bf16 v[168:171], a[224:227], a[128:131], v[216:219]\n"
    "v_exp_f32 v177, v177\n"
    "v_mfma_f32_16x16x32_bf16 v[168:171], a[228:231], a[132:135], v[168:171]\n"
    "buffer_load_dwordx4 v254, s[48:51], s60 offen offset:3072 nt lds\n"
    "v_cvt_pk_bf16_f32 v200, v176, v177\n"
    "v_mfma_f32_16x16x32_bf16 v[168:171], a[232:235], a[136:139], v[168:171]\n"
    "v_exp_f32 v178, v178\n"
    "v_mfma_f32_16x16x32_bf16 v[168:171], a[236:239], a[140:143], v[168:171]\n"
    "v_exp_f32 v179, v179\n"
    "v_mfma_f32_16x16x32_bf16 v[172:175], a[252:255], a[140:143], v[216:219]\n"
    "v_exp_f32 v180, v180\n"
    "v_mfma_f32_16x16x32_bf16 v[172:175], a[248:251], a[136:139], v[172:175]\n"
    "v_exp_f32 v181, v181\n"
    "v_mfma_f32_16x16x32_bf16 v[172:175], a[244:247], a[132:135], v[172:175]\n"
    "v_cvt_pk_bf16_f32 v201, v178, v179\n"
    "v_cvt_pk_bf16_f32 v202, v180, v181\n"
    "v_mfma_f32_16x16x32_bf16 v[172:175], a[240:243], a[128:131], v[172:175]\n"
    "v_exp_f32 v182, v182\n"
    "s_waitcnt lgkmcnt(0)\n"
    "v_mfma_f32_16x16x32_bf16 v[0:3], v[192:195], v[96:99], v[0:3]\n"
    "v_exp_f32 v183, v183\n"
    "v_mfma_f32_16x16x32_bf16 v[0:3], v[196:199], v[100:103], v[0:3]\n"
    "v_exp_f32 v184, v184\n"
    "v_mfma_f32_16x16x32_bf16 v[4:7], v[192:195], v[104:107], v[4:7]\n"
    "v_exp_f32 v185, v185\n"
    "v_mfma_f32_16x16x32_bf16 v[4:7], v[196:199], v[108:111], v[4:7]\n"
    "s_add_u32 s74, s60, 4096\n"
    "v_cvt_pk_bf16_f32 v203, v182, v183\n"
    "v_mfma_f32_16x16x32_bf16 v[8:11], v[192:195], v[112:115], v[8:11]\n"
    "s_add_u32 m0, s91, 36864\n"
    "v_cvt_pk_bf16_f32 v204, v184, v185\n"
    "v_mfma_f32_16x16x32_bf16 v[8:11], v[196:199], v[116:119], v[8:11]\n"
    "v_exp_f32 v186, v186\n"
    "v_mfma_f32_16x16x32_bf16 v[12:15], v[192:195], v[120:123], v[12:15]\n"
    "buffer_load_dwordx4 v253, s[48:51], s74 offen offset:0 nt lds\n"
    "v_mfma_f32_16x16x32_bf16 v[12:15], v[196:199], v[124:127], v[12:15]\n"
    "v_exp_f32 v187, v187\n"
    "v_mfma_f32_16x16x32_bf16 a[0:3], v[192:195], v[128:131], a[0:3]\n"
    "v_exp_f32 v188, v188\n"
    "v_mfma_f32_16x16x32_bf16 a[0:3], v[196:199], v[132:135], a[0:3]\n"
    "v_exp_f32 v189, v189\n"
    "v_mfma_f32_16x16x32_bf16 a[4:7], v[192:195], v[136:139], a[4:7]\n"
    "v_cvt_pk_bf16_f32 v205, v186, v187\n"
    "v_cvt_pk_bf16_f32 v206, v188, v189\n"
    "v_mfma_f32_16x16x32_bf16 a[4:7], v[196:199], v[140:143], a[4:7]\n"
    "v_exp_f32 v190, v190\n"
    "v_mfma_f32_16x16x32_bf16 a[8:11], v[192:195], v[144:147], a[8:11]\n"
    "v_exp_f32 v191, v191\n"
    "v_mfma_f32_16x16x32_bf16 a[8:11], v[196:199], v[148:151], a[8:11]\n"
    "v_exp_f32 v160, v160\n"
    "v_mfma_f32_16x16x32_bf16 a[12:15], v[192:195], v[152:155], a[12:15]\n"
    "v_exp_f32 v161, v161\n"
    "v_mfma_f32_16x16x32_bf16 a[12:15], v[196:199], v[156:159], a[12:15]\n"
    "v_exp_f32 v162, v162\n"
    "v_mfma_f32_16x16x32_bf16 v[232:235], v[246:249], v[192:195], v[232:235]\n"
    "v_cvt_pk_bf16_f32 v207, v190, v191\n"
    "v_cvt_pk_bf16_f32 v192, v160, v161\n"
    "v_mfma_f32_16x16x32_bf16 v[232:235], v[246:249], v[196:199], v[232:235]\n"
    "v_exp_f32 v163, v163\n"
    "v_mfma_f32_16x16x32_bf16 v[176:179], a[192:195], a[144:147], v[220:223]\n"
    "v_exp_f32 v164, v164\n"
    "v_mfma_f32_16x16x32_bf16 v[176:179], a[196:199], a[148:151], v[176:179]\n"
    "buffer_load_dwordx4 v254, s[48:51], s74 offen offset:1024 nt lds\n"
    "v_cvt_pk_bf16_f32 v193, v162, v163\n"
    "v_mfma_f32_16x16x32_bf16 v[176:179], a[200:203], a[152:155], v[176:179]\n"
    "v_exp_f32 v165, v165\n"
    "v_mfma_f32_16x16x32_bf16 v[176:179], a[204:207], a[156:159], v[176:179]\n"
    "v_exp_f32 v166, v166\n"
    "v_mfma_f32_16x16x32_bf16 v[180:183], a[220:223], a[156:159], v[220:223]\n"
    "v_exp_f32 v167, v167\n"
    "v_mfma_f32_16x16x32_bf16 v[180:183], a[216:219], a[152:155], v[180:183]\n"
    "v_cvt_pk_bf16_f32 v194, v164, v165\n"
    "v_cvt_pk_bf16_f32 v195, v166, v167\n"
    "v_mfma_f32_16x16x32_bf16 v[180:183], a[212:215], a[148:151], v[180:183]\n"
    "v_exp_f32 v168, v168\n"
    "v_mfma_f32_16x16x32_bf16 v[180:183], a[208:211], a[144:147], v[180:183]\n"
    "v_exp_f32 v169, v169\n"
    "v_mfma_f32_16x16x32_bf16 v[184:187], a[224:227], a[144:147], v[220:223]\n"
    "v_exp_f32 v170, v170\n"
    "v_mfma_f32_16x16x32_bf16 v[184:187], a[228:231], a[148:151], v[184:187]\n"
    "v_exp_f32 v171, v171\n"
    "v_mfma_f32_16x16x32_bf16 v[184:187], a[232:235], a[152:155], v[184:187]\n"
    "v_cvt_pk_bf16_f32 v196, v168, v169\n"
    "v_cvt_pk_bf16_f32 v197, v170, v171\n"
    "v_mfma_f32_16x16x32_bf16 v[184:187], a[236:239], a[156:159], v[184:187]\n"
    "v_exp_f32 v172, v172\n"
    "v_mfma_f32_16x16x32_bf16 v[188:191], a[252:255], a[156:159], v[220:223]\n"
    "v_exp_f32 v173, v173\n"
    "v_mfma_f32_16x16x32_bf16 v[188:191], a[248:251], a[152:155], v[188:191]\n"
    "v_exp_f32 v174, v174\n"
    "v_mfma_f32_16x16x32_bf16 v[188:191], a[244:247], a[148:151], v[188:191]\n"
    "buffer_load_dwordx4 v253, s[48:51], s74 offen offset:2048 nt lds\n"
    "v_cvt_pk_bf16_f32 v198, v172, v173\n"
    "v_mfma_f32_16x16x32_bf16 v[188:191], a[240:243], a[144:147], v[188:191]\n"
    "v_exp_f32 v175, v175\n"
    "v_mfma_f32_16x16x32_bf16 v[16:19], v[200:203], v[96:99], v[16:19]\n"
    "v_cvt_pk_bf16_f32 v199, v174, v175\n"
    "v_mfma_f32_16x16x32_bf16 v[16:19], v[204:207], v[100:103], v[16:19]\n"
    "v_mfma_f32_16x16x32_bf16 v[20:23], v[200:203], v[104:107], v[20:23]\n"
    "v_mfma_f32_16x16x32_bf16 v[20:23], v[204:207], v[108:111], v[20:23]\n"
    "v_mfma_f32_16x16x32_bf16 v[24:27], v[200:203], v[112:115], v[24:27]\n"
    "v_mfma_f32_16x16x32_bf16 v[24:27], v[204:207], v[116:119], v[24:27]\n"
    "v_mfma_f32_16x16x32_bf16 v[28:31], v[200:203], v[120:123], v[28:31]\n"
    "v_mfma_f32_16x16x32_bf16 v[28:31], v[204:207], v[124:127], v[28:31]\n"
    "v_mfma_f32_16x16x32_bf16 a[16:19], v[200:203], v[128:131], a[16:19]\n"
    "v_mfma_f32_16x16x32_bf16 a[16:19], v[204:207], v[132:135], a[16:19]\n"
    "v_mfma_f32_16x16x32_bf16 a[20:23], v[200:203], v[136:139], a[20:23]\n"
    "v_mfma_f32_16x16x32_bf16 a[20:23], v[204:207], v[140:143], a[20:23]\n"
    "buffer_load_dwordx4 v254, s[48:51], s74 offen offset:3072 nt lds\n"
    "v_mfma_f32_16x16x32_bf16 a[24:27], v[200:203], v[144:147], a[24:27]\n"
    "v_mfma_f32_16x16x32_bf16 a[24:27], v[204:207], v[148:151], a[24:27]\n"
    "v_mfma_f32_16x16x32_bf16 a[28:31], v[200:203], v[152:155], a[28:31]\n"
    "v_mfma_f32_16x16x32_bf16 a[28:31], v[204:207], v[156:159], a[28:31]\n"
    "v_mfma_f32_16x16x32_bf16 v[234:237], v[246:249], v[200:203], v[234:237]\n"
    "v_mfma_f32_16x16x32_bf16 v[234:237], v[246:249], v[204:207], v[234:237]\n"
    "v_mfma_f32_16x16x32_bf16 v[160:163], a[192:195], a[160:163], v[224:227]\n"
    "v_exp_f32 v176, v176\n"
    "v_mfma_f32_16x16x32_bf16 v[160:163], a[196:199], a[164:167], v[160:163]\n"
    "v_exp_f32 v177, v177\n"
    "v_mfma_f32_16x16x32_bf16 v[160:163], a[200:203], a[168:171], v[160:163]\n"
    "v_mbcnt_lo_u32_b32 v254, -1, 0\n"
    "v_cvt_pk_bf16_f32 v200, v176, v177\n"
    "v_mfma_f32_16x16x32_bf16 v[160:163], a[204:207], a[172:175], v[160:163]\n"
    "v_mbcnt_hi_u32_b32 v254, -1, v254\n"
    "v_mfma_f32_16x16x32_bf16 v[164:167], a[220:223], a[172:175], v[224:227]\n"
    "v_bfe_u32 v253, v254, 1, 4\n"
    "v_mfma_f32_16x16x32_bf16 v[164:167], a[216:219], a[168:171], v[164:167]\n"
    "v_mul_u32_u24 v253, 1280, v253\n"
    "v_mfma_f32_16x16x32_bf16 v[164:167], a[212:215], a[164:167], v[164:167]\n"
    "v_and_b32 v254, 1, v254\n"
    "v_mfma_f32_16x16x32_bf16 v[164:167], a[208:211], a[160:163], v[164:167]\n"
    "v_lshl_add_u32 v253, v254, 7, v253\n"
    "v_mfma_f32_16x16x32_bf16 v[168:171], a[224:227], a[160:163], v[224:227]\n"
    "v_add_u32 v253, s16, v253\n"
    "v_mfma_f32_16x16x32_bf16 v[168:171], a[228:231], a[164:167], v[168:171]\n"
    "s_sub_u32 s44, s52, s66\n"
    "s_subb_u32 s45, s53, 0\n"
    "v_mfma_f32_16x16x32_bf16 v[168:171], a[232:235], a[168:171], v[168:171]\n"
    "s_mov_b32 s46, 41943040\n"
    "v_mfma_f32_16x16x32_bf16 v[168:171], a[236:239], a[172:175], v[168:171]\n"
    "v_exp_f32 v178, v178\n"
    "v_mfma_f32_16x16x32_bf16 v[172:175], a[252:255], a[172:175], v[224:227]\n"
    "v_exp_f32 v179, v179\n"
    "v_mfma_f32_16x16x32_bf16 v[172:175], a[248:251], a[168:171], v[172:175]\n"
    "v_exp_f32 v180, v180\n"
    "v_mfma_f32_16x16x32_bf16 v[172:175], a[244:247], a[164:167], v[172:175]\n"
    "v_exp_f32 v181, v181\n"
    "v_mfma_f32_16x16x32_bf16 v[172:175], a[240:243], a[160:163], v[172:175]\n"
    "v_cvt_pk_bf16_f32 v201, v178, v179\n"
    "v_cvt_pk_bf16_f32 v202, v180, v181\n"
    "v_mfma_f32_16x16x32_bf16 v[32:35], v[192:195], v[96:99], v[32:35]\n"
    "v_exp_f32 v182, v182\n"
    "v_mfma_f32_16x16x32_bf16 v[32:35], v[196:199], v[100:103], v[32:35]\n"
    "v_exp_f32 v183, v183\n"
    "v_mfma_f32_16x16x32_bf16 v[36:39], v[192:195], v[104:107], v[36:39]\n"
    "v_exp_f32 v184, v184\n"
    "v_mfma_f32_16x16x32_bf16 v[36:39], v[196:199], v[108:111], v[36:39]\n"
    "v_exp_f32 v185, v185\n"
    "v_mfma_f32_16x16x32_bf16 v[40:43], v[192:195], v[112:115], v[40:43]\n"
    "v_cvt_pk_bf16_f32 v203, v182, v183\n"
    "v_cvt_pk_bf16_f32 v204, v184, v185\n"
    "v_mfma_f32_16x16x32_bf16 v[40:43], v[196:199], v[116:119], v[40:43]\n"
    "v_exp_f32 v186, v186\n"
    "v_mfma_f32_16x16x32_bf16 v[44:47], v[192:195], v[120:123], v[44:47]\n"
    "v_exp_f32 v187, v187\n"
    "v_mfma_f32_16x16x32_bf16 v[44:47], v[196:199], v[124:127], v[44:47]\n"
    "v_exp_f32 v188, v188\n"
    "v_mfma_f32_16x16x32_bf16 a[32:35], v[192:195], v[128:131], a[32:35]\n"
    "v_exp_f32 v189, v189\n"
    "v_mfma_f32_16x16x32_bf16 a[32:35], v[196:199], v[132:135], a[32:35]\n"
    "v_cvt_pk_bf16_f32 v205, v186, v187\n"
    "v_cvt_pk_bf16_f32 v206, v188, v189\n"
    "v_mfma_f32_16x16x32_bf16 a[36:39], v[192:195], v[136:139], a[36:39]\n"
    "v_exp_f32 v190, v190\n"
    "v_mfma_f32_16x16x32_bf16 a[36:39], v[196:199], v[140:143], a[36:39]\n"
    "v_exp_f32 v191, v191\n"
    "v_mfma_f32_16x16x32_bf16 a[40:43], v[192:195], v[144:147], a[40:43]\n"
    "v_exp_f32 v160, v160\n"
    "v_mfma_f32_16x16x32_bf16 a[40:43], v[196:199], v[148:151], a[40:43]\n"
    "v_exp_f32 v161, v161\n"
    "v_mfma_f32_16x16x32_bf16 a[44:47], v[192:195], v[152:155], a[44:47]\n"
    "v_exp_f32 v162, v162\n"
    "v_mfma_f32_16x16x32_bf16 a[44:47], v[196:199], v[156:159], a[44:47]\n"
    "v_exp_f32 v163, v163\n"
    "v_mfma_f32_16x16x32_bf16 v[236:239], v[246:249], v[192:195], v[236:239]\n"
    "v_cvt_pk_bf16_f32 v207, v190, v191\n"
    "v_cvt_pk_bf16_f32 v192, v160, v161\n"
    "v_mfma_f32_16x16x32_bf16 v[236:239], v[246:249], v[196:199], v[236:239]\n"
    "s_waitcnt vmcnt(8)\n"
    "s_barrier\n"
    "v_mfma_f32_16x16x32_bf16 v[176:179], a[192:195], a[176:179], v[228:231]\n"
    "ds_read_b128 a[192:195], v250 offset:0\n"
    "v_cvt_pk_bf16_f32 v193, v162, v163\n"
    "v_mfma_f32_16x16x32_bf16 v[176:179], a[196:199], a[180:183], v[176:179]\n"
    "v_exp_f32 v164, v164\n"
    "v_mfma_f32_16x16x32_bf16 v[176:179], a[200:203], a[184:187], v[176:179]\n"
    "ds_read_b128 a[196:199], v250 offset:256\n"
    "v_mfma_f32_16x16x32_bf16 v[176:179], a[204:207], a[188:191], v[176:179]\n"
    "ds_read_b128 a[200:203], v250 offset:512\n"
    "v_mfma_f32_16x16x32_bf16 v[180:183], a[220:223], a[188:191], v[228:231]\n"
    "buffer_load_dword v233, v253, s[84:87], 0 offen\n"
    "ds_read_b128 a[204:207], v250 offset:768\n"
    "v_mfma_f32_16x16x32_bf16 v[180:183], a[216:219], a[184:187], v[180:183]\n"
    "v_add_u32 v253, 20480, v253\n"
    "ds_read_b128 a[220:223], v250 offset:4864\n"
    "v_mfma_f32_16x16x32_bf16 v[180:183], a[212:215], a[180:183], v[180:183]\n"
    "ds_read_b128 a[216:219], v250 offset:4608\n"
    "v_mfma_f32_16x16x32_bf16 v[180:183], a[208:211], a[176:179], v[180:183]\n"
    "ds_read_b128 a[212:215], v250 offset:4352\n"
    "v_mfma_f32_16x16x32_bf16 v[184:187], a[224:227], a[176:179], v[228:231]\n"
    "ds_read_b128 a[208:211], v250 offset:4096\n"
    "v_mfma_f32_16x16x32_bf16 v[184:187], a[228:231], a[180:183], v[184:187]\n"
    "ds_read_b128 a[224:227], v250 offset:8192\n"
    "v_mfma_f32_16x16x32_bf16 v[184:187], a[232:235], a[184:187], v[184:187]\n"
    "ds_read_b128 a[228:231], v250 offset:8448\n"
    "v_mfma_f32_16x16x32_bf16 v[184:187], a[236:239], a[188:191], v[184:187]\n"
    "ds_read_b128 a[232:235], v250 offset:8704\n"
    "v_mfma_f32_16x16x32_bf16 v[188:191], a[252:255], a[188:191], v[228:231]\n"
    "ds_read_b128 a[236:239], v250 offset:8960\n"
    "v_mfma_f32_16x16x32_bf16 v[188:191], a[248:251], a[184:187], v[188:191]\n"
    "ds_read_b128 a[252:255], v250 offset:13056\n"
    "v_mfma_f32_16x16x32_bf16 v[188:191], a[244:247], a[180:183], v[188:191]\n"
    "ds_read_b128 a[248:251], v250 offset:12800\n"
    "v_mfma_f32_16x16x32_bf16 v[188:191], a[240:243], a[176:179], v[188:191]\n"
    "ds_read_b128 a[244:247], v250 offset:12544\n"
    "v_mfma_f32_16x16x32_bf16 v[48:51], v[200:203], v[96:99], v[48:51]\n"
    "v_exp_f32 v165, v165\n"
    "v_mfma_f32_16x16x32_bf16 v[48:51], v[204:207], v[100:103], v[48:51]\n"
    "ds_read_b128 a[240:243], v250 offset:12288\n"
    "v_xor_b32 v250, 0x10000, v250\n"
    "v_mfma_f32_16x16x32_bf16 v[52:55], v[200:203], v[104:107], v[52:55]\n"
    "v_exp_f32 v166, v166\n"
    "v_mfma_f32_16x16x32_bf16 v[52:55], v[204:207], v[108:111], v[52:55]\n"
    "v_exp_f32 v167, v167\n"
    "v_mfma_f32_16x16x32_bf16 v[56:59], v[200:203], v[112:115], v[56:59]\n"
    "v_cvt_pk_bf16_f32 v194, v164, v165\n"
    "v_cvt_pk_bf16_f32 v195, v166, v167\n"
    "v_mfma_f32_16x16x32_bf16 v[56:59], v[204:207], v[116:119], v[56:59]\n"
    "v_exp_f32 v168, v168\n"
    "v_mfma_f32_16x16x32_bf16 v[60:63], v[200:203], v[120:123], v[60:63]\n"
    "v_exp_f32 v169, v169\n"
    "v_mfma_f32_16x16x32_bf16 v[60:63], v[204:207], v[124:127], v[60:63]\n"
    "v_exp_f32 v170, v170\n"
    "v_mfma_f32_16x16x32_bf16 a[48:51], v[200:203], v[128:131], a[48:51]\n"
    "v_exp_f32 v171, v171\n"
    "v_mfma_f32_16x16x32_bf16 a[48:51], v[204:207], v[132:135], a[48:51]\n"
    "v_cvt_pk_bf16_f32 v196, v168, v169\n"
    "v_cvt_pk_bf16_f32 v197, v170, v171\n"
    "v_mfma_f32_16x16x32_bf16 a[52:55], v[200:203], v[136:139], a[52:55]\n"
    "v_exp_f32 v172, v172\n"
    "v_mfma_f32_16x16x32_bf16 a[52:55], v[204:207], v[140:143], a[52:55]\n"
    "v_exp_f32 v173, v173\n"
    "v_mfma_f32_16x16x32_bf16 a[56:59], v[200:203], v[144:147], a[56:59]\n"
    "v_exp_f32 v174, v174\n"
    "v_mfma_f32_16x16x32_bf16 a[56:59], v[204:207], v[148:151], a[56:59]\n"
    "v_exp_f32 v175, v175\n"
    "v_mfma_f32_16x16x32_bf16 a[60:63], v[200:203], v[152:155], a[60:63]\n"
    "v_cvt_pk_bf16_f32 v198, v172, v173\n"
    "v_cvt_pk_bf16_f32 v199, v174, v175\n"
    "v_mfma_f32_16x16x32_bf16 a[60:63], v[204:207], v[156:159], a[60:63]\n"
    "v_exp_f32 v176, v176\n"
    "v_mfma_f32_16x16x32_bf16 v[238:241], v[246:249], v[200:203], v[238:241]\n"
    "v_exp_f32 v177, v177\n"
    "v_mfma_f32_16x16x32_bf16 v[238:241], v[246:249], v[204:207], v[238:241]\n"
    "v_exp_f32 v178, v178\n"
    "s_waitcnt lgkmcnt(0)\n"
    "v_mfma_f32_16x16x32_bf16 v[160:163], a[192:195], a[96:99], v[208:211]\n"
    "buffer_load_dword v233, v253, s[84:87], 0 offen\n"
    "v_cvt_pk_bf16_f32 v200, v176, v177\n"
    "v_mfma_f32_16x16x32_bf16 v[160:163], a[196:199], a[100:103], v[160:163]\n"
    "v_add_u32 v253, 20480, v253\n"
    "v_mfma_f32_16x16x32_bf16 v[160:163], a[200:203], a[104:107], v[160:163]\n"
    "v_exp_f32 v179, v179\n"
    "v_mfma_f32_16x16x32_bf16 v[160:163], a[204:207], a[108:111], v[160:163]\n"
    "v_exp_f32 v180, v180\n"
    "v_mfma_f32_16x16x32_bf16 v[164:167], a[220:223], a[108:111], v[208:211]\n"
    "v_exp_f32 v181, v181\n"
    "v_mfma_f32_16x16x32_bf16 v[164:167], a[216:219], a[104:107], v[164:167]\n"
    "v_cvt_pk_bf16_f32 v201, v178, v179\n"
    "v_cvt_pk_bf16_f32 v202, v180, v181\n"
    "v_mfma_f32_16x16x32_bf16 v[164:167], a[212:215], a[100:103], v[164:167]\n"
    "v_exp_f32 v182, v182\n"
    "v_mfma_f32_16x16x32_bf16 v[164:167], a[208:211], a[96:99], v[164:167]\n"
    "v_exp_f32 v183, v183\n"
    "v_mfma_f32_16x16x32_bf16 v[168:171], a[224:227], a[96:99], v[208:211]\n"
    "v_exp_f32 v184, v184\n"
    "v_mfma_f32_16x16x32_bf16 v[168:171], a[228:231], a[100:103], v[168:171]\n"
    "v_exp_f32 v185, v185\n"
    "v_mfma_f32_16x16x32_bf16 v[168:171], a[232:235], a[104:107], v[168:171]\n"
    "v_cvt_pk_bf16_f32 v203, v182, v183\n"
    "v_cvt_pk_bf16_f32 v204, v184, v185\n"
    "v_mfma_f32_16x16x32_bf16 v[168:171], a[236:239], a[108:111], v[168:171]\n"
    "v_exp_f32 v186, v186\n"
    "v_mfma_f32_16x16x32_bf16 v[172:175], a[252:255], a[108:111], v[208:211]\n"
    "v_exp_f32 v187, v187\n"
    "v_mfma_f32_16x16x32_bf16 v[172:175], a[248:251], a[104:107], v[172:175]\n"
    "v_exp_f32 v188, v188\n"
    "v_mfma_f32_16x16x32_bf16 v[172:175], a[244:247], a[100:103], v[172:175]\n"
    "v_exp_f32 v189, v189\n"
    "v_mfma_f32_16x16x32_bf16 v[172:175], a[240:243], a[96:99], v[172:175]\n"
    "v_cvt_pk_bf16_f32 v205, v186, v187\n"
    "v_cvt_pk_bf16_f32 v206, v188, v189\n"
    "v_mfma_f32_16x16x32_bf16 v[64:67], v[192:195], v[96:99], v[64:67]\n"
    "v_exp_f32 v190, v190\n"
    "v_mfma_f32_16x16x32_bf16 v[64:67], v[196:199], v[100:103], v[64:67]\n"
    "v_exp_f32 v191, v191\n"
    "v_mfma_f32_16x16x32_bf16 v[68:71], v[192:195], v[104:107], v[68:71]\n"
    "v_cvt_pk_bf16_f32 v207, v190, v191\n"
    "v_mfma_f32_16x16x32_bf16 v[68:71], v[196:199], v[108:111], v[68:71]\n"
    "v_mfma_f32_16x16x32_bf16 v[72:75], v[192:195], v[112:115], v[72:75]\n"
    "v_mfma_f32_16x16x32_bf16 v[72:75], v[196:199], v[116:119], v[72:75]\n"
    "v_mfma_f32_16x16x32_bf16 v[76:79], v[192:195], v[120:123], v[76:79]\n"
    "v_mfma_f32_16x16x32_bf16 v[76:79], v[196:199], v[124:127], v[76:79]\n"
    "v_mfma_f32_16x16x32_bf16 a[64:67], v[192:195], v[128:131], a[64:67]\n"
    "v_mfma_f32_16x16x32_bf16 a[64:67], v[196:199], v[132:135], a[64:67]\n"
    "v_mfma_f32_16x16x32_bf16 a[68:71], v[192:195], v[136:139], a[68:71]\n"
    "v_mfma_f32_16x16x32_bf16 a[68:71], v[196:199], v[140:143], a[68:71]\n"
    "v_mfma_f32_16x16x32_bf16 a[72:75], v[192:195], v[144:147], a[72:75]\n"
    "buffer_load_dword v233, v253, s[84:87], 0 offen\n"
    "v_mfma_f32_16x16x32_bf16 a[72:75], v[196:199], v[148:151], a[72:75]\n"
    "v_add_u32 v253, 20480, v253\n"
    "v_mfma_f32_16x16x32_bf16 a[76:79], v[192:195], v[152:155], a[76:79]\n"
    "v_mfma_f32_16x16x32_bf16 a[76:79], v[196:199], v[156:159], a[76:79]\n"
    "v_mfma_f32_16x16x32_bf16 v[240:243], v[246:249], v[192:195], v[240:243]\n"
    "v_mfma_f32_16x16x32_bf16 v[240:243], v[246:249], v[196:199], v[240:243]\n"
    "v_mfma_f32_16x16x32_bf16 v[176:179], a[192:195], a[112:115], v[212:215]\n"
    "v_exp_f32 v160, v160\n"
    "v_mfma_f32_16x16x32_bf16 v[176:179], a[196:199], a[116:119], v[176:179]\n"
    "v_exp_f32 v161, v161\n"
    "v_mfma_f32_16x16x32_bf16 v[176:179], a[200:203], a[120:123], v[176:179]\n"
    "v_cvt_pk_bf16_f32 v192, v160, v161\n"
    "v_exp_f32 v162, v162\n"
    "v_mfma_f32_16x16x32_bf16 v[176:179], a[204:207], a[124:127], v[176:179]\n"
    "v_exp_f32 v163, v163\n"
    "v_mfma_f32_16x16x32_bf16 v[180:183], a[220:223], a[124:127], v[212:215]\n"
    "v_cvt_pk_bf16_f32 v193, v162, v163\n"
    "v_exp_f32 v164, v164\n"
    "v_mfma_f32_16x16x32_bf16 v[180:183], a[216:219], a[120:123], v[180:183]\n"
    "v_exp_f32 v165, v165\n"
    "v_mfma_f32_16x16x32_bf16 v[180:183], a[212:215], a[116:119], v[180:183]\n"
    "v_cvt_pk_bf16_f32 v194, v164, v165\n"
    "v_exp_f32 v166, v166\n"
    "v_mfma_f32_16x16x32_bf16 v[180:183], a[208:211], a[112:115], v[180:183]\n"
    "v_exp_f32 v167, v167\n"
    "v_mfma_f32_16x16x32_bf16 v[184:187], a[224:227], a[112:115], v[212:215]\n"
    "v_cvt_pk_bf16_f32 v195, v166, v167\n"
    "v_exp_f32 v168, v168\n"
    "v_mfma_f32_16x16x32_bf16 v[184:187], a[228:231], a[116:119], v[184:187]\n"
    "v_exp_f32 v169, v169\n"
    "v_mfma_f32_16x16x32_bf16 v[184:187], a[232:235], a[120:123], v[184:187]\n"
    "v_cvt_pk_bf16_f32 v196, v168, v169\n"
    "v_exp_f32 v170, v170\n"
    "v_mfma_f32_16x16x32_bf16 v[184:187], a[236:239], a[124:127], v[184:187]\n"
    "v_exp_f32 v171, v171\n"
    "v_mfma_f32_16x16x32_bf16 v[188:191], a[252:255], a[124:127], v[212:215]\n"
    "v_cvt_pk_bf16_f32 v197, v170, v171\n"
    "v_exp_f32 v172, v172\n"
    "v_mfma_f32_16x16x32_bf16 v[188:191], a[248:251], a[120:123], v[188:191]\n"
    "v_exp_f32 v173, v173\n"
    "v_mfma_f32_16x16x32_bf16 v[188:191], a[244:247], a[116:119], v[188:191]\n"
    "v_cvt_pk_bf16_f32 v198, v172, v173\n"
    "v_exp_f32 v174, v174\n"
    "v_mfma_f32_16x16x32_bf16 v[188:191], a[240:243], a[112:115], v[188:191]\n"
    "s_waitcnt vmcnt(3)\n"
    "s_barrier\n"
    "v_mfma_f32_16x16x32_bf16 a[80:83], v[200:203], v[128:131], a[80:83]\n"
    "ds_read_b64_tr_b16 v[128:129], v251 offset:512\n"
    "v_exp_f32 v175, v175\n"
    "v_mfma_f32_16x16x32_bf16 a[80:83], v[204:207], v[132:135], a[80:83]\n"
    "ds_read_b64_tr_b16 v[130:131], v251 offset:4608\n"
    "v_cvt_pk_bf16_f32 v199, v174, v175\n"
    "v_mfma_f32_16x16x32_bf16 a[84:87], v[200:203], v[136:139], a[84:87]\n"
    "ds_read_b64_tr_b16 v[132:133], v251 offset:8704\n"
    "v_mbcnt_lo_u32_b32 v253, -1, 0\n"
    "v_mfma_f32_16x16x32_bf16 a[84:87], v[204:207], v[140:143], a[84:87]\n"
    "ds_read_b64_tr_b16 v[134:135], v251 offset:12800\n"
    "v_mbcnt_hi_u32_b32 v253, -1, v253\n"
    "v_mfma_f32_16x16x32_bf16 a[88:91], v[200:203], v[144:147], a[88:91]\n"
    "ds_read_b64_tr_b16 v[136:137], v252 offset:512\n"
    "v_bfe_u32 v254, v253, 2, 2\n"
    "v_mfma_f32_16x16x32_bf16 a[88:91], v[204:207], v[148:151], a[88:91]\n"
    "ds_read_b64_tr_b16 v[138:139], v252 offset:4608\n"
    "v_lshrrev_b32 v255, 4, v253\n"
    "v_mfma_f32_16x16x32_bf16 a[92:95], v[200:203], v[152:155], a[92:95]\n"
    "ds_read_b64_tr_b16 v[140:141], v252 offset:8704\n"
    "v_cmp_eq_u32 s[92:93], v254, v255\n"
    "v_mfma_f32_16x16x32_bf16 a[92:95], v[204:207], v[156:159], a[92:95]\n"
    "ds_read_b64_tr_b16 v[142:143], v252 offset:12800\n"
    "v_and_b32 v254, 1, v253\n"
    "v_mfma_f32_16x16x32_bf16 v[80:83], v[200:203], v[96:99], v[80:83]\n"
    "ds_read_b64_tr_b16 v[144:145], v251 offset:768\n"
    "v_lshlrev_b32 v254, 4, v254\n"
    "v_mfma_f32_16x16x32_bf16 v[80:83], v[204:207], v[100:103], v[80:83]\n"
    "ds_read_b64_tr_b16 v[146:147], v251 offset:4864\n"
    "v_mov_b32 v255, 0x3f80\n"
    "v_mfma_f32_16x16x32_bf16 v[84:87], v[200:203], v[104:107], v[84:87]\n"
    "ds_read_b64_tr_b16 v[148:149], v251 offset:8960\n"
    "v_lshlrev_b32 v254, v254, v255\n"
    "v_mfma_f32_16x16x32_bf16 v[84:87], v[204:207], v[108:111], v[84:87]\n"
    "ds_read_b64_tr_b16 v[150:151], v251 offset:13056\n"
    "v_cndmask_b32 v254, 0, v254, s[92:93]\n"
    "v_mfma_f32_16x16x32_bf16 v[88:91], v[200:203], v[112:115], v[88:91]\n"
    "ds_read_b64_tr_b16 v[152:153], v252 offset:768\n"
    "v_and_b32 v255, 2, v253\n"
    "v_mfma_f32_16x16x32_bf16 v[88:91], v[204:207], v[116:119], v[88:91]\n"
    "ds_read_b64_tr_b16 v[154:155], v252 offset:4864\n"
    "v_cmp_eq_u32 s[92:93], 0, v255\n"
    "v_mfma_f32_16x16x32_bf16 v[92:95], v[200:203], v[120:123], v[92:95]\n"
    "ds_read_b64_tr_b16 v[156:157], v252 offset:8960\n"
    "v_cndmask_b32 v255, 0, v254, s[92:93]\n"
    "v_mfma_f32_16x16x32_bf16 v[92:95], v[204:207], v[124:127], v[92:95]\n"
    "ds_read_b64_tr_b16 v[158:159], v252 offset:13056\n"
    "v_accvgpr_write_b32 a96, v255\n"
    "v_mfma_f32_16x16x32_bf16 v[242:245], v[246:249], v[200:203], v[242:245]\n"
    "ds_read_b64_tr_b16 v[96:97], v251 offset:0\n"
    "v_accvgpr_write_b32 a98, v255\n"
    "v_mfma_f32_16x16x32_bf16 v[242:245], v[246:249], v[204:207], v[242:245]\n"
    "ds_read_b64_tr_b16 v[98:99], v251 offset:4096\n"
    "v_sub_u32 v255, v254, v255\n"
    "v_mfma_f32_16x16x32_bf16 v[160:163], a[192:195], a[128:131], v[216:219]\n"
    "ds_read_b64_tr_b16 v[100:101], v251 offset:8192\n"
    "v_accvgpr_write_b32 a97, v255\n"
    "v_mfma_f32_16x16x32_bf16 v[160:163], a[196:199], a[132:135], v[160:163]\n"
    "ds_read_b64_tr_b16 v[102:103], v251 offset:12288\n"
    "v_accvgpr_write_b32 a99, v255\n"
    "v_mfma_f32_16x16x32_bf16 v[160:163], a[200:203], a[136:139], v[160:163]\n"
    "ds_read_b64_tr_b16 v[104:105], v252 offset:0\n"
    "v_accvgpr_write_b32 a100, 0\n"
    "v_mfma_f32_16x16x32_bf16 v[160:163], a[204:207], a[140:143], v[160:163]\n"
    "ds_read_b64_tr_b16 v[106:107], v252 offset:4096\n"
    "v_accvgpr_write_b32 a101, 0\n"
    "v_mfma_f32_16x16x32_bf16 v[164:167], a[220:223], a[140:143], v[216:219]\n"
    "ds_read_b64_tr_b16 v[108:109], v252 offset:8192\n"
    "v_accvgpr_write_b32 a102, 0\n"
    "v_mfma_f32_16x16x32_bf16 v[164:167], a[216:219], a[136:139], v[164:167]\n"
    "ds_read_b64_tr_b16 v[110:111], v252 offset:12288\n"
    "v_accvgpr_write_b32 a103, 0\n"
    "v_mfma_f32_16x16x32_bf16 v[164:167], a[212:215], a[132:135], v[164:167]\n"
    "ds_read_b64_tr_b16 v[112:113], v251 offset:256\n"
    "v_lshlrev_b32 v254, 2, v253\n"
    "v_mfma_f32_16x16x32_bf16 v[164:167], a[208:211], a[128:131], v[164:167]\n"
    "ds_read_b64_tr_b16 v[114:115], v251 offset:4352\n"
    "v_and_b32 v255, 60, v254\n"
    "v_mfma_f32_16x16x32_bf16 v[168:171], a[224:227], a[128:131], v[216:219]\n"
    "ds_read_b64_tr_b16 v[116:117], v251 offset:8448\n"
    "v_add_u32 v254, s22, v254\n"
    "v_mfma_f32_16x16x32_bf16 v[168:171], a[228:231], a[132:135], v[168:171]\n"
    "ds_read_b64_tr_b16 v[118:119], v251 offset:12544\n"
    "v_add_u32 v255, s23, v255\n"
    "v_mfma_f32_16x16x32_bf16 v[168:171], a[232:235], a[136:139], v[168:171]\n"
    "ds_read_b64_tr_b16 v[120:121], v252 offset:256\n"
    "v_lshlrev_b32 v253, 4, v253\n"
    "v_mfma_f32_16x16x32_bf16 v[168:171], a[236:239], a[140:143], v[168:171]\n"
    "ds_read_b64_tr_b16 v[122:123], v252 offset:4352\n"
    "v_add_u32 v250, s66, v253\n"
    "v_mfma_f32_16x16x32_bf16 v[172:175], a[252:255], a[140:143], v[216:219]\n"
    "ds_read_b64_tr_b16 v[124:125], v252 offset:8448\n"
    "v_xor_b32 v253, 4096, v250\n"
    "v_mfma_f32_16x16x32_bf16 v[172:175], a[248:251], a[136:139], v[172:175]\n"
    "ds_read_b64_tr_b16 v[126:127], v252 offset:12544\n"
    "v_exp_f32 v176, v176\n"
    "v_mfma_f32_16x16x32_bf16 v[172:175], a[244:247], a[132:135], v[172:175]\n"
    "v_exp_f32 v177, v177\n"
    "v_mfma_f32_16x16x32_bf16 v[172:175], a[240:243], a[128:131], v[172:175]\n"
    "v_cvt_pk_bf16_f32 v200, v176, v177\n"
    "v_exp_f32 v178, v178\n"
    "s_waitcnt lgkmcnt(5)\n"
    "v_mfma_f32_16x16x32_bf16 a[0:3], v[192:195], v[128:131], a[0:3]\n"
    "v_exp_f32 v179, v179\n"
    "v_mfma_f32_16x16x32_bf16 a[0:3], v[196:199], v[132:135], a[0:3]\n"
    "v_cvt_pk_bf16_f32 v201, v178, v179\n"
    "v_exp_f32 v180, v180\n"
    "v_mfma_f32_16x16x32_bf16 a[4:7], v[192:195], v[136:139], a[4:7]\n"
    "v_exp_f32 v181, v181\n"
    "v_mfma_f32_16x16x32_bf16 a[4:7], v[196:199], v[140:143], a[4:7]\n"
    "s_nop 2\n"
    "ds_write_b128 v250, a[0:3] offset:0\n"
    "v_mfma_f32_16x16x32_bf16 a[8:11], v[192:195], v[144:147], a[8:11]\n"
    "v_cvt_pk_bf16_f32 v202, v180, v181\n"
    "v_exp_f32 v182, v182\n"
    "v_mfma_f32_16x16x32_bf16 a[8:11], v[196:199], v[148:151], a[8:11]\n"
    "v_exp_f32 v183, v183\n"
    "v_mfma_f32_16x16x32_bf16 a[12:15], v[192:195], v[152:155], a[12:15]\n"
    "ds_write_b128 v250, a[4:7] offset:1024\n"
    "v_mfma_f32_16x16x32_bf16 a[12:15], v[196:199], v[156:159], a[12:15]\n"
    "v_cvt_pk_bf16_f32 v203, v182, v183\n"
    "v_exp_f32 v184, v184\n"
    "v_mfma_f32_16x16x32_bf16 v[232:235], v[246:249], v[192:195], v[232:235]\n"
    "v_exp_f32 v185, v185\n"
    "v_mfma_f32_16x16x32_bf16 v[232:235], v[246:249], v[196:199], v[232:235]\n"
    "ds_write_b128 v250, a[8:11] offset:2048\n"
    "v_mfma_f32_16x16x32_bf16 v[0:3], v[192:195], v[96:99], v[0:3]\n"
    "v_cvt_pk_bf16_f32 v204, v184, v185\n"
    "v_exp_f32 v186, v186\n"
    "v_mfma_f32_16x16x32_bf16 v[0:3], v[196:199], v[100:103], v[0:3]\n"
    "v_exp_f32 v187, v187\n"
    "v_mfma_f32_16x16x32_bf16 v[4:7], v[192:195], v[104:107], v[4:7]\n"
    "ds_write_b128 v250, a[12:15] offset:3072\n"
    "v_mfma_f32_16x16x32_bf16 v[4:7], v[196:199], v[108:111], v[4:7]\n"
    "v_cvt_pk_bf16_f32 v205, v186, v187\n"
    "v_exp_f32 v188, v188\n"
    "v_mfma_f32_16x16x32_bf16 v[8:11], v[192:195], v[112:115], v[8:11]\n"
    "v_exp_f32 v189, v189\n"
    "s_waitcnt lgkmcnt(2)\n"
    "v_mfma_f32_16x16x32_bf16 v[8:11], v[196:199], v[116:119], v[8:11]\n"
    "ds_write_b32 v254, v232 offset:0\n"
    "v_cvt_pk_bf16_f32 v206, v188, v189\n"
    "v_mfma_f32_16x16x32_bf16 v[12:15], v[192:195], v[120:123], v[12:15]\n"
    "v_exp_f32 v190, v190\n"
    "v_mfma_f32_16x16x32_bf16 v[12:15], v[196:199], v[124:127], v[12:15]\n"
    "v_exp_f32 v191, v191\n"
    "v_mfma_f32_16x16x32_bf16 v[176:179], a[192:195], a[144:147], v[220:223]\n"
    "v_cvt_pk_bf16_f32 v207, v190, v191\n"
    "v_exp_f32 v160, v160\n"
    "v_mfma_f32_16x16x32_bf16 v[176:179], a[196:199], a[148:151], v[176:179]\n"
    "v_exp_f32 v161, v161\n"
    "v_mfma_f32_16x16x32_bf16 v[176:179], a[200:203], a[152:155], v[176:179]\n"
    "s_waitcnt lgkmcnt(0)\n"
    "s_barrier\n"
    "v_mfma_f32_16x16x32_bf16 v[176:179], a[204:207], a[156:159], v[176:179]\n"
    "ds_read2st64_b32 v[208:209], v255 offset0:0 offset1:8\n"
    "v_cvt_pk_bf16_f32 v192, v160, v161\n"
    "v_mfma_f32_16x16x32_bf16 v[180:183], a[220:223], a[156:159], v[220:223]\n"
    "ds_read_b128 v[212:215], v253 offset:0\n"
    "v_exp_f32 v162, v162\n"
    "v_mfma_f32_16x16x32_bf16 v[180:183], a[216:219], a[152:155], v[180:183]\n"
    "ds_read_b128 v[216:219], v253 offset:1024\n"
    "v_exp_f32 v163, v163\n"
    "v_mfma_f32_16x16x32_bf16 v[180:183], a[212:215], a[148:151], v[180:183]\n"
    "v_cvt_pk_bf16_f32 v193, v162, v163\n"
    "v_exp_f32 v164, v164\n"
    "v_mfma_f32_16x16x32_bf16 v[180:183], a[208:211], a[144:147], v[180:183]\n"
    "v_exp_f32 v165, v165\n"
    "v_mfma_f32_16x16x32_bf16 v[184:187], a[224:227], a[144:147], v[220:223]\n"
    "v_cvt_pk_bf16_f32 v194, v164, v165\n"
    "v_exp_f32 v166, v166\n"
    "v_mfma_f32_16x16x32_bf16 v[184:187], a[228:231], a[148:151], v[184:187]\n"
    "v_exp_f32 v167, v167\n"
    "v_mfma_f32_16x16x32_bf16 v[184:187], a[232:235], a[152:155], v[184:187]\n"
    "v_cvt_pk_bf16_f32 v195, v166, v167\n"
    "v_exp_f32 v168, v168\n"
    "v_mfma_f32_16x16x32_bf16 v[184:187], a[236:239], a[156:159], v[184:187]\n"
    "v_exp_f32 v169, v169\n"
    "v_mfma_f32_16x16x32_bf16 v[188:191], a[252:255], a[156:159], v[220:223]\n"
    "v_cvt_pk_bf16_f32 v196, v168, v169\n"
    "v_exp_f32 v170, v170\n"
    "v_mfma_f32_16x16x32_bf16 v[188:191], a[248:251], a[152:155], v[188:191]\n"
    "ds_read_b128 v[220:223], v253 offset:2048\n"
    "v_exp_f32 v171, v171\n"
    "v_mfma_f32_16x16x32_bf16 v[188:191], a[244:247], a[148:151], v[188:191]\n"
    "v_cvt_pk_bf16_f32 v197, v170, v171\n"
    "v_exp_f32 v172, v172\n"
    "v_mfma_f32_16x16x32_bf16 v[188:191], a[240:243], a[144:147], v[188:191]\n"
    "v_exp_f32 v173, v173\n"
    "v_mfma_f32_16x16x32_bf16 a[16:19], v[200:203], v[128:131], a[16:19]\n"
    "v_cvt_pk_bf16_f32 v198, v172, v173\n"
    "v_exp_f32 v174, v174\n"
    "v_mfma_f32_16x16x32_bf16 a[16:19], v[204:207], v[132:135], a[16:19]\n"
    "v_exp_f32 v175, v175\n"
    "v_mfma_f32_16x16x32_bf16 a[20:23], v[200:203], v[136:139], a[20:23]\n"
    "v_cvt_pk_bf16_f32 v199, v174, v175\n"
    "s_waitcnt lgkmcnt(1)\n"
    "v_add_f32 v208, v208, v209\n"
    "v_mfma_f32_16x16x32_bf16 a[20:23], v[204:207], v[140:143], a[20:23]\n"
    "v_rcp_f32 v209, v208\n"
    "v_mfma_f32_16x16x32_bf16 a[24:27], v[200:203], v[144:147], a[24:27]\n"
    "ds_write_b128 v250, a[16:19] offset:16384\n"
    "v_mfma_f32_16x16x32_bf16 a[24:27], v[204:207], v[148:151], a[24:27]\n"
    "v_mov_b32 v251, v208\n"
    "v_mov_b32 v252, v208\n"
    "v_mfma_f32_16x16x32_bf16 a[28:31], v[200:203], v[152:155], a[28:31]\n"
    "s_add_u32 s72, s71, 0\n"
    "v_cvt_pk_bf16_f32 v0, v0, v1\n"
    "v_mfma_f32_16x16x32_bf16 a[28:31], v[204:207], v[156:159], a[28:31]\n"
    "ds_write_b128 v250, a[20:23] offset:17408\n"
    "v_mfma_f32_16x16x32_bf16 v[234:237], v[246:249], v[200:203], v[234:237]\n"
    "v_cvt_pk_bf16_f32 v1, v2, v3\n"
    "v_cvt_pk_bf16_f32 v2, v212, v213\n"
    "v_mfma_f32_16x16x32_bf16 v[234:237], v[246:249], v[204:207], v[234:237]\n"
    "v_cvt_pk_bf16_f32 v3, v214, v215\n"
    "v_cvt_pk_bf16_f32 v4, v4, v5\n"
    "v_mfma_f32_16x16x32_bf16 v[16:19], v[200:203], v[96:99], v[16:19]\n"
    "ds_write_b128 v250, a[24:27] offset:18432\n"
    "v_mfma_f32_16x16x32_bf16 v[16:19], v[204:207], v[100:103], v[16:19]\n"
    "v_cvt_pk_bf16_f32 v5, v6, v7\n"
    "v_cvt_pk_bf16_f32 v6, v216, v217\n"
    "v_mfma_f32_16x16x32_bf16 v[20:23], v[200:203], v[104:107], v[20:23]\n"
    "v_cvt_pk_bf16_f32 v7, v218, v219\n"
    "v_cvt_pk_bf16_f32 v8, v8, v9\n"
    "v_mfma_f32_16x16x32_bf16 v[20:23], v[204:207], v[108:111], v[20:23]\n"
    "ds_write_b128 v250, a[28:31] offset:19456\n"
    "v_mfma_f32_16x16x32_bf16 v[24:27], v[200:203], v[112:115], v[24:27]\n"
    "v_cvt_pk_bf16_f32 v9, v10, v11\n"
    "s_waitcnt lgkmcnt(3)\n"
    "v_cvt_pk_bf16_f32 v10, v220, v221\n"
    "v_mfma_f32_16x16x32_bf16 v[24:27], v[204:207], v[116:119], v[24:27]\n"
    "v_cvt_pk_bf16_f32 v11, v222, v223\n"
    "v_cvt_pk_bf16_f32 v12, v12, v13\n"
    "v_mfma_f32_16x16x32_bf16 v[28:31], v[200:203], v[120:123], v[28:31]\n"
    "ds_write_b32 v254, v234 offset:256\n"
    "v_cvt_pk_bf16_f32 v13, v14, v15\n"
    "v_mfma_f32_16x16x32_bf16 v[28:31], v[204:207], v[124:127], v[28:31]\n"
    "v_exp_f32 v176, v176\n"
    "v_mfma_f32_16x16x32_bf16 v[160:163], a[192:195], a[160:163], v[224:227]\n"
    "v_exp_f32 v177, v177\n"
    "v_mfma_f32_16x16x32_bf16 v[160:163], a[196:199], a[164:167], v[160:163]\n"
    "v_cvt_pk_bf16_f32 v200, v176, v177\n"
    "v_exp_f32 v178, v178\n"
    "v_mfma_f32_16x16x32_bf16 v[160:163], a[200:203], a[168:171], v[160:163]\n"
    "v_exp_f32 v179, v179\n"
    "v_mfma_f32_16x16x32_bf16 v[160:163], a[204:207], a[172:175], v[160:163]\n"
    "v_cvt_pk_bf16_f32 v201, v178, v179\n"
    "v_exp_f32 v180, v180\n"
    "v_mfma_f32_16x16x32_bf16 v[164:167], a[220:223], a[172:175], v[224:227]\n"
    "v_exp_f32 v181, v181\n"
    "v_mfma_f32_16x16x32_bf16 v[164:167], a[216:219], a[168:171], v[164:167]\n"
    "v_cvt_pk_bf16_f32 v202, v180, v181\n"
    "v_exp_f32 v182, v182\n"
    "v_mfma_f32_16x16x32_bf16 v[164:167], a[212:215], a[164:167], v[164:167]\n"
    "v_exp_f32 v183, v183\n"
    "v_mfma_f32_16x16x32_bf16 v[164:167], a[208:211], a[160:163], v[164:167]\n"
    "v_cvt_pk_bf16_f32 v203, v182, v183\n"
    "v_exp_f32 v184, v184\n"
    "v_mfma_f32_16x16x32_bf16 v[168:171], a[224:227], a[160:163], v[224:227]\n"
    "v_exp_f32 v185, v185\n"
    "v_mfma_f32_16x16x32_bf16 v[168:171], a[228:231], a[164:167], v[168:171]\n"
    "v_cvt_pk_bf16_f32 v204, v184, v185\n"
    "v_exp_f32 v186, v186\n"
    "v_mfma_f32_16x16x32_bf16 v[168:171], a[232:235], a[168:171], v[168:171]\n"
    "v_exp_f32 v187, v187\n"
    "v_mfma_f32_16x16x32_bf16 v[168:171], a[236:239], a[172:175], v[168:171]\n"
    "v_cvt_pk_bf16_f32 v205, v186, v187\n"
    "v_exp_f32 v188, v188\n"
    "v_mfma_f32_16x16x32_bf16 v[172:175], a[252:255], a[172:175], v[224:227]\n"
    "v_exp_f32 v189, v189\n"
    "v_mfma_f32_16x16x32_bf16 v[172:175], a[248:251], a[168:171], v[172:175]\n"
    "ds_read_b128 v[224:227], v253 offset:3072\n"
    "v_cvt_pk_bf16_f32 v206, v188, v189\n"
    "v_mfma_f32_16x16x32_bf16 v[172:175], a[244:247], a[164:167], v[172:175]\n"
    "v_exp_f32 v190, v190\n"
    "v_mfma_f32_16x16x32_bf16 v[172:175], a[240:243], a[160:163], v[172:175]\n"
    "v_exp_f32 v191, v191\n"
    "v_mfma_f32_16x16x32_bf16 a[32:35], v[192:195], v[128:131], a[32:35]\n"
    "v_cvt_pk_bf16_f32 v207, v190, v191\n"
    "s_add_u32 s73, s71, 131072\n"
    "v_mfma_f32_16x16x32_bf16 a[32:35], v[196:199], v[132:135], a[32:35]\n"
    "v_cvt_pk_bf16_f32 v16, v16, v17\n"
    "v_cvt_pk_bf16_f32 v17, v18, v19\n"
    "v_mfma_f32_16x16x32_bf16 a[36:39], v[192:195], v[136:139], a[36:39]\n"
    "v_cvt_pk_bf16_f32 v20, v20, v21\n"
    "s_waitcnt lgkmcnt(0)\n"
    "s_barrier\n"
    "v_mfma_f32_16x16x32_bf16 a[36:39], v[196:199], v[140:143], a[36:39]\n"
    "ds_read2st64_b32 v[210:211], v255 offset0:1 offset1:9\n"
    "v_cvt_pk_bf16_f32 v14, v224, v225\n"
    "v_mfma_f32_16x16x32_bf16 a[40:43], v[192:195], v[144:147], a[40:43]\n"
    "ds_write_b128 v250, a[32:35] offset:32768\n"
    "v_mfma_f32_16x16x32_bf16 a[40:43], v[196:199], v[148:151], a[40:43]\n"
    "v_cvt_pk_bf16_f32 v15, v226, v227\n"
    "v_cvt_pk_bf16_f32 v21, v22, v23\n"
    "v_mfma_f32_16x16x32_bf16 a[44:47], v[192:195], v[152:155], a[44:47]\n"
    "v_cvt_pk_bf16_f32 v24, v24, v25\n"
    "v_cvt_pk_bf16_f32 v25, v26, v27\n"
    "v_mfma_f32_16x16x32_bf16 a[44:47], v[196:199], v[156:159], a[44:47]\n"
    "ds_write_b128 v250, a[36:39] offset:33792\n"
    "v_mfma_f32_16x16x32_bf16 v[236:239], v[246:249], v[192:195], v[236:239]\n"
    "v_cvt_pk_bf16_f32 v28, v28, v29\n"
    "s_waitcnt lgkmcnt(2)\n"
    "v_add_f32 v210, v210, v211\n"
    "v_mfma_f32_16x16x32_bf16 v[236:239], v[246:249], v[196:199], v[236:239]\n"
    "v_rcp_f32 v211, v210\n"
    "v_mfma_f32_16x16x32_bf16 v[32:35], v[192:195], v[96:99], v[32:35]\n"
    "ds_write_b128 v250, a[40:43] offset:34816\n"
    "v_mfma_f32_16x16x32_bf16 v[32:35], v[196:199], v[100:103], v[32:35]\n"
    "v_add_f32 v251, v251, v210\n"
    "v_min_f32 v252, v252, v210\n"
    "v_mfma_f32_16x16x32_bf16 v[36:39], v[192:195], v[104:107], v[36:39]\n"
    "v_cvt_pk_bf16_f32 v29, v30, v31\n"
    "v_exp_f32 v160, v160\n"
    "v_mfma_f32_16x16x32_bf16 v[36:39], v[196:199], v[108:111], v[36:39]\n"
    "ds_write_b128 v250, a[44:47] offset:35840\n"
    "v_mfma_f32_16x16x32_bf16 v[40:43], v[192:195], v[112:115], v[40:43]\n"
    "v_exp_f32 v161, v161\n"
    "v_mfma_f32_16x16x32_bf16 v[40:43], v[196:199], v[116:119], v[40:43]\n"
    "v_exp_f32 v162, v162\n"
    "v_mfma_f32_16x16x32_bf16 v[44:47], v[192:195], v[120:123], v[44:47]\n"
    "ds_write_b32 v254, v236 offset:512\n"
    "v_cvt_pk_bf16_f32 v192, v160, v161\n"
    "v_mfma_f32_16x16x32_bf16 v[44:47], v[196:199], v[124:127], v[44:47]\n"
    "v_exp_f32 v163, v163\n"
    "v_mfma_f32_16x16x32_bf16 v[212:215], v[0:3], a[96:99], 0\n"
    "v_cvt_pk_bf16_f32 v193, v162, v163\n"
    "v_exp_f32 v164, v164\n"
    "v_mfma_f32_16x16x32_bf16 v[216:219], v[4:7], a[96:99], 0\n"
    "ds_read_b128 v[4:7], v253 offset:17408\n"
    "v_exp_f32 v165, v165\n"
    "v_mfma_f32_16x16x32_bf16 v[220:223], v[8:11], a[96:99], 0\n"
    "s_nop 1\n"
    "v_mul_f32_e64 v212, v212, v209\n"
    "v_mul_f32_e64 v213, v213, v209\n"
    "v_mfma_f32_16x16x32_bf16 v[224:227], v[12:15], a[96:99], 0\n"
    "ds_read_b128 v[12:15], v253 offset:19456\n"
    "v_mul_f32_e64 v214, v214, v209\n"
    "v_mfma_f32_16x16x32_bf16 v[176:179], a[192:195], a[176:179], v[228:231]\n"
    "v_mul_f32_e64 v215, v215, v209\n"
    "v_mul_f32_e64 v216, v216, v209\n"
    "v_mfma_f32_16x16x32_bf16 v[176:179], a[196:199], a[180:183], v[176:179]\n"
    "v_mul_f32_e64 v217, v217, v209\n"
    "v_mul_f32_e64 v218, v218, v209\n"
    "v_mfma_f32_16x16x32_bf16 v[176:179], a[200:203], a[184:187], v[176:179]\n"
    "v_mul_f32_e64 v219, v219, v209\n"
    "v_cvt_pk_bf16_f32 v0, v212, v213\n"
    "v_mfma_f32_16x16x32_bf16 v[176:179], a[204:207], a[188:191], v[176:179]\n"
    "v_cvt_pk_bf16_f32 v1, v214, v215\n"
    "v_cvt_pk_bf16_f32 v2, v216, v217\n"
    "v_mfma_f32_16x16x32_bf16 v[180:183], a[220:223], a[188:191], v[228:231]\n"
    "v_cvt_pk_bf16_f32 v3, v218, v219\n"
    "v_mul_f32_e64 v220, v220, v209\n"
    "v_mfma_f32_16x16x32_bf16 v[180:183], a[216:219], a[184:187], v[180:183]\n"
    "buffer_store_dwordx4 v[0:3], v250, s[44:47], s72 offen offset:0 sc1\n"
    "v_mul_f32_e64 v221, v221, v209\n"
    "v_mfma_f32_16x16x32_bf16 a[104:107], v[0:3], a[100:103], 0\n"
    "ds_read_b128 v[0:3], v253 offset:16384\n"
    "v_mul_f32_e64 v222, v222, v209\n"
    "v_mfma_f32_16x16x32_bf16 v[180:183], a[212:215], a[180:183], v[180:183]\n"
    "v_mul_f32_e64 v223, v223, v209\n"
    "v_mul_f32_e64 v224, v224, v209\n"
    "v_mfma_f32_16x16x32_bf16 v[180:183], a[208:211], a[176:179], v[180:183]\n"
    "v_mul_f32_e64 v225, v225, v209\n"
    "v_mul_f32_e64 v226, v226, v209\n"
    "v_mfma_f32_16x16x32_bf16 v[184:187], a[224:227], a[176:179], v[228:231]\n"
    "v_mul_f32_e64 v227, v227, v209\n"
    "v_cvt_pk_bf16_f32 v8, v220, v221\n"
    "v_mfma_f32_16x16x32_bf16 v[184:187], a[228:231], a[180:183], v[184:187]\n"
    "v_cvt_pk_bf16_f32 v9, v222, v223\n"
    "v_cvt_pk_bf16_f32 v10, v224, v225\n"
    "v_mfma_f32_16x16x32_bf16 v[184:187], a[232:235], a[184:187], v[184:187]\n"
    "v_cvt_pk_bf16_f32 v11, v226, v227\n"
    "s_waitcnt lgkmcnt(1)\n"
    "v_cvt_pk_bf16_f32 v22, v4, v5\n"
    "v_mfma_f32_16x16x32_bf16 v[184:187], a[236:239], a[188:191], v[184:187]\n"
    "buffer_store_dwordx4 v[8:11], v250, s[44:47], s72 offen offset:1024 sc1\n"
    "v_cvt_pk_bf16_f32 v23, v6, v7\n"
    "v_mfma_f32_16x16x32_bf16 a[104:107], v[8:11], a[100:103], a[104:107]\n"
    "ds_read_b128 v[8:11], v253 offset:18432\n"
    "s_waitcnt lgkmcnt(1)\n"
    "v_cvt_pk_bf16_f32 v18, v0, v1\n"
    "v_mfma_f32_16x16x32_bf16 v[188:191], a[252:255], a[188:191], v[228:231]\n"
    "v_cvt_pk_bf16_f32 v19, v2, v3\n"
    "v_cvt_pk_bf16_f32 v30, v12, v13\n"
    "v_mfma_f32_16x16x32_bf16 v[188:191], a[248:251], a[184:187], v[188:191]\n"
    "v_cvt_pk_bf16_f32 v31, v14, v15\n"
    "v_cvt_pk_bf16_f32 v194, v164, v165\n"
    "v_mfma_f32_16x16x32_bf16 v[188:191], a[244:247], a[180:183], v[188:191]\n"
    "v_exp_f32 v166, v166\n"
    "v_mfma_f32_16x16x32_bf16 v[188:191], a[240:243], a[176:179], v[188:191]\n"
    "v_exp_f32 v167, v167\n"
    "v_mfma_f32_16x16x32_bf16 a[48:51], v[200:203], v[128:131], a[48:51]\n"
    "v_cvt_pk_bf16_f32 v195, v166, v167\n"
    "s_waitcnt lgkmcnt(0)\n"
    "s_barrier\n"
    "v_mfma_f32_16x16x32_bf16 a[48:51], v[204:207], v[132:135], a[48:51]\n"
    "ds_read2st64_b32 v[208:209], v255 offset0:2 offset1:10\n"
    "v_cvt_pk_bf16_f32 v26, v8, v9\n"
    "v_mfma_f32_16x16x32_bf16 a[52:55], v[200:203], v[136:139], a[52:55]\n"
    "ds_read_b128 v[212:215], v253 offset:32768\n"
    "v_cvt_pk_bf16_f32 v27, v10, v11\n"
    "v_mfma_f32_16x16x32_bf16 a[52:55], v[204:207], v[140:143], a[52:55]\n"
    "ds_read_b128 v[216:219], v253 offset:33792\n"
    "v_exp_f32 v168, v168\n"
    "v_mfma_f32_16x16x32_bf16 a[56:59], v[200:203], v[144:147], a[56:59]\n"
    "ds_read_b128 v[220:223], v253 offset:34816\n"
    "v_exp_f32 v169, v169\n"
    "v_mfma_f32_16x16x32_bf16 a[56:59], v[204:207], v[148:151], a[56:59]\n"
    "ds_read_b128 v[224:227], v253 offset:35840\n"
    "v_cvt_pk_bf16_f32 v196, v168, v169\n"
    "v_mfma_f32_16x16x32_bf16 a[60:63], v[200:203], v[152:155], a[60:63]\n"
    "ds_write_b128 v250, a[48:51] offset:0\n"
    "v_mfma_f32_16x16x32_bf16 a[60:63], v[204:207], v[156:159], a[60:63]\n"
    "v_exp_f32 v170, v170\n"
    "v_mfma_f32_16x16x32_bf16 v[238:241], v[246:249], v[200:203], v[238:241]\n"
    "v_exp_f32 v171, v171\n"
    "v_mfma_f32_16x16x32_bf16 v[238:241], v[246:249], v[204:207], v[238:241]\n"
    "ds_write_b128 v250, a[52:55] offset:1024\n"
    "v_mfma_f32_16x16x32_bf16 v[48:51], v[200:203], v[96:99], v[48:51]\n"
    "v_cvt_pk_bf16_f32 v197, v170, v171\n"
    "v_exp_f32 v172, v172\n"
    "v_mfma_f32_16x16x32_bf16 v[48:51], v[204:207], v[100:103], v[48:51]\n"
    "v_exp_f32 v173, v173\n"
    "v_mfma_f32_16x16x32_bf16 v[52:55], v[200:203], v[104:107], v[52:55]\n"
    "ds_write_b128 v250, a[56:59] offset:2048\n"
    "v_mfma_f32_16x16x32_bf16 v[52:55], v[204:207], v[108:111], v[52:55]\n"
    "v_cvt_pk_bf16_f32 v198, v172, v173\n"
    "v_exp_f32 v174, v174\n"
    "v_mfma_f32_16x16x32_bf16 v[56:59], v[200:203], v[112:115], v[56:59]\n"
    "v_exp_f32 v175, v175\n"
    "v_mfma_f32_16x16x32_bf16 v[56:59], v[204:207], v[116:119], v[56:59]\n"
    "ds_write_b128 v250, a[60:63] offset:3072\n"
    "v_mfma_f32_16x16x32_bf16 v[60:63], v[200:203], v[120:123], v[60:63]\n"
    "v_cvt_pk_bf16_f32 v199, v174, v175\n"
    "s_waitcnt lgkmcnt(3)\n"
    "v_add_f32 v208, v208, v209\n"
    "v_mfma_f32_16x16x32_bf16 v[60:63], v[204:207], v[124:127], v[60:63]\n"
    "v_rcp_f32 v209, v208\n"
    "v_mfma_f32_16x16x32_bf16 v[0:3], v[16:19], a[96:99], 0\n"
    "ds_write_b32 v254, v238 offset:768\n"
    "v_add_f32 v251, v251, v208\n"
    "v_mfma_f32_16x16x32_bf16 v[4:7], v[20:23], a[96:99], 0\n"
    "v_min_f32 v252, v252, v208\n"
    "s_add_u32 s74, s71, 262144\n"
    "v_mfma_f32_16x16x32_bf16 v[8:11], v[24:27], a[96:99], 0\n"
    "v_cvt_pk_bf16_f32 v32, v32, v33\n"
    "s_nop 0\n"
    "v_mul_f32_e64 v0, v0, v211\n"
    "v_mfma_f32_16x16x32_bf16 v[12:15], v[28:31], a[96:99], 0\n"
    "v_mul_f32_e64 v1, v1, v211\n"
    "v_mul_f32_e64 v2, v2, v211\n"
    "v_mfma_f32_16x16x32_bf16 a[64:67], v[192:195], v[128:131], a[64:67]\n"
    "v_mul_f32_e64 v3, v3, v211\n"
    "v_mul_f32_e64 v4, v4, v211\n"
    "v_mfma_f32_16x16x32_bf16 a[64:67], v[196:199], v[132:135], a[64:67]\n"
    "v_mul_f32_e64 v5, v5, v211\n"
    "s_waitcnt lgkmcnt(0)\n"
    "s_barrier\n"
    "v_mfma_f32_16x16x32_bf16 a[68:71], v[192:195], v[136:139], a[68:71]\n"
    "ds_read_b128 v[20:23], v253 offset:1024\n"
    "v_mul_f32_e64 v6, v6, v211\n"
    "v_mfma_f32_16x16x32_bf16 a[68:71], v[196:199], v[140:143], a[68:71]\n"
    "ds_read_b128 v[28:31], v253 offset:3072\n"
    "v_mul_f32_e64 v7, v7, v211\n"
    "v_mfma_f32_16x16x32_bf16 a[72:75], v[192:195], v[144:147], a[72:75]\n"
    "ds_write_b128 v250, a[64:67] offset:16384\n"
    "v_mfma_f32_16x16x32_bf16 a[72:75], v[196:199], v[148:151], a[72:75]\n"
    "v_cvt_pk_bf16_f32 v16, v0, v1\n"
    "v_cvt_pk_bf16_f32 v17, v2, v3\n"
    "v_mfma_f32_16x16x32_bf16 a[76:79], v[192:195], v[152:155], a[76:79]\n"
    "v_cvt_pk_bf16_f32 v18, v4, v5\n"
    "v_cvt_pk_bf16_f32 v19, v6, v7\n"
    "v_mfma_f32_16x16x32_bf16 a[76:79], v[196:199], v[156:159], a[76:79]\n"
    "ds_write_b128 v250, a[68:71] offset:17408\n"
    "v_mfma_f32_16x16x32_bf16 a[104:107], v[16:19], a[100:103], a[104:107]\n"
    "buffer_store_dwordx4 v[16:19], v250, s[44:47], s73 offen offset:0 sc1\n"
    "v_mul_f32_e64 v8, v8, v211\n"
    "v_mfma_f32_16x16x32_bf16 v[240:243], v[246:249], v[192:195], v[240:243]\n"
    "v_mul_f32_e64 v9, v9, v211\n"
    "v_mul_f32_e64 v10, v10, v211\n"
    "v_mfma_f32_16x16x32_bf16 v[240:243], v[246:249], v[196:199], v[240:243]\n"
    "ds_read_b128 v[16:19], v253 offset:0\n"
    "v_mul_f32_e64 v11, v11, v211\n"
    "v_mfma_f32_16x16x32_bf16 v[64:67], v[192:195], v[96:99], v[64:67]\n"
    "ds_write_b128 v250, a[72:75] offset:18432\n"
    "v_mfma_f32_16x16x32_bf16 v[64:67], v[196:199], v[100:103], v[64:67]\n"
    "v_mul_f32_e64 v12, v12, v211\n"
    "v_mul_f32_e64 v13, v13, v211\n"
    "v_mfma_f32_16x16x32_bf16 v[68:71], v[192:195], v[104:107], v[68:71]\n"
    "v_mul_f32_e64 v14, v14, v211\n"
    "v_mul_f32_e64 v15, v15, v211\n"
    "v_mfma_f32_16x16x32_bf16 v[68:71], v[196:199], v[108:111], v[68:71]\n"
    "ds_read2st64_b32 v[210:211], v255 offset0:3 offset1:11\n"
    "v_cvt_pk_bf16_f32 v24, v8, v9\n"
    "v_mfma_f32_16x16x32_bf16 v[72:75], v[192:195], v[112:115], v[72:75]\n"
    "ds_write_b128 v250, a[76:79] offset:19456\n"
    "v_mfma_f32_16x16x32_bf16 v[72:75], v[196:199], v[116:119], v[72:75]\n"
    "v_cvt_pk_bf16_f32 v25, v10, v11\n"
    "v_cvt_pk_bf16_f32 v26, v12, v13\n"
    "v_mfma_f32_16x16x32_bf16 v[76:79], v[192:195], v[120:123], v[76:79]\n"
    "v_cvt_pk_bf16_f32 v27, v14, v15\n"
    "v_cvt_pk_bf16_f32 v33, v34, v35\n"
    "v_mfma_f32_16x16x32_bf16 v[76:79], v[196:199], v[124:127], v[76:79]\n"
    "ds_write_b32 v254, v240 offset:1024\n"
    "buffer_store_dwordx4 v[24:27], v250, s[44:47], s73 offen offset:1024 sc1\n"
    "v_mfma_f32_16x16x32_bf16 a[104:107], v[24:27], a[100:103], a[104:107]\n"
    "v_cvt_pk_bf16_f32 v34, v212, v213\n"
    "ds_read_b128 v[24:27], v253 offset:2048\n"
    "v_cvt_pk_bf16_f32 v35, v214, v215\n"
    "v_cvt_pk_bf16_f32 v36, v36, v37\n"
    "v_cvt_pk_bf16_f32 v37, v38, v39\n"
    "v_mfma_f32_16x16x32_bf16 v[212:215], v[32:35], a[96:99], 0\n"
    "v_cvt_pk_bf16_f32 v38, v216, v217\n"
    "v_cvt_pk_bf16_f32 v39, v218, v219\n"
    "v_cvt_pk_bf16_f32 v40, v40, v41\n"
    "v_cvt_pk_bf16_f32 v41, v42, v43\n"
    "v_mfma_f32_16x16x32_bf16 v[216:219], v[36:39], a[96:99], 0\n"
    "v_cvt_pk_bf16_f32 v42, v220, v221\n"
    "v_cvt_pk_bf16_f32 v43, v222, v223\n"
    "v_cvt_pk_bf16_f32 v44, v44, v45\n"
    "v_mul_f32_e64 v212, v212, v209\n"
    "v_mfma_f32_16x16x32_bf16 v[220:223], v[40:43], a[96:99], 0\n"
    "v_mul_f32_e64 v213, v213, v209\n"
    "v_mul_f32_e64 v214, v214, v209\n"
    "v_mul_f32_e64 v215, v215, v209\n"
    "s_waitcnt lgkmcnt(0)\n"
    "s_barrier\n"
    "ds_read_b128 v[0:3], v253 offset:16384\n"
    "v_mul_f32_e64 v216, v216, v209\n"
    "v_mul_f32_e64 v217, v217, v209\n"
    "v_mul_f32_e64 v218, v218, v209\n"
    "ds_read_b128 v[4:7], v253 offset:17408\n"
    "v_mul_f32_e64 v219, v219, v209\n"
    "v_cvt_pk_bf16_f32 v32, v212, v213\n"
    "v_cvt_pk_bf16_f32 v33, v214, v215\n"
    "ds_read_b128 v[8:11], v253 offset:18432\n"
    "v_cvt_pk_bf16_f32 v34, v216, v217\n"
    "v_cvt_pk_bf16_f32 v35, v218, v219\n"
    "v_cvt_pk_bf16_f32 v45, v46, v47\n"
    "ds_read_b128 v[12:15], v253 offset:19456\n"
    "v_mfma_f32_16x16x32_bf16 a[104:107], v[32:35], a[100:103], a[104:107]\n"
    "buffer_store_dwordx4 v[32:35], v250, s[44:47], s74 offen offset:0 sc1\n"
    "v_cvt_pk_bf16_f32 v46, v224, v225\n"
    "v_cvt_pk_bf16_f32 v47, v226, v227\n"
    "v_mul_f32_e64 v220, v220, v209\n"
    "v_mul_f32_e64 v221, v221, v209\n"
    "v_mfma_f32_16x16x32_bf16 v[224:227], v[44:47], a[96:99], 0\n"
    "v_mul_f32_e64 v222, v222, v209\n"
    "v_mul_f32_e64 v223, v223, v209\n"
    "v_cvt_pk_bf16_f32 v40, v220, v221\n"
    "v_cvt_pk_bf16_f32 v41, v222, v223\n"
    "v_exp_f32 v176, v176\n"
    "v_exp_f32 v177, v177\n"
    "v_exp_f32 v178, v178\n"
    "s_nop 0\n"
    "v_mul_f32_e64 v224, v224, v209\n"
    "v_mul_f32_e64 v225, v225, v209\n"
    "v_mul_f32_e64 v226, v226, v209\n"
    "v_mul_f32_e64 v227, v227, v209\n"
    "ds_read2st64_b32 v[208:209], v255 offset0:4 offset1:12\n"
    "v_cvt_pk_bf16_f32 v42, v224, v225\n"
    "v_cvt_pk_bf16_f32 v43, v226, v227\n"
    "v_cvt_pk_bf16_f32 v200, v176, v177\n"
    "v_exp_f32 v179, v179\n"
    "v_mfma_f32_16x16x32_bf16 a[104:107], v[40:43], a[100:103], a[104:107]\n"
    "buffer_store_dwordx4 v[40:43], v250, s[44:47], s74 offen offset:1024 sc1\n"
    "v_cvt_pk_bf16_f32 v201, v178, v179\n"
    "v_exp_f32 v180, v180\n"
    "v_exp_f32 v181, v181\n"
    "v_exp_f32 v182, v182\n"
    "v_cvt_pk_bf16_f32 v202, v180, v181\n"
    "v_exp_f32 v183, v183\n"
    "v_exp_f32 v184, v184\n"
    "v_cvt_pk_bf16_f32 v203, v182, v183\n"
    "v_exp_f32 v185, v185\n"
    "s_nop 0\n"
    "v_mfma_f32_16x16x32_bf16 a[80:83], v[200:203], v[128:131], a[80:83]\n"
    "v_cvt_pk_bf16_f32 v204, v184, v185\n"
    "v_exp_f32 v186, v186\n"
    "v_mfma_f32_16x16x32_bf16 a[84:87], v[200:203], v[136:139], a[84:87]\n"
    "v_exp_f32 v187, v187\n"
    "v_mfma_f32_16x16x32_bf16 a[88:91], v[200:203], v[144:147], a[88:91]\n"
    "v_cvt_pk_bf16_f32 v205, v186, v187\n"
    "v_exp_f32 v188, v188\n"
    "v_mfma_f32_16x16x32_bf16 a[92:95], v[200:203], v[152:155], a[92:95]\n"
    "v_exp_f32 v189, v189\n"
    "v_mfma_f32_16x16x32_bf16 v[242:245], v[246:249], v[200:203], v[242:245]\n"
    "v_cvt_pk_bf16_f32 v206, v188, v189\n"
    "v_exp_f32 v190, v190\n"
    "v_mfma_f32_16x16x32_bf16 v[80:83], v[200:203], v[96:99], v[80:83]\n"
    "v_exp_f32 v191, v191\n"
    "v_mfma_f32_16x16x32_bf16 v[84:87], v[200:203], v[104:107], v[84:87]\n"
    "v_cvt_pk_bf16_f32 v207, v190, v191\n"
    "v_add_f32 v210, v210, v211\n"
    "v_mfma_f32_16x16x32_bf16 v[88:91], v[200:203], v[112:115], v[88:91]\n"
    "v_rcp_f32 v211, v210\n"
    "v_mfma_f32_16x16x32_bf16 a[80:83], v[204:207], v[132:135], a[80:83]\n"
    "v_add_f32 v251, v251, v210\n"
    "v_min_f32 v252, v252, v210\n"
    "v_mfma_f32_16x16x32_bf16 a[84:87], v[204:207], v[140:143], a[84:87]\n"
    "s_add_u32 s72, s71, 393216\n"
    "v_cvt_pk_bf16_f32 v48, v48, v49\n"
    "v_mfma_f32_16x16x32_bf16 a[88:91], v[204:207], v[148:151], a[88:91]\n"
    "v_cvt_pk_bf16_f32 v49, v50, v51\n"
    "s_nop 0\n"
    "ds_write_b128 v250, a[80:83] offset:32768\n"
    "v_mfma_f32_16x16x32_bf16 a[92:95], v[204:207], v[156:159], a[92:95]\n"
    "v_cvt_pk_bf16_f32 v50, v16, v17\n"
    "v_cvt_pk_bf16_f32 v51, v18, v19\n"
    "v_mfma_f32_16x16x32_bf16 v[242:245], v[246:249], v[204:207], v[242:245]\n"
    "v_cvt_pk_bf16_f32 v52, v52, v53\n"
    "v_cvt_pk_bf16_f32 v53, v54, v55\n"
    "v_mfma_f32_16x16x32_bf16 v[80:83], v[204:207], v[100:103], v[80:83]\n"
    "ds_write_b128 v250, a[84:87] offset:33792\n"
    "v_mfma_f32_16x16x32_bf16 v[84:87], v[204:207], v[108:111], v[84:87]\n"
    "v_cvt_pk_bf16_f32 v54, v20, v21\n"
    "v_cvt_pk_bf16_f32 v55, v22, v23\n"
    "v_mfma_f32_16x16x32_bf16 v[88:91], v[204:207], v[116:119], v[88:91]\n"
    "v_cvt_pk_bf16_f32 v56, v56, v57\n"
    "v_cvt_pk_bf16_f32 v57, v58, v59\n"
    "v_mfma_f32_16x16x32_bf16 v[92:95], v[200:203], v[120:123], v[92:95]\n"
    "ds_write_b128 v250, a[88:91] offset:34816\n"
    "v_mfma_f32_16x16x32_bf16 v[92:95], v[204:207], v[124:127], v[92:95]\n"
    "v_cvt_pk_bf16_f32 v58, v24, v25\n"
    "v_cvt_pk_bf16_f32 v59, v26, v27\n"
    "v_mfma_f32_16x16x32_bf16 v[16:19], v[48:51], a[96:99], 0\n"
    "v_cvt_pk_bf16_f32 v60, v60, v61\n"
    "v_cvt_pk_bf16_f32 v61, v62, v63\n"
    "v_mfma_f32_16x16x32_bf16 v[20:23], v[52:55], a[96:99], 0\n"
    "ds_write_b128 v250, a[92:95] offset:35840\n"
    "v_mfma_f32_16x16x32_bf16 v[24:27], v[56:59], a[96:99], 0\n"
    "s_nop 2\n"
    "v_mul_f32_e64 v16, v16, v211\n"
    "v_mul_f32_e64 v17, v17, v211\n"
    "v_mul_f32_e64 v18, v18, v211\n"
    "v_mul_f32_e64 v19, v19, v211\n"
    "v_mul_f32_e64 v20, v20, v211\n"
    "v_mul_f32_e64 v21, v21, v211\n"
    "v_mul_f32_e64 v22, v22, v211\n"
    "ds_write_b32 v254, v242 offset:1280\n"
    "v_mul_f32_e64 v23, v23, v211\n"
    "v_cvt_pk_bf16_f32 v48, v16, v17\n"
    "v_cvt_pk_bf16_f32 v49, v18, v19\n"
    "v_cvt_pk_bf16_f32 v50, v20, v21\n"
    "v_cvt_pk_bf16_f32 v51, v22, v23\n"
    "v_cvt_pk_bf16_f32 v62, v28, v29\n"
    "v_cvt_pk_bf16_f32 v63, v30, v31\n"
    "v_mfma_f32_16x16x32_bf16 a[104:107], v[48:51], a[100:103], a[104:107]\n"
    "buffer_store_dwordx4 v[48:51], v250, s[44:47], s72 offen offset:0 sc1\n"
    "v_mul_f32_e64 v24, v24, v211\n"
    "v_mfma_f32_16x16x32_bf16 v[28:31], v[60:63], a[96:99], 0\n"
    "v_mul_f32_e64 v25, v25, v211\n"
    "v_mul_f32_e64 v26, v26, v211\n"
    "v_mul_f32_e64 v27, v27, v211\n"
    "v_cvt_pk_bf16_f32 v56, v24, v25\n"
    "v_cvt_pk_bf16_f32 v57, v26, v27\n"
    "s_waitcnt lgkmcnt(1)\n"
    "v_add_f32 v208, v208, v209\n"
    "s_add_u32 s73, s71, 524288\n"
    "s_waitcnt lgkmcnt(0)\n"
    "s_barrier\n"
    "ds_read_b128 v[32:35], v253 offset:32768\n"
    "v_mul_f32_e64 v28, v28, v211\n"
    "v_mul_f32_e64 v29, v29, v211\n"
    "v_mul_f32_e64 v30, v30, v211\n"
    "ds_read_b128 v[36:39], v253 offset:33792\n"
    "v_mul_f32_e64 v31, v31, v211\n"
    "v_cvt_pk_bf16_f32 v58, v28, v29\n"
    "v_cvt_pk_bf16_f32 v59, v30, v31\n"
    "ds_read2st64_b32 v[210:211], v255 offset0:5 offset1:13\n"
    "v_rcp_f32 v209, v208\n"
    "v_mfma_f32_16x16x32_bf16 a[104:107], v[56:59], a[100:103], a[104:107]\n"
    "ds_read_b128 v[40:43], v253 offset:34816\n"
    "buffer_store_dwordx4 v[56:59], v250, s[44:47], s72 offen offset:1024 sc1\n"
    "v_add_f32 v251, v251, v208\n"
    "v_min_f32 v252, v252, v208\n"
    "ds_read_b128 v[44:47], v253 offset:35840\n"
    "v_cvt_pk_bf16_f32 v64, v64, v65\n"
    "v_cvt_pk_bf16_f32 v65, v66, v67\n"
    "v_cvt_pk_bf16_f32 v66, v0, v1\n"
    "v_cvt_pk_bf16_f32 v67, v2, v3\n"
    "v_cvt_pk_bf16_f32 v68, v68, v69\n"
    "v_cvt_pk_bf16_f32 v69, v70, v71\n"
    "v_mfma_f32_16x16x32_bf16 v[0:3], v[64:67], a[96:99], 0\n"
    "v_cvt_pk_bf16_f32 v70, v4, v5\n"
    "v_cvt_pk_bf16_f32 v71, v6, v7\n"
    "v_cvt_pk_bf16_f32 v72, v72, v73\n"
    "v_cvt_pk_bf16_f32 v73, v74, v75\n"
    "v_mfma_f32_16x16x32_bf16 v[4:7], v[68:71], a[96:99], 0\n"
    "v_cvt_pk_bf16_f32 v74, v8, v9\n"
    "v_cvt_pk_bf16_f32 v75, v10, v11\n"
    "v_cvt_pk_bf16_f32 v76, v76, v77\n"
    "v_mul_f32_e64 v0, v0, v209\n"
    "v_mfma_f32_16x16x32_bf16 v[8:11], v[72:75], a[96:99], 0\n"
    "v_mul_f32_e64 v1, v1, v209\n"
    "v_mul_f32_e64 v2, v2, v209\n"
    "v_mul_f32_e64 v3, v3, v209\n"
    "v_mul_f32_e64 v4, v4, v209\n"
    "v_mul_f32_e64 v5, v5, v209\n"
    "v_mul_f32_e64 v6, v6, v209\n"
    "v_mul_f32_e64 v7, v7, v209\n"
    "v_cvt_pk_bf16_f32 v64, v0, v1\n"
    "v_cvt_pk_bf16_f32 v65, v2, v3\n"
    "v_cvt_pk_bf16_f32 v66, v4, v5\n"
    "v_cvt_pk_bf16_f32 v67, v6, v7\n"
    "v_cvt_pk_bf16_f32 v77, v78, v79\n"
    "v_cvt_pk_bf16_f32 v78, v12, v13\n"
    "v_mfma_f32_16x16x32_bf16 a[104:107], v[64:67], a[100:103], a[104:107]\n"
    "buffer_store_dwordx4 v[64:67], v250, s[44:47], s73 offen offset:0 sc1\n"
    "v_cvt_pk_bf16_f32 v79, v14, v15\n"
    "v_mul_f32_e64 v8, v8, v209\n"
    "v_mul_f32_e64 v9, v9, v209\n"
    "v_mfma_f32_16x16x32_bf16 v[12:15], v[76:79], a[96:99], 0\n"
    "v_mul_f32_e64 v10, v10, v209\n"
    "v_mul_f32_e64 v11, v11, v209\n"
    "v_cvt_pk_bf16_f32 v72, v8, v9\n"
    "v_cvt_pk_bf16_f32 v73, v10, v11\n"
    "s_waitcnt lgkmcnt(0)\n"
    "v_add_f32 v210, v210, v211\n"
    "s_add_u32 s74, s71, 655360\n"
    "v_rcp_f32 v211, v210\n"
    "v_add_f32 v251, v251, v210\n"
    "v_mul_f32_e64 v12, v12, v209\n"
    "v_mul_f32_e64 v13, v13, v209\n"
    "v_mul_f32_e64 v14, v14, v209\n"
    "v_mul_f32_e64 v15, v15, v209\n"
    "v_cvt_pk_bf16_f32 v74, v12, v13\n"
    "v_cvt_pk_bf16_f32 v75, v14, v15\n"
    "v_min_f32 v252, v252, v210\n"
    "v_cvt_pk_bf16_f32 v80, v80, v81\n"
    "v_mfma_f32_16x16x32_bf16 a[104:107], v[72:75], a[100:103], a[104:107]\n"
    "buffer_store_dwordx4 v[72:75], v250, s[44:47], s73 offen offset:1024 sc1\n"
    "v_cvt_pk_bf16_f32 v81, v82, v83\n"
    "v_cvt_pk_bf16_f32 v82, v32, v33\n"
    "v_cvt_pk_bf16_f32 v83, v34, v35\n"
    "v_cvt_pk_bf16_f32 v84, v84, v85\n"
    "v_cvt_pk_bf16_f32 v85, v86, v87\n"
    "v_mfma_f32_16x16x32_bf16 v[32:35], v[80:83], a[96:99], 0\n"
    "v_cvt_pk_bf16_f32 v86, v36, v37\n"
    "v_cvt_pk_bf16_f32 v87, v38, v39\n"
    "v_cvt_pk_bf16_f32 v88, v88, v89\n"
    "v_cvt_pk_bf16_f32 v89, v90, v91\n"
    "v_mfma_f32_16x16x32_bf16 v[36:39], v[84:87], a[96:99], 0\n"
    "v_cvt_pk_bf16_f32 v90, v40, v41\n"
    "v_cvt_pk_bf16_f32 v91, v42, v43\n"
    "v_cvt_pk_bf16_f32 v92, v92, v93\n"
    "v_cvt_pk_bf16_f32 v93, v94, v95\n"
    "v_mfma_f32_16x16x32_bf16 v[40:43], v[88:91], a[96:99], 0\n"
    "v_cvt_pk_bf16_f32 v94, v44, v45\n"
    "v_cvt_pk_bf16_f32 v95, v46, v47\n"
    "v_pk_mul_f32 v[32:33], v[32:33], v[210:211] op_sel:[0,1]\n"
    "s_nop 0\n"
    "v_mfma_f32_16x16x32_bf16 v[44:47], v[92:95], a[96:99], 0\n"
    "v_cvt_pk_bf16_f32 v80, v32, v33\n"
    "v_pk_mul_f32 v[34:35], v[34:35], v[210:211] op_sel:[0,1]\n"
    "v_pk_mul_f32 v[36:37], v[36:37], v[210:211] op_sel:[0,1]\n"
    "v_pk_mul_f32 v[38:39], v[38:39], v[210:211] op_sel:[0,1]\n"
    "v_cvt_pk_bf16_f32 v81, v34, v35\n"
    "v_cvt_pk_bf16_f32 v82, v36, v37\n"
    "v_cvt_pk_bf16_f32 v83, v38, v39\n"
    "v_pk_mul_f32 v[40:41], v[40:41], v[210:211] op_sel:[0,1]\n"
    "v_pk_mul_f32 v[42:43], v[42:43], v[210:211] op_sel:[0,1]\n"
    "v_mfma_f32_16x16x32_bf16 a[104:107], v[80:83], a[100:103], a[104:107]\n"
    "buffer_store_dwordx4 v[80:83], v250, s[44:47], s74 offen offset:0 sc1\n"
    "v_cvt_pk_bf16_f32 v88, v40, v41\n"
    "v_cvt_pk_bf16_f32 v89, v42, v43\n"
    "v_pk_mul_f32 v[44:45], v[44:45], v[210:211] op_sel:[0,1]\n"
    "v_pk_mul_f32 v[46:47], v[46:47], v[210:211] op_sel:[0,1]\n"
    "v_cvt_pk_bf16_f32 v90, v44, v45\n"
    "v_cvt_pk_bf16_f32 v91, v46, v47\n"
    "s_nop 1\n"
    "v_mfma_f32_16x16x32_bf16 a[104:107], v[88:91], a[100:103], a[104:107]\n"
    "buffer_store_dwordx4 v[88:91], v250, s[44:47], s74 offen offset:1024 sc1\n"
    "s_nop 6\n"
    "v_accvgpr_read_b32 v96, a104\n"
    "v_accvgpr_read_b32 v97, a105\n"
    "v_accvgpr_read_b32 v98, a106\n"
    "v_accvgpr_read_b32 v99, a107\n"
    "v_add_f32 v251, v251, v96\n"
    "v_add_f32 v251, v251, v97\n"
    "v_add_f32 v251, v251, v98\n"
    "v_add_f32 v251, v251, v99\n"
    "v_mov_b32 v96, 0x5f800000\n"
    "v_cmp_nle_f32 vcc, v251, v96\n"
    "s_mov_b64 s[82:83], vcc\n"
    "v_cmp_nle_f32 vcc, 0.5, v252\n"
    "s_or_b64 s[82:83], s[82:83], vcc\n"
    "s_cbranch_scc1 SLOW_%=\n"
    "v_mbcnt_lo_u32_b32 v208, -1, 0\n"
    "v_mbcnt_hi_u32_b32 v208, -1, v208\n"
    "v_lshlrev_b32 v209, 4, v208\n"
    "v_mov_b32 v228, s78\n"
    "v_mov_b32 v229, s79\n"
    "v_bfe_u32 v246, v208, 2, 2\n"
    "v_mul_u32_u24 v246, 1280, v246\n"
    "v_lshrrev_b32 v247, 4, v208\n"
    "s_add_u32 s72, s89, 1\n"
    "v_add_u32 v247, s72, v247\n"
    "v_and_b32 v247, 3, v247\n"
    "v_lshl_add_u32 v246, v247, 6, v246\n"
    "v_and_b32 v247, 3, v208\n"
    "v_lshl_add_u32 v210, v247, 4, v246\n"
    "v_xor_b32 v247, 32, v210\n"
    "v_add_u32 v211, 4096, v247\n"
    "v_add_u32 v212, 8192, v210\n"
    "v_add_u32 v213, 12288, v247\n"
    "v_mov_b32 v222, s21\n"
    "v_mov_b32 v223, s20\n"
    "v_and_b32 v220, 7, v208\n"
    "v_lshlrev_b32 v220, 3, v220\n"
    "v_add_u32 v220, s18, v220\n"
    "v_and_b32 v221, 3, v208\n"
    "v_min_u32 v221, 2, v221\n"
    "v_add_u32 v221, s17, v221\n"
    "v_and_b32 v221, 3, v221\n"
    "v_lshlrev_b32 v221, 3, v221\n"
    "v_add_u32 v221, s19, v221\n"
    "v_bfe_u32 v216, v208, 2, 2\n"
    "v_lshrrev_b32 v217, 4, v208\n"
    "v_and_b32 v218, 3, v208\n"
    "v_and_b32 v219, 1, v217\n"
    "v_lshlrev_b32 v219, 5, v219\n"
    "v_lshl_add_u32 v219, v216, 6, v219\n"
    "v_lshl_add_u32 v219, v218, 3, v219\n"
    "v_lshl_add_u32 v214, v217, 10, v219\n"
    "v_xor_b32 v215, 32, v214\n"
    "v_and_b32 v224, 15, v208\n"
    "v_mul_u32_u24 v224, 1280, v224\n"
    "v_lshl_add_u32 v224, v217, 3, v224\n"
    "v_add_u32 v225, 20480, v224\n"
    "v_add_u32 v216, 65536, v214\n"
    "v_add_u32 v217, 65536, v215\n"
    "v_add_u32 v218, 131072, v214\n"
    "v_add_u32 v219, 131072, v215\n"
    "v_add_u32 v226, 131072, v209\n"
    "s_waitcnt vmcnt(0)\n"
    "s_barrier\n"
    "s_cmp_lg_u32 %[w], 0\n"
    "s_cbranch_scc1 NOFLAGF_%=\n"
    "s_mov_b64 exec, 1\n"
    "global_store_dwordx2 v222, v[228:229], s[28:29] sc1\n"
    "s_mov_b64 exec, -1\n"
    "NOFLAGF_%=:\n"
    "s_cmp_lt_u32 %[rk], 30\n"
    "s_cbranch_scc0 IDLEF_%=\n"
    "s_add_u32 m0, s66, 98304\n"
    "s_add_u32 s72, s65, 0\n"
    "buffer_load_dwordx4 v210, s[84:87], s72 offen offset:0 lds\n"
    "buffer_load_dwordx4 v211, s[84:87], s72 offen offset:1024 lds\n"
    "buffer_load_dwordx4 v212, s[84:87], s72 offen offset:2048 lds\n"
    "buffer_load_dwordx4 v213, s[84:87], s72 offen offset:3072 lds\n"
    "s_add_u32 m0, s66, 114688\n"
    "s_add_u32 s72, s65, 81920\n"
    "buffer_load_dwordx4 v210, s[84:87], s72 offen offset:0 lds\n"
    "buffer_load_dwordx4 v211, s[84:87], s72 offen offset:1024 lds\n"
    "buffer_load_dwordx4 v212, s[84:87], s72 offen offset:2048 lds\n"
    "buffer_load_dwordx4 v213, s[84:87], s72 offen offset:3072 lds\n"
    "s_add_u32 m0, s66, 131072\n"
    "s_add_u32 s72, s65, 163840\n"
    "buffer_load_dwordx4 v210, s[84:87], s72 offen offset:0 lds\n"
    "buffer_load_dwordx4 v211, s[84:87], s72 offen offset:1024 lds\n"
    "buffer_load_dwordx4 v212, s[84:87], s72 offen offset:2048 lds\n"
    "buffer_load_dwordx4 v213, s[84:87], s72 offen offset:3072 lds\n"
    "global_load_dwordx2 v[230:231], v220, s[28:29] sc1\n"
    "s_add_u32 m0, s66, 147456\n"
    "s_add_u32 s72, s65, 245760\n"
    "buffer_load_dwordx4 v210, s[84:87], s72 offen offset:0 lds\n"
    "buffer_load_dwordx4 v211, s[84:87], s72 offen offset:1024 lds\n"
    "buffer_load_dwordx4 v212, s[84:87], s72 offen offset:2048 lds\n"
    "buffer_load_dwordx4 v213, s[84:87], s72 offen offset:3072 lds\n"
    "s_add_u32 m0, s66, 65536\n"
    "s_add_u32 s72, s65, 327680\n"
    "buffer_load_dwordx4 v210, s[84:87], s72 offen offset:0 lds\n"
    "buffer_load_dwordx4 v211, s[84:87], s72 offen offset:1024 lds\n"
    "buffer_load_dwordx4 v212, s[84:87], s72 offen offset:2048 lds\n"
    "buffer_load_dwordx4 v213, s[84:87], s72 offen offset:3072 lds\n"
    "s_waitcnt vmcnt(13)\n"
    "s_barrier\n"
    "ds_read_b64_tr_b16 v[64:65], v216 offset:32768\n"
    "ds_read_b64_tr_b16 v[66:67], v216 offset:36864\n"
    "ds_read_b64_tr_b16 v[68:69], v216 offset:40960\n"
    "ds_read_b64_tr_b16 v[70:71], v216 offset:45056\n"
    "ds_read_b64_tr_b16 v[72:73], v217 offset:32768\n"
    "ds_read_b64_tr_b16 v[74:75], v217 offset:36864\n"
    "ds_read_b64_tr_b16 v[76:77], v217 offset:40960\n"
    "ds_read_b64_tr_b16 v[78:79], v217 offset:45056\n"
    "ds_read_b64_tr_b16 v[80:81], v216 offset:33024\n"
    "ds_read_b64_tr_b16 v[82:83], v216 offset:37120\n"
    "ds_read_b64_tr_b16 v[84:85], v216 offset:41216\n"
    "ds_read_b64_tr_b16 v[86:87], v216 offset:45312\n"
    "ds_read_b64_tr_b16 v[88:89], v217 offset:33024\n"
    "ds_read_b64_tr_b16 v[90:91], v217 offset:37120\n"
    "ds_read_b64_tr_b16 v[92:93], v217 offset:41216\n"
    "ds_read_b64_tr_b16 v[94:95], v217 offset:45312\n"
    "ds_read_b64_tr_b16 v[96:97], v216 offset:33280\n"
    "ds_read_b64_tr_b16 v[98:99], v216 offset:37376\n"
    "ds_read_b64_tr_b16 v[100:101], v216 offset:41472\n"
    "ds_read_b64_tr_b16 v[102:103], v216 offset:45568\n"
    "ds_read_b64_tr_b16 v[104:105], v217 offset:33280\n"
    "ds_read_b64_tr_b16 v[106:107], v217 offset:37376\n"
    "ds_read_b64_tr_b16 v[108:109], v217 offset:41472\n"
    "ds_read_b64_tr_b16 v[110:111], v217 offset:45568\n"
    "ds_read_b64_tr_b16 v[112:113], v216 offset:33536\n"
    "ds_read_b64_tr_b16 v[114:115], v216 offset:37632\n"
    "ds_read_b64_tr_b16 v[116:117], v216 offset:41728\n"
    "ds_read_b64_tr_b16 v[118:119], v216 offset:45824\n"
    "ds_read_b64_tr_b16 v[120:121], v217 offset:33536\n"
    "ds_read_b64_tr_b16 v[122:123], v217 offset:37632\n"
    "ds_read_b64_tr_b16 v[124:125], v217 offset:41728\n"
    "ds_read_b64_tr_b16 v[126:127], v217 offset:45824\n"
    "s_waitcnt vmcnt(8) lgkmcnt(0)\n"
    "v_cmp_ne_u64 vcc, v[230:231], v[228:229]\n"
    "s_and_b32 s73, vcc_lo, 255\n"
    "s_cbranch_scc1 P1SF_%=\n"
    "P1BF_%=:\n"
    "s_add_u32 s41, s40, 0\n"
    "buffer_load_dwordx4 a[0:3], v209, s[52:55], s41 offen offset:0 sc1\n"
    "buffer_load_dwordx4 a[8:11], v209, s[52:55], s41 offen offset:1024 sc1\n"
    "buffer_load_dwordx4 a[4:7], v226, s[52:55], s41 offen offset:0 sc1\n"
    "buffer_load_dwordx4 a[12:15], v226, s[52:55], s41 offen offset:1024 sc1\n"
    "buffer_load_dwordx4 a[16:19], v209, s[52:55], s41 offen offset:2048 sc1\n"
    "buffer_load_dwordx4 a[24:27], v209, s[52:55], s41 offen offset:3072 sc1\n"
    "buffer_load_dwordx4 a[20:23], v226, s[52:55], s41 offen offset:2048 sc1\n"
    "buffer_load_dwordx4 a[28:31], v226, s[52:55], s41 offen offset:3072 sc1\n"
    "s_add_u32 s41, s40, 4096\n"
    "buffer_load_dwordx4 a[32:35], v209, s[52:55], s41 offen offset:0 sc1\n"
    "buffer_load_dwordx4 a[40:43], v209, s[52:55], s41 offen offset:1024 sc1\n"
    "buffer_load_dwordx4 a[36:39], v226, s[52:55], s41 offen offset:0 sc1\n"
    "buffer_load_dwordx4 a[44:47], v226, s[52:55], s41 offen offset:1024 sc1\n"
    "s_add_u32 m0, s66, 81920\n"
    "s_add_u32 s72, s65, 409600\n"
    "buffer_load_dwordx4 v210, s[84:87], s72 offen offset:0 lds\n"
    "buffer_load_dwordx4 v211, s[84:87], s72 offen offset:1024 lds\n"
    "buffer_load_dwordx4 v212, s[84:87], s72 offen offset:2048 lds\n"
    "buffer_load_dwordx4 v213, s[84:87], s72 offen offset:3072 lds\n"
    "s_add_u32 m0, s66, 0\n"
    "s_add_u32 s72, s65, 491520\n"
    "buffer_load_dwordx4 v210, s[84:87], s72 offen offset:0 lds\n"
    "buffer_load_dwordx4 v211, s[84:87], s72 offen offset:1024 lds\n"
    "buffer_load_dwordx4 v212, s[84:87], s72 offen offset:2048 lds\n"
    "buffer_load_dwordx4 v213, s[84:87], s72 offen offset:3072 lds\n"
    "s_waitcnt vmcnt(16)\n"
    "v_mfma_f32_16x16x32_bf16 v[0:3], v[64:67], a[0:3], 0\n"
    "v_mfma_f32_16x16x32_bf16 v[0:3], v[68:71], a[8:11], v[0:3]\n"
    "v_mfma_f32_16x16x32_bf16 v[4:7], v[64:67], a[4:7], 0\n"
    "v_mfma_f32_16x16x32_bf16 v[4:7], v[68:71], a[12:15], v[4:7]\n"
    "v_mfma_f32_16x16x32_bf16 v[12:15], v[72:75], a[4:7], 0\n"
    "ds_read_b64_tr_b16 v[64:65], v216 offset:49152\n"
    "buffer_load_dwordx4 a[48:51], v209, s[52:55], s41 offen offset:2048 sc1\n"
    "v_mfma_f32_16x16x32_bf16 v[12:15], v[76:79], a[12:15], v[12:15]\n"
    "ds_read_b64_tr_b16 v[66:67], v216 offset:53248\n"
    "v_mfma_f32_16x16x32_bf16 v[8:11], v[72:75], a[0:3], 0\n"
    "ds_read_b64_tr_b16 v[68:69], v216 offset:57344\n"
    "v_mfma_f32_16x16x32_bf16 v[8:11], v[76:79], a[8:11], v[8:11]\n"
    "ds_read_b64_tr_b16 v[70:71], v216 offset:61440\n"
    "v_mfma_f32_16x16x32_bf16 v[16:19], v[80:83], a[0:3], 0\n"
    "ds_read_b64_tr_b16 v[72:73], v217 offset:49152\n"
    "v_mfma_f32_16x16x32_bf16 v[16:19], v[84:87], a[8:11], v[16:19]\n"
    "ds_read_b64_tr_b16 v[74:75], v217 offset:53248\n"
    "v_mfma_f32_16x16x32_bf16 v[20:23], v[80:83], a[4:7], 0\n"
    "ds_read_b64_tr_b16 v[76:77], v217 offset:57344\n"
    "v_mfma_f32_16x16x32_bf16 v[20:23], v[84:87], a[12:15], v[20:23]\n"
    "ds_read_b64_tr_b16 v[78:79], v217 offset:61440\n"
    "v_mfma_f32_16x16x32_bf16 v[28:31], v[88:91], a[4:7], 0\n"
    "ds_read_b64_tr_b16 v[80:81], v216 offset:49408\n"
    "buffer_load_dwordx4 a[56:59], v209, s[52:55], s41 offen offset:3072 sc1\n"
    "v_mfma_f32_16x16x32_bf16 v[28:31], v[92:95], a[12:15], v[28:31]\n"
    "ds_read_b64_tr_b16 v[82:83], v216 offset:53504\n"
    "v_mfma_f32_16x16x32_bf16 v[24:27], v[88:91], a[0:3], 0\n"
    "ds_read_b64_tr_b16 v[84:85], v216 offset:57600\n"
    "v_mfma_f32_16x16x32_bf16 v[24:27], v[92:95], a[8:11], v[24:27]\n"
    "ds_read_b64_tr_b16 v[86:87], v216 offset:61696\n"
    "v_mfma_f32_16x16x32_bf16 v[32:35], v[96:99], a[0:3], 0\n"
    "ds_read_b64_tr_b16 v[88:89], v217 offset:49408\n"
    "v_mfma_f32_16x16x32_bf16 v[32:35], v[100:103], a[8:11], v[32:35]\n"
    "ds_read_b64_tr_b16 v[90:91], v217 offset:53504\n"
    "v_mfma_f32_16x16x32_bf16 v[36:39], v[96:99], a[4:7], 0\n"
    "ds_read_b64_tr_b16 v[92:93], v217 offset:57600\n"
    "v_mfma_f32_16x16x32_bf16 v[36:39], v[100:103], a[12:15], v[36:39]\n"
    "ds_read_b64_tr_b16 v[94:95], v217 offset:61696\n"
    "v_mfma_f32_16x16x32_bf16 v[44:47], v[104:107], a[4:7], 0\n"
    "ds_read_b64_tr_b16 v[96:97], v216 offset:49664\n"
    "buffer_load_dwordx4 a[52:55], v226, s[52:55], s41 offen offset:2048 sc1\n"
    "v_mfma_f32_16x16x32_bf16 v[44:47], v[108:111], a[12:15], v[44:47]\n"
    "ds_read_b64_tr_b16 v[98:99], v216 offset:53760\n"
    "v_mfma_f32_16x16x32_bf16 v[40:43], v[104:107], a[0:3], 0\n"
    "ds_read_b64_tr_b16 v[100:101], v216 offset:57856\n"
    "v_mfma_f32_16x16x32_bf16 v[40:43], v[108:111], a[8:11], v[40:43]\n"
    "ds_read_b64_tr_b16 v[102:103], v216 offset:61952\n"
    "v_mfma_f32_16x16x32_bf16 v[48:51], v[112:115], a[0:3], 0\n"
    "ds_read_b64_tr_b16 v[104:105], v217 offset:49664\n"
    "v_mfma_f32_16x16x32_bf16 v[48:51], v[116:119], a[8:11], v[48:51]\n"
    "ds_read_b64_tr_b16 v[106:107], v217 offset:53760\n"
    "v_mfma_f32_16x16x32_bf16 v[52:55], v[112:115], a[4:7], 0\n"
    "ds_read_b64_tr_b16 v[108:109], v217 offset:57856\n"
    "v_mfma_f32_16x16x32_bf16 v[52:55], v[116:119], a[12:15], v[52:55]\n"
    "ds_read_b64_tr_b16 v[110:111], v217 offset:61952\n"
    "v_mfma_f32_16x16x32_bf16 v[60:63], v[120:123], a[4:7], 0\n"
    "ds_read_b64_tr_b16 v[112:113], v216 offset:49920\n"
    "buffer_load_dwordx4 a[60:63], v226, s[52:55], s41 offen offset:3072 sc1\n"
    "v_mfma_f32_16x16x32_bf16 v[60:63], v[124:127], a[12:15], v[60:63]\n"
    "ds_read_b64_tr_b16 v[114:115], v216 offset:54016\n"
    "v_mfma_f32_16x16x32_bf16 v[56:59], v[120:123], a[0:3], 0\n"
    "ds_read_b64_tr_b16 v[116:117], v216 offset:58112\n"
    "v_mfma_f32_16x16x32_bf16 v[56:59], v[124:127], a[8:11], v[56:59]\n"
    "ds_read_b64_tr_b16 v[118:119], v216 offset:62208\n"
    "s_waitcnt vmcnt(16)\n"
    "s_barrier\n"
    "s_waitcnt lgkmcnt(8)\n"
    "v_mfma_f32_16x16x32_bf16 v[0:3], v[64:67], a[16:19], v[0:3]\n"
    "ds_read_b64_tr_b16 v[120:121], v217 offset:49920\n"
    "v_mfma_f32_16x16x32_bf16 v[0:3], v[68:71], a[24:27], v[0:3]\n"
    "ds_read_b64_tr_b16 v[122:123], v217 offset:54016\n"
    "v_mfma_f32_16x16x32_bf16 v[4:7], v[64:67], a[20:23], v[4:7]\n"
    "ds_read_b64_tr_b16 v[124:125], v217 offset:58112\n"
    "v_mfma_f32_16x16x32_bf16 v[4:7], v[68:71], a[28:31], v[4:7]\n"
    "ds_read_b64_tr_b16 v[126:127], v217 offset:62208\n"
    "s_add_u32 s41, s40, 8192\n"
    "v_mfma_f32_16x16x32_bf16 v[12:15], v[72:75], a[20:23], v[12:15]\n"
    "ds_read_b64_tr_b16 v[64:65], v218 offset:0\n"
    "v_mfma_f32_16x16x32_bf16 v[12:15], v[76:79], a[28:31], v[12:15]\n"
    "ds_read_b64_tr_b16 v[66:67], v218 offset:4096\n"
    "v_mfma_f32_16x16x32_bf16 v[8:11], v[72:75], a[16:19], v[8:11]\n"
    "ds_read_b64_tr_b16 v[68:69], v218 offset:8192\n"
    "v_mfma_f32_16x16x32_bf16 v[8:11], v[76:79], a[24:27], v[8:11]\n"
    "ds_read_b64_tr_b16 v[70:71], v218 offset:12288\n"
    "v_mfma_f32_16x16x32_bf16 v[16:19], v[80:83], a[16:19], v[16:19]\n"
    "ds_read_b64_tr_b16 v[72:73], v219 offset:0\n"
    "v_mfma_f32_16x16x32_bf16 v[16:19], v[84:87], a[24:27], v[16:19]\n"
    "ds_read_b64_tr_b16 v[74:75], v219 offset:4096\n"
    "buffer_load_dwordx4 a[64:67], v209, s[52:55], s41 offen offset:0 sc1\n"
    "v_mfma_f32_16x16x32_bf16 v[20:23], v[80:83], a[20:23], v[20:23]\n"
    "ds_read_b64_tr_b16 v[76:77], v219 offset:8192\n"
    "v_mfma_f32_16x16x32_bf16 v[20:23], v[84:87], a[28:31], v[20:23]\n"
    "ds_read_b64_tr_b16 v[78:79], v219 offset:12288\n"
    "v_mfma_f32_16x16x32_bf16 v[28:31], v[88:91], a[20:23], v[28:31]\n"
    "ds_read_b64_tr_b16 v[80:81], v218 offset:256\n"
    "v_mfma_f32_16x16x32_bf16 v[28:31], v[92:95], a[28:31], v[28:31]\n"
    "ds_read_b64_tr_b16 v[82:83], v218 offset:4352\n"
    "v_mfma_f32_16x16x32_bf16 v[24:27], v[88:91], a[16:19], v[24:27]\n"
    "ds_read_b64_tr_b16 v[84:85], v218 offset:8448\n"
    "v_mfma_f32_16x16x32_bf16 v[24:27], v[92:95], a[24:27], v[24:27]\n"
    "ds_read_b64_tr_b16 v[86:87], v218 offset:12544\n"
    "v_mfma_f32_16x16x32_bf16 v[32:35], v[96:99], a[16:19], v[32:35]\n"
    "ds_read_b64_tr_b16 v[88:89], v219 offset:256\n"
    "buffer_load_dwordx4 a[72:75], v209, s[52:55], s41 offen offset:1024 sc1\n"
    "v_mfma_f32_16x16x32_bf16 v[32:35], v[100:103], a[24:27], v[32:35]\n"
    "ds_read_b64_tr_b16 v[90:91], v219 offset:4352\n"
    "v_mfma_f32_16x16x32_bf16 v[36:39], v[96:99], a[20:23], v[36:39]\n"
    "ds_read_b64_tr_b16 v[92:93], v219 offset:8448\n"
    "v_mfma_f32_16x16x32_bf16 v[36:39], v[100:103], a[28:31], v[36:39]\n"
    "ds_read_b64_tr_b16 v[94:95], v219 offset:12544\n"
    "s_waitcnt lgkmcnt(9)\n"
    "v_mfma_f32_16x16x32_bf16 v[44:47], v[104:107], a[20:23], v[44:47]\n"
    "ds_read_b64_tr_b16 v[96:97], v218 offset:512\n"
    "v_mfma_f32_16x16x32_bf16 v[44:47], v[108:111], a[28:31], v[44:47]\n"
    "ds_read_b64_tr_b16 v[98:99], v218 offset:4608\n"
    "v_mfma_f32_16x16x32_bf16 v[40:43], v[104:107], a[16:19], v[40:43]\n"
    "ds_read_b64_tr_b16 v[100:101], v218 offset:8704\n"
    "buffer_load_dwordx4 a[68:71], v226, s[52:55], s41 offen offset:0 sc1\n"
    "v_mfma_f32_16x16x32_bf16 v[40:43], v[108:111], a[24:27], v[40:43]\n"
    "ds_read_b64_tr_b16 v[102:103], v218 offset:12800\n"
    "v_mfma_f32_16x16x32_bf16 v[48:51], v[112:115], a[16:19], v[48:51]\n"
    "ds_read_b64_tr_b16 v[104:105], v219 offset:512\n"
    "v_mfma_f32_16x16x32_bf16 v[48:51], v[116:119], a[24:27], v[48:51]\n"
    "ds_read_b64_tr_b16 v[106:107], v219 offset:4608\n"
    "v_mfma_f32_16x16x32_bf16 v[52:55], v[112:115], a[20:23], v[52:55]\n"
    "ds_read_b64_tr_b16 v[108:109], v219 offset:8704\n"
    "v_mfma_f32_16x16x32_bf16 v[52:55], v[116:119], a[28:31], v[52:55]\n"
    "ds_read_b64_tr_b16 v[110:111], v219 offset:12800\n"
    "v_mfma_f32_16x16x32_bf16 v[60:63], v[120:123], a[20:23], v[60:63]\n"
    "ds_read_b64_tr_b16 v[112:113], v218 offset:768\n"
    "buffer_load_dwordx4 a[76:79], v226, s[52:55], s41 offen offset:1024 sc1\n"
    "v_mfma_f32_16x16x32_bf16 v[60:63], v[124:127], a[28:31], v[60:63]\n"
    "ds_read_b64_tr_b16 v[114:115], v218 offset:4864\n"
    "v_mfma_f32_16x16x32_bf16 v[56:59], v[120:123], a[16:19], v[56:59]\n"
    "ds_read_b64_tr_b16 v[116:117], v218 offset:8960\n"
    "v_mfma_f32_16x16x32_bf16 v[56:59], v[124:127], a[24:27], v[56:59]\n"
    "ds_read_b64_tr_b16 v[118:119], v218 offset:13056\n"
    "s_waitcnt vmcnt(16)\n"
    "v_mfma_f32_16x16x32_bf16 v[0:3], v[64:67], a[32:35], v[0:3]\n"
    "ds_read_b64_tr_b16 v[120:121], v219 offset:768\n"
    "v_mfma_f32_16x16x32_bf16 v[0:3], v[68:71], a[40:43], v[0:3]\n"
    "ds_read_b64_tr_b16 v[122:123], v219 offset:4864\n"
    "buffer_load_dwordx4 a[80:83], v209, s[52:55], s41 offen offset:2048 sc1\n"
    "v_mfma_f32_16x16x32_bf16 v[4:7], v[64:67], a[36:39], v[4:7]\n"
    "ds_read_b64_tr_b16 v[124:125], v219 offset:8960\n"
    "v_mfma_f32_16x16x32_bf16 v[4:7], v[68:71], a[44:47], v[4:7]\n"
    "ds_read_b64_tr_b16 v[126:127], v219 offset:13056\n"
    "v_mfma_f32_16x16x32_bf16 v[12:15], v[72:75], a[36:39], v[12:15]\n"
    "ds_read_b64_tr_b16 v[64:65], v218 offset:16384\n"
    "buffer_load_dwordx4 a[88:91], v209, s[52:55], s41 offen offset:3072 sc1\n"
    "s_waitcnt lgkmcnt(8)\n"
    "v_mfma_f32_16x16x32_bf16 v[12:15], v[76:79], a[44:47], v[12:15]\n"
    "ds_read_b64_tr_b16 v[66:67], v218 offset:20480\n"
    "v_mfma_f32_16x16x32_bf16 v[8:11], v[72:75], a[32:35], v[8:11]\n"
    "ds_read_b64_tr_b16 v[68:69], v218 offset:24576\n"
    "v_mfma_f32_16x16x32_bf16 v[8:11], v[76:79], a[40:43], v[8:11]\n"
    "ds_read_b64_tr_b16 v[70:71], v218 offset:28672\n"
    "v_mfma_f32_16x16x32_bf16 v[16:19], v[80:83], a[32:35], v[16:19]\n"
    "ds_read_b64_tr_b16 v[72:73], v219 offset:16384\n"
    "buffer_load_dwordx4 a[84:87], v226, s[52:55], s41 offen offset:2048 sc1\n"
    "v_mfma_f32_16x16x32_bf16 v[16:19], v[84:87], a[40:43], v[16:19]\n"
    "ds_read_b64_tr_b16 v[74:75], v219 offset:20480\n"
    "v_mfma_f32_16x16x32_bf16 v[20:23], v[80:83], a[36:39], v[20:23]\n"
    "ds_read_b64_tr_b16 v[76:77], v219 offset:24576\n"
    "v_mfma_f32_16x16x32_bf16 v[20:23], v[84:87], a[44:47], v[20:23]\n"
    "ds_read_b64_tr_b16 v[78:79], v219 offset:28672\n"
    "buffer_load_dwordx4 a[92:95], v226, s[52:55], s41 offen offset:3072 sc1\n"
    "v_mfma_f32_16x16x32_bf16 v[28:31], v[88:91], a[36:39], v[28:31]\n"
    "ds_read_b64_tr_b16 v[80:81], v218 offset:16640\n"
    "v_mfma_f32_16x16x32_bf16 v[28:31], v[92:95], a[44:47], v[28:31]\n"
    "ds_read_b64_tr_b16 v[82:83], v218 offset:20736\n"
    "v_mfma_f32_16x16x32_bf16 v[24:27], v[88:91], a[32:35], v[24:27]\n"
    "ds_read_b64_tr_b16 v[84:85], v218 offset:24832\n"
    "s_add_u32 m0, s66, 16384\n"
    "v_mfma_f32_16x16x32_bf16 v[24:27], v[92:95], a[40:43], v[24:27]\n"
    "ds_read_b64_tr_b16 v[86:87], v218 offset:28928\n"
    "v_mfma_f32_16x16x32_bf16 v[32:35], v[96:99], a[32:35], v[32:35]\n"
    "ds_read_b64_tr_b16 v[88:89], v219 offset:16640\n"
    "v_mfma_f32_16x16x32_bf16 v[32:35], v[100:103], a[40:43], v[32:35]\n"
    "ds_read_b64_tr_b16 v[90:91], v219 offset:20736\n"
    "s_add_u32 s72, s65, 573440\n"
    "v_mfma_f32_16x16x32_bf16 v[36:39], v[96:99], a[36:39], v[36:39]\n"
    "ds_read_b64_tr_b16 v[92:93], v219 offset:24832\n"
    "v_mfma_f32_16x16x32_bf16 v[36:39], v[100:103], a[44:47], v[36:39]\n"
    "ds_read_b64_tr_b16 v[94:95], v219 offset:28928\n"
    "v_mfma_f32_16x16x32_bf16 v[44:47], v[104:107], a[36:39], v[44:47]\n"
    "ds_read_b64_tr_b16 v[96:97], v218 offset:16896\n"
    "buffer_load_dwordx4 v210, s[84:87], s72 offen offset:0 lds\n"
    "v_mfma_f32_16x16x32_bf16 v[44:47], v[108:111], a[44:47], v[44:47]\n"
    "ds_read_b64_tr_b16 v[98:99], v218 offset:20992\n"
    "v_mfma_f32_16x16x32_bf16 v[40:43], v[104:107], a[32:35], v[40:43]\n"
    "ds_read_b64_tr_b16 v[100:101], v218 offset:25088\n"
    "v_mfma_f32_16x16x32_bf16 v[40:43], v[108:111], a[40:43], v[40:43]\n"
    "ds_read_b64_tr_b16 v[102:103], v218 offset:29184\n"
    "s_waitcnt lgkmcnt(9)\n"
    "v_mfma_f32_16x16x32_bf16 v[48:51], v[112:115], a[32:35], v[48:51]\n"
    "ds_read_b64_tr_b16 v[104:105], v219 offset:16896\n"
    "buffer_load_dwordx4 v211, s[84:87], s72 offen offset:1024 lds\n"
    "v_mfma_f32_16x16x32_bf16 v[48:51], v[116:119], a[40:43], v[48:51]\n"
    "ds_read_b64_tr_b16 v[106:107], v219 offset:20992\n"
    "v_mfma_f32_16x16x32_bf16 v[52:55], v[112:115], a[36:39], v[52:55]\n"
    "ds_read_b64_tr_b16 v[108:109], v219 offset:25088\n"
    "v_mfma_f32_16x16x32_bf16 v[52:55], v[116:119], a[44:47], v[52:55]\n"
    "ds_read_b64_tr_b16 v[110:111], v219 offset:29184\n"
    "buffer_load_dwordx4 v212, s[84:87], s72 offen offset:2048 lds\n"
    "v_mfma_f32_16x16x32_bf16 v[60:63], v[120:123], a[36:39], v[60:63]\n"
    "ds_read_b64_tr_b16 v[112:113], v218 offset:17152\n"
    "v_mfma_f32_16x16x32_bf16 v[60:63], v[124:127], a[44:47], v[60:63]\n"
    "ds_read_b64_tr_b16 v[114:115], v218 offset:21248\n"
    "v_mfma_f32_16x16x32_bf16 v[56:59], v[120:123], a[32:35], v[56:59]\n"
    "ds_read_b64_tr_b16 v[116:117], v218 offset:25344\n"
    "buffer_load_dwordx4 v213, s[84:87], s72 offen offset:3072 lds\n"
    "v_mfma_f32_16x16x32_bf16 v[56:59], v[124:127], a[40:43], v[56:59]\n"
    "ds_read_b64_tr_b16 v[118:119], v218 offset:29440\n"
    "s_waitcnt vmcnt(12)\n"
    "s_barrier\n"
    "v_mfma_f32_16x16x32_bf16 v[0:3], v[64:67], a[48:51], v[0:3]\n"
    "ds_read_b64_tr_b16 v[120:121], v219 offset:17152\n"
    "v_mfma_f32_16x16x32_bf16 v[0:3], v[68:71], a[56:59], v[0:3]\n"
    "ds_read_b64_tr_b16 v[122:123], v219 offset:21248\n"
    "s_add_u32 s41, s40, 12288\n"
    "v_mfma_f32_16x16x32_bf16 v[4:7], v[64:67], a[52:55], v[4:7]\n"
    "ds_read_b64_tr_b16 v[124:125], v219 offset:25344\n"
    "v_mfma_f32_16x16x32_bf16 v[4:7], v[68:71], a[60:63], v[4:7]\n"
    "ds_read_b64_tr_b16 v[126:127], v219 offset:29440\n"
    "v_mfma_f32_16x16x32_bf16 v[12:15], v[72:75], a[52:55], v[12:15]\n"
    "ds_read_b64_tr_b16 v[64:65], v216 offset:0\n"
    "buffer_load_dwordx4 a[96:99], v209, s[52:55], s41 offen offset:0 sc1\n"
    "v_mfma_f32_16x16x32_bf16 v[12:15], v[76:79], a[60:63], v[12:15]\n"
    "ds_read_b64_tr_b16 v[66:67], v216 offset:4096\n"
    "v_mfma_f32_16x16x32_bf16 v[8:11], v[72:75], a[48:51], v[8:11]\n"
    "ds_read_b64_tr_b16 v[68:69], v216 offset:8192\n"
    "v_mfma_f32_16x16x32_bf16 v[8:11], v[76:79], a[56:59], v[8:11]\n"
    "ds_read_b64_tr_b16 v[70:71], v216 offset:12288\n"
    "buffer_load_dwordx4 a[104:107], v209, s[52:55], s41 offen offset:1024 sc1\n"
    "v_mfma_f32_16x16x32_bf16 v[16:19], v[80:83], a[48:51], v[16:19]\n"
    "ds_read_b64_tr_b16 v[72:73], v217 offset:0\n"
    "s_waitcnt lgkmcnt(8)\n"
    "v_mfma_f32_16x16x32_bf16 v[16:19], v[84:87], a[56:59], v[16:19]\n"
    "ds_read_b64_tr_b16 v[74:75], v217 offset:4096\n"
    "v_mfma_f32_16x16x32_bf16 v[20:23], v[80:83], a[52:55], v[20:23]\n"
    "ds_read_b64_tr_b16 v[76:77], v217 offset:8192\n"
    "buffer_load_dwordx4 a[100:103], v226, s[52:55], s41 offen offset:0 sc1\n"
    "v_mfma_f32_16x16x32_bf16 v[20:23], v[84:87], a[60:63], v[20:23]\n"
    "ds_read_b64_tr_b16 v[78:79], v217 offset:12288\n"
    "v_mfma_f32_16x16x32_bf16 v[28:31], v[88:91], a[52:55], v[28:31]\n"
    "ds_read_b64_tr_b16 v[80:81], v216 offset:256\n"
    "v_mfma_f32_16x16x32_bf16 v[28:31], v[92:95], a[60:63], v[28:31]\n"
    "ds_read_b64_tr_b16 v[82:83], v216 offset:4352\n"
    "buffer_load_dwordx4 a[108:111], v226, s[52:55], s41 offen offset:1024 sc1\n"
    "v_mfma_f32_16x16x32_bf16 v[24:27], v[88:91], a[48:51], v[24:27]\n"
    "ds_read_b64_tr_b16 v[84:85], v216 offset:8448\n"
    "v_mfma_f32_16x16x32_bf16 v[24:27], v[92:95], a[56:59], v[24:27]\n"
    "ds_read_b64_tr_b16 v[86:87], v216 offset:12544\n"
    "v_mfma_f32_16x16x32_bf16 v[32:35], v[96:99], a[48:51], v[32:35]\n"
    "ds_read_b64_tr_b16 v[88:89], v217 offset:256\n"
    "s_add_u32 m0, s66, 32768\n"
    "v_mfma_f32_16x16x32_bf16 v[32:35], v[100:103], a[56:59], v[32:35]\n"
    "ds_read_b64_tr_b16 v[90:91], v217 offset:4352\n"
    "v_mfma_f32_16x16x32_bf16 v[36:39], v[96:99], a[52:55], v[36:39]\n"
    "ds_read_b64_tr_b16 v[92:93], v217 offset:8448\n"
    "s_add_u32 s72, s65, 655360\n"
    "v_mfma_f32_16x16x32_bf16 v[36:39], v[100:103], a[60:63], v[36:39]\n"
    "ds_read_b64_tr_b16 v[94:95], v217 offset:12544\n"
    "v_mfma_f32_16x16x32_bf16 v[44:47], v[104:107], a[52:55], v[44:47]\n"
    "ds_read_b64_tr_b16 v[96:97], v216 offset:512\n"
    "v_mfma_f32_16x16x32_bf16 v[44:47], v[108:111], a[60:63], v[44:47]\n"
    "ds_read_b64_tr_b16 v[98:99], v216 offset:4608\n"
    "buffer_load_dwordx4 v210, s[84:87], s72 offen offset:0 lds\n"
    "v_mfma_f32_16x16x32_bf16 v[40:43], v[104:107], a[48:51], v[40:43]\n"
    "ds_read_b64_tr_b16 v[100:101], v216 offset:8704\n"
    "v_mfma_f32_16x16x32_bf16 v[40:43], v[108:111], a[56:59], v[40:43]\n"
    "ds_read_b64_tr_b16 v[102:103], v216 offset:12800\n"
    "v_mfma_f32_16x16x32_bf16 v[48:51], v[112:115], a[48:51], v[48:51]\n"
    "ds_read_b64_tr_b16 v[104:105], v217 offset:512\n"
    "buffer_load_dwordx4 v211, s[84:87], s72 offen offset:1024 lds\n"
    "v_mfma_f32_16x16x32_bf16 v[48:51], v[116:119], a[56:59], v[48:51]\n"
    "ds_read_b64_tr_b16 v[106:107], v217 offset:4608\n"
    "v_mfma_f32_16x16x32_bf16 v[52:55], v[112:115], a[52:55], v[52:55]\n"
    "ds_read_b64_tr_b16 v[108:109], v217 offset:8704\n"
    "v_mfma_f32_16x16x32_bf16 v[52:55], v[116:119], a[60:63], v[52:55]\n"
    "ds_read_b64_tr_b16 v[110:111], v217 offset:12800\n"
    "buffer_load_dwordx4 v212, s[84:87], s72 offen offset:2048 lds\n"
    "s_waitcnt lgkmcnt(8)\n"
    "v_mfma_f32_16x16x32_bf16 v[60:63], v[120:123], a[52:55], v[60:63]\n"
    "ds_read_b64_tr_b16 v[112:113], v216 offset:768\n"
    "v_mfma_f32_16x16x32_bf16 v[60:63], v[124:127], a[60:63], v[60:63]\n"
    "ds_read_b64_tr_b16 v[114:115], v216 offset:4864\n"
    "v_mfma_f32_16x16x32_bf16 v[56:59], v[120:123], a[48:51], v[56:59]\n"
    "ds_read_b64_tr_b16 v[116:117], v216 offset:8960\n"
    "buffer_load_dwordx4 v213, s[84:87], s72 offen offset:3072 lds\n"
    "v_mfma_f32_16x16x32_bf16 v[56:59], v[124:127], a[56:59], v[56:59]\n"
    "ds_read_b64_tr_b16 v[118:119], v216 offset:13056\n"
    "s_waitcnt vmcnt(16)\n"
    "v_mfma_f32_16x16x32_bf16 v[0:3], v[64:67], a[64:67], v[0:3]\n"
    "ds_read_b64_tr_b16 v[120:121], v217 offset:768\n"
    "v_mfma_f32_16x16x32_bf16 v[0:3], v[68:71], a[72:75], v[0:3]\n"
    "ds_read_b64_tr_b16 v[122:123], v217 offset:4864\n"
    "buffer_load_dwordx4 a[112:115], v209, s[52:55], s41 offen offset:2048 sc1\n"
    "v_mfma_f32_16x16x32_bf16 v[4:7], v[64:67], a[68:71], v[4:7]\n"
    "ds_read_b64_tr_b16 v[124:125], v217 offset:8960\n"
    "v_mfma_f32_16x16x32_bf16 v[4:7], v[68:71], a[76:79], v[4:7]\n"
    "ds_read_b64_tr_b16 v[126:127], v217 offset:13056\n"
    "v_mfma_f32_16x16x32_bf16 v[12:15], v[72:75], a[68:71], v[12:15]\n"
    "ds_read_b64_tr_b16 v[64:65], v216 offset:16384\n"
    "buffer_load_dwordx4 a[120:123], v209, s[52:55], s41 offen offset:3072 sc1\n"
    "v_mfma_f32_16x16x32_bf16 v[12:15], v[76:79], a[76:79], v[12:15]\n"
    "ds_read_b64_tr_b16 v[66:67], v216 offset:20480\n"
    "v_mfma_f32_16x16x32_bf16 v[8:11], v[72:75], a[64:67], v[8:11]\n"
    "ds_read_b64_tr_b16 v[68:69], v216 offset:24576\n"
    "v_mfma_f32_16x16x32_bf16 v[8:11], v[76:79], a[72:75], v[8:11]\n"
    "ds_read_b64_tr_b16 v[70:71], v216 offset:28672\n"
    "v_mfma_f32_16x16x32_bf16 v[16:19], v[80:83], a[64:67], v[16:19]\n"
    "ds_read_b64_tr_b16 v[72:73], v217 offset:16384\n"
    "buffer_load_dwordx4 a[116:119], v226, s[52:55], s41 offen offset:2048 sc1\n"
    "v_mfma_f32_16x16x32_bf16 v[16:19], v[84:87], a[72:75], v[16:19]\n"
    "ds_read_b64_tr_b16 v[74:75], v217 offset:20480\n"
    "v_mfma_f32_16x16x32_bf16 v[20:23], v[80:83], a[68:71], v[20:23]\n"
    "ds_read_b64_tr_b16 v[76:77], v217 offset:24576\n"
    "v_mfma_f32_16x16x32_bf16 v[20:23], v[84:87], a[76:79], v[20:23]\n"
    "ds_read_b64_tr_b16 v[78:79], v217 offset:28672\n"
    "buffer_load_dwordx4 a[124:127], v226, s[52:55], s41 offen offset:3072 sc1\n"
    "v_mfma_f32_16x16x32_bf16 v[28:31], v[88:91], a[68:71], v[28:31]\n"
    "ds_read_b64_tr_b16 v[80:81], v216 offset:16640\n"
    "v_mfma_f32_16x16x32_bf16 v[28:31], v[92:95], a[76:79], v[28:31]\n"
    "ds_read_b64_tr_b16 v[82:83], v216 offset:20736\n"
    "v_mfma_f32_16x16x32_bf16 v[24:27], v[88:91], a[64:67], v[24:27]\n"
    "ds_read_b64_tr_b16 v[84:85], v216 offset:24832\n"
    "s_add_u32 m0, s66, 49152\n"
    "v_mfma_f32_16x16x32_bf16 v[24:27], v[92:95], a[72:75], v[24:27]\n"
    "ds_read_b64_tr_b16 v[86:87], v216 offset:28928\n"
    "s_waitcnt lgkmcnt(8)\n"
    "v_mfma_f32_16x16x32_bf16 v[32:35], v[96:99], a[64:67], v[32:35]\n"
    "ds_read_b64_tr_b16 v[88:89], v217 offset:16640\n"
    "v_mfma_f32_16x16x32_bf16 v[32:35], v[100:103], a[72:75], v[32:35]\n"
    "ds_read_b64_tr_b16 v[90:91], v217 offset:20736\n"
    "s_add_u32 s72, s65, 737280\n"
    "v_mfma_f32_16x16x32_bf16 v[36:39], v[96:99], a[68:71], v[36:39]\n"
    "ds_read_b64_tr_b16 v[92:93], v217 offset:24832\n"
    "v_mfma_f32_16x16x32_bf16 v[36:39], v[100:103], a[76:79], v[36:39]\n"
    "ds_read_b64_tr_b16 v[94:95], v217 offset:28928\n"
    "v_mfma_f32_16x16x32_bf16 v[44:47], v[104:107], a[68:71], v[44:47]\n"
    "ds_read_b64_tr_b16 v[96:97], v216 offset:16896\n"
    "buffer_load_dwordx4 v210, s[84:87], s72 offen offset:0 lds\n"
    "v_mfma_f32_16x16x32_bf16 v[44:47], v[108:111], a[76:79], v[44:47]\n"
    "ds_read_b64_tr_b16 v[98:99], v216 offset:20992\n"
    "v_mfma_f32_16x16x32_bf16 v[40:43], v[104:107], a[64:67], v[40:43]\n"
    "ds_read_b64_tr_b16 v[100:101], v216 offset:25088\n"
    "v_mfma_f32_16x16x32_bf16 v[40:43], v[108:111], a[72:75], v[40:43]\n"
    "ds_read_b64_tr_b16 v[102:103], v216 offset:29184\n"
    "v_mfma_f32_16x16x32_bf16 v[48:51], v[112:115], a[64:67], v[48:51]\n"
    "ds_read_b64_tr_b16 v[104:105], v217 offset:16896\n"
    "buffer_load_dwordx4 v211, s[84:87], s72 offen offset:1024 lds\n"
    "v_mfma_f32_16x16x32_bf16 v[48:51], v[116:119], a[72:75], v[48:51]\n"
    "ds_read_b64_tr_b16 v[106:107], v217 offset:20992\n"
    "v_mfma_f32_16x16x32_bf16 v[52:55], v[112:115], a[68:71], v[52:55]\n"
    "ds_read_b64_tr_b16 v[108:109], v217 offset:25088\n"
    "v_mfma_f32_16x16x32_bf16 v[52:55], v[116:119], a[76:79], v[52:55]\n"
    "ds_read_b64_tr_b16 v[110:111], v217 offset:29184\n"
    "buffer_load_dwordx4 v212, s[84:87], s72 offen offset:2048 lds\n"
    "v_mfma_f32_16x16x32_bf16 v[60:63], v[120:123], a[68:71], v[60:63]\n"
    "ds_read_b64_tr_b16 v[112:113], v216 offset:17152\n"
    "v_mfma_f32_16x16x32_bf16 v[60:63], v[124:127], a[76:79], v[60:63]\n"
    "ds_read_b64_tr_b16 v[114:115], v216 offset:21248\n"
    "v_mfma_f32_16x16x32_bf16 v[56:59], v[120:123], a[64:67], v[56:59]\n"
    "ds_read_b64_tr_b16 v[116:117], v216 offset:25344\n"
    "buffer_load_dwordx4 v213, s[84:87], s72 offen offset:3072 lds\n"
    "v_mfma_f32_16x16x32_bf16 v[56:59], v[124:127], a[72:75], v[56:59]\n"
    "ds_read_b64_tr_b16 v[118:119], v216 offset:29440\n"
    "s_waitcnt vmcnt(16)\n"
    "s_barrier\n"
    "v_mfma_f32_16x16x32_bf16 v[0:3], v[64:67], a[80:83], v[0:3]\n"
    "ds_read_b64_tr_b16 v[120:121], v217 offset:17152\n"
    "v_mfma_f32_16x16x32_bf16 v[0:3], v[68:71], a[88:91], v[0:3]\n"
    "ds_read_b64_tr_b16 v[122:123], v217 offset:21248\n"
    "s_add_u32 s41, s40, 16384\n"
    "v_mfma_f32_16x16x32_bf16 v[4:7], v[64:67], a[84:87], v[4:7]\n"
    "ds_read_b64_tr_b16 v[124:125], v217 offset:25344\n"
    "v_mfma_f32_16x16x32_bf16 v[4:7], v[68:71], a[92:95], v[4:7]\n"
    "ds_read_b64_tr_b16 v[126:127], v217 offset:29440\n"
    "s_waitcnt lgkmcnt(8)\n"
    "v_mfma_f32_16x16x32_bf16 v[12:15], v[72:75], a[84:87], v[12:15]\n"
    "ds_read_b64_tr_b16 v[64:65], v214 offset:0\n"
    "buffer_load_dwordx4 a[128:131], v209, s[52:55], s41 offen offset:0 sc1\n"
    "v_mfma_f32_16x16x32_bf16 v[12:15], v[76:79], a[92:95], v[12:15]\n"
    "ds_read_b64_tr_b16 v[66:67], v214 offset:4096\n"
    "v_mfma_f32_16x16x32_bf16 v[8:11], v[72:75], a[80:83], v[8:11]\n"
    "ds_read_b64_tr_b16 v[68:69], v214 offset:8192\n"
    "v_mfma_f32_16x16x32_bf16 v[8:11], v[76:79], a[88:91], v[8:11]\n"
    "ds_read_b64_tr_b16 v[70:71], v214 offset:12288\n"
    "buffer_load_dwordx4 a[136:139], v209, s[52:55], s41 offen offset:1024 sc1\n"
    "v_mfma_f32_16x16x32_bf16 v[16:19], v[80:83], a[80:83], v[16:19]\n"
    "ds_read_b64_tr_b16 v[72:73], v215 offset:0\n"
    "v_mfma_f32_16x16x32_bf16 v[16:19], v[84:87], a[88:91], v[16:19]\n"
    "ds_read_b64_tr_b16 v[74:75], v215 offset:4096\n"
    "v_mfma_f32_16x16x32_bf16 v[20:23], v[80:83], a[84:87], v[20:23]\n"
    "ds_read_b64_tr_b16 v[76:77], v215 offset:8192\n"
    "buffer_load_dwordx4 a[132:135], v226, s[52:55], s41 offen offset:0 sc1\n"
    "v_mfma_f32_16x16x32_bf16 v[20:23], v[84:87], a[92:95], v[20:23]\n"
    "ds_read_b64_tr_b16 v[78:79], v215 offset:12288\n"
    "v_mfma_f32_16x16x32_bf16 v[28:31], v[88:91], a[84:87], v[28:31]\n"
    "ds_read_b64_tr_b16 v[80:81], v214 offset:256\n"
    "v_mfma_f32_16x16x32_bf16 v[28:31], v[92:95], a[92:95], v[28:31]\n"
    "ds_read_b64_tr_b16 v[82:83], v214 offset:4352\n"
    "buffer_load_dwordx4 a[140:143], v226, s[52:55], s41 offen offset:1024 sc1\n"
    "v_mfma_f32_16x16x32_bf16 v[24:27], v[88:91], a[80:83], v[24:27]\n"
    "ds_read_b64_tr_b16 v[84:85], v214 offset:8448\n"
    "v_mfma_f32_16x16x32_bf16 v[24:27], v[92:95], a[88:91], v[24:27]\n"
    "ds_read_b64_tr_b16 v[86:87], v214 offset:12544\n"
    "v_mfma_f32_16x16x32_bf16 v[32:35], v[96:99], a[80:83], v[32:35]\n"
    "ds_read_b64_tr_b16 v[88:89], v215 offset:256\n"
    "s_add_u32 m0, s66, 98304\n"
    "v_mfma_f32_16x16x32_bf16 v[32:35], v[100:103], a[88:91], v[32:35]\n"
    "ds_read_b64_tr_b16 v[90:91], v215 offset:4352\n"
    "v_mfma_f32_16x16x32_bf16 v[36:39], v[96:99], a[84:87], v[36:39]\n"
    "ds_read_b64_tr_b16 v[92:93], v215 offset:8448\n"
    "s_add_u32 s72, s65, 819200\n"
    "v_mfma_f32_16x16x32_bf16 v[36:39], v[100:103], a[92:95], v[36:39]\n"
    "ds_read_b64_tr_b16 v[94:95], v215 offset:12544\n"
    "v_mfma_f32_16x16x32_bf16 v[44:47], v[104:107], a[84:87], v[44:47]\n"
    "ds_read_b64_tr_b16 v[96:97], v214 offset:512\n"
    "v_mfma_f32_16x16x32_bf16 v[44:47], v[108:111], a[92:95], v[44:47]\n"
    "ds_read_b64_tr_b16 v[98:99], v214 offset:4608\n"
    "buffer_load_dwordx4 v210, s[84:87], s72 offen offset:0 lds\n"
    "v_mfma_f32_16x16x32_bf16 v[40:43], v[104:107], a[80:83], v[40:43]\n"
    "ds_read_b64_tr_b16 v[100:101], v214 offset:8704\n"
    "v_mfma_f32_16x16x32_bf16 v[40:43], v[108:111], a[88:91], v[40:43]\n"
    "ds_read_b64_tr_b16 v[102:103], v214 offset:12800\n"
    "s_waitcnt lgkmcnt(8)\n"
    "v_mfma_f32_16x16x32_bf16 v[48:51], v[112:115], a[80:83], v[48:51]\n"
    "ds_read_b64_tr_b16 v[104:105], v215 offset:512\n"
    "buffer_load_dwordx4 v211, s[84:87], s72 offen offset:1024 lds\n"
    "v_mfma_f32_16x16x32_bf16 v[48:51], v[116:119], a[88:91], v[48:51]\n"
    "ds_read_b64_tr_b16 v[106:107], v215 offset:4608\n"
    "v_mfma_f32_16x16x32_bf16 v[52:55], v[112:115], a[84:87], v[52:55]\n"
    "ds_read_b64_tr_b16 v[108:109], v215 offset:8704\n"
    "v_mfma_f32_16x16x32_bf16 v[52:55], v[116:119], a[92:95], v[52:55]\n"
    "ds_read_b64_tr_b16 v[110:111], v215 offset:12800\n"
    "buffer_load_dwordx4 v212, s[84:87], s72 offen offset:2048 lds\n"
    "v_mfma_f32_16x16x32_bf16 v[60:63], v[120:123], a[84:87], v[60:63]\n"
    "v_mfma_f32_16x16x32_bf16 v[60:63], v[124:127], a[92:95], v[60:63]\n"
    "v_mfma_f32_16x16x32_bf16 v[56:59], v[120:123], a[80:83], v[56:59]\n"
    "buffer_load_dwordx4 v213, s[84:87], s72 offen offset:3072 lds\n"
    "v_mfma_f32_16x16x32_bf16 v[56:59], v[124:127], a[88:91], v[56:59]\n"
    "s_waitcnt vmcnt(20)\n"
    "v_mfma_f32_16x16x32_bf16 v[0:3], v[64:67], a[96:99], v[0:3]\n"
    "v_mfma_f32_16x16x32_bf16 v[0:3], v[68:71], a[104:107], v[0:3]\n"
    "buffer_load_dwordx4 a[144:147], v209, s[52:55], s41 offen offset:2048 sc1\n"
    "v_mfma_f32_16x16x32_bf16 v[4:7], v[64:67], a[100:103], v[4:7]\n"
    "v_mfma_f32_16x16x32_bf16 v[4:7], v[68:71], a[108:111], v[4:7]\n"
    "buffer_load_dwordx4 a[152:155], v209, s[52:55], s41 offen offset:3072 sc1\n"
    "v_mfma_f32_16x16x32_bf16 v[12:15], v[72:75], a[100:103], v[12:15]\n"
    "ds_read_b64_tr_b16 v[64:65], v214 offset:16384\n"
    "v_mfma_f32_16x16x32_bf16 v[12:15], v[76:79], a[108:111], v[12:15]\n"
    "ds_read_b64_tr_b16 v[66:67], v214 offset:20480\n"
    "v_mfma_f32_16x16x32_bf16 v[8:11], v[72:75], a[96:99], v[8:11]\n"
    "ds_read_b64_tr_b16 v[68:69], v214 offset:24576\n"
    "buffer_load_dwordx4 a[148:151], v226, s[52:55], s41 offen offset:2048 sc1\n"
    "v_mfma_f32_16x16x32_bf16 v[8:11], v[76:79], a[104:107], v[8:11]\n"
    "ds_read_b64_tr_b16 v[70:71], v214 offset:28672\n"
    "v_mfma_f32_16x16x32_bf16 v[16:19], v[80:83], a[96:99], v[16:19]\n"
    "ds_read_b64_tr_b16 v[72:73], v215 offset:16384\n"
    "buffer_load_dwordx4 a[156:159], v226, s[52:55], s41 offen offset:3072 sc1\n"
    "v_mfma_f32_16x16x32_bf16 v[16:19], v[84:87], a[104:107], v[16:19]\n"
    "ds_read_b64_tr_b16 v[74:75], v215 offset:20480\n"
    "v_mfma_f32_16x16x32_bf16 v[20:23], v[80:83], a[100:103], v[20:23]\n"
    "ds_read_b64_tr_b16 v[76:77], v215 offset:24576\n"
    "s_add_u32 m0, s66, 114688\n"
    "v_mfma_f32_16x16x32_bf16 v[20:23], v[84:87], a[108:111], v[20:23]\n"
    "ds_read_b64_tr_b16 v[78:79], v215 offset:28672\n"
    "s_waitcnt lgkmcnt(8)\n"
    "v_mfma_f32_16x16x32_bf16 v[28:31], v[88:91], a[100:103], v[28:31]\n"
    "ds_read_b64_tr_b16 v[80:81], v214 offset:16640\n"
    "v_mfma_f32_16x16x32_bf16 v[28:31], v[92:95], a[108:111], v[28:31]\n"
    "ds_read_b64_tr_b16 v[82:83], v214 offset:20736\n"
    "s_add_u32 s72, s65, 901120\n"
    "v_mfma_f32_16x16x32_bf16 v[24:27], v[88:91], a[96:99], v[24:27]\n"
    "ds_read_b64_tr_b16 v[84:85], v214 offset:24832\n"
    "v_mfma_f32_16x16x32_bf16 v[24:27], v[92:95], a[104:107], v[24:27]\n"
    "ds_read_b64_tr_b16 v[86:87], v214 offset:28928\n"
    "buffer_load_dwordx4 v210, s[84:87], s72 offen offset:0 lds\n"
    "v_mfma_f32_16x16x32_bf16 v[32:35], v[96:99], a[96:99], v[32:35]\n"
    "ds_read_b64_tr_b16 v[88:89], v215 offset:16640\n"
    "v_mfma_f32_16x16x32_bf16 v[32:35], v[100:103], a[104:107], v[32:35]\n"
    "ds_read_b64_tr_b16 v[90:91], v215 offset:20736\n"
    "v_mfma_f32_16x16x32_bf16 v[36:39], v[96:99], a[100:103], v[36:39]\n"
    "ds_read_b64_tr_b16 v[92:93], v215 offset:24832\n"
    "buffer_load_dwordx4 v211, s[84:87], s72 offen offset:1024 lds\n"
    "v_mfma_f32_16x16x32_bf16 v[36:39], v[100:103], a[108:111], v[36:39]\n"
    "ds_read_b64_tr_b16 v[94:95], v215 offset:28928\n"
    "v_mfma_f32_16x16x32_bf16 v[44:47], v[104:107], a[100:103], v[44:47]\n"
    "ds_read_b64_tr_b16 v[96:97], v214 offset:16896\n"
    "buffer_load_dwordx4 v212, s[84:87], s72 offen offset:2048 lds\n"
    "v_mfma_f32_16x16x32_bf16 v[44:47], v[108:111], a[108:111], v[44:47]\n"
    "ds_read_b64_tr_b16 v[98:99], v214 offset:20992\n"
    "v_mfma_f32_16x16x32_bf16 v[40:43], v[104:107], a[96:99], v[40:43]\n"
    "ds_read_b64_tr_b16 v[100:101], v214 offset:25088\n"
    "buffer_load_dwordx4 v213, s[84:87], s72 offen offset:3072 lds\n"
    "v_mfma_f32_16x16x32_bf16 v[40:43], v[108:111], a[104:107], v[40:43]\n"
    "ds_read_b64_tr_b16 v[102:103], v214 offset:29184\n"
    "s_waitcnt vmcnt(16)\n"
    "s_barrier\n"
    "s_waitcnt lgkmcnt(7)\n"
    "v_mfma_f32_16x16x32_bf16 v[0:3], v[64:67], a[112:115], v[0:3]\n"
    "ds_read_b64_tr_b16 v[104:105], v215 offset:16896\n"
    "v_mfma_f32_16x16x32_bf16 v[0:3], v[68:71], a[120:123], v[0:3]\n"
    "ds_read_b64_tr_b16 v[106:107], v215 offset:20992\n"
    "s_add_u32 s41, s40, 20480\n"
    "v_mfma_f32_16x16x32_bf16 v[4:7], v[64:67], a[116:119], v[4:7]\n"
    "ds_read_b64_tr_b16 v[108:109], v215 offset:25088\n"
    "v_mfma_f32_16x16x32_bf16 v[4:7], v[68:71], a[124:127], v[4:7]\n"
    "ds_read_b64_tr_b16 v[110:111], v215 offset:29184\n"
    "buffer_load_dwordx4 a[160:163], v209, s[52:55], s41 offen offset:0 sc1\n"
    "v_mfma_f32_16x16x32_bf16 v[12:15], v[72:75], a[116:119], v[12:15]\n"
    "ds_read_b64_tr_b16 v[64:65], v214 offset:32768\n"
    "v_mfma_f32_16x16x32_bf16 v[12:15], v[76:79], a[124:127], v[12:15]\n"
    "ds_read_b64_tr_b16 v[66:67], v214 offset:36864\n"
    "buffer_load_dwordx4 a[168:171], v209, s[52:55], s41 offen offset:1024 sc1\n"
    "v_mfma_f32_16x16x32_bf16 v[8:11], v[72:75], a[112:115], v[8:11]\n"
    "ds_read_b64_tr_b16 v[68:69], v214 offset:40960\n"
    "v_mfma_f32_16x16x32_bf16 v[8:11], v[76:79], a[120:123], v[8:11]\n"
    "ds_read_b64_tr_b16 v[70:71], v214 offset:45056\n"
    "buffer_load_dwordx4 a[164:167], v226, s[52:55], s41 offen offset:0 sc1\n"
    "v_mfma_f32_16x16x32_bf16 v[16:19], v[80:83], a[112:115], v[16:19]\n"
    "ds_read_b64_tr_b16 v[72:73], v215 offset:32768\n"
    "v_mfma_f32_16x16x32_bf16 v[16:19], v[84:87], a[120:123], v[16:19]\n"
    "ds_read_b64_tr_b16 v[74:75], v215 offset:36864\n"
    "buffer_load_dwordx4 a[172:175], v226, s[52:55], s41 offen offset:1024 sc1\n"
    "v_mfma_f32_16x16x32_bf16 v[20:23], v[80:83], a[116:119], v[20:23]\n"
    "ds_read_b64_tr_b16 v[76:77], v215 offset:40960\n"
    "v_mfma_f32_16x16x32_bf16 v[20:23], v[84:87], a[124:127], v[20:23]\n"
    "ds_read_b64_tr_b16 v[78:79], v215 offset:45056\n"
    "s_waitcnt lgkmcnt(8)\n"
    "v_mfma_f32_16x16x32_bf16 v[28:31], v[88:91], a[116:119], v[28:31]\n"
    "ds_read_b64_tr_b16 v[80:81], v214 offset:33024\n"
    "s_add_u32 m0, s66, 131072\n"
    "v_mfma_f32_16x16x32_bf16 v[28:31], v[92:95], a[124:127], v[28:31]\n"
    "ds_read_b64_tr_b16 v[82:83], v214 offset:37120\n"
    "v_mfma_f32_16x16x32_bf16 v[24:27], v[88:91], a[112:115], v[24:27]\n"
    "ds_read_b64_tr_b16 v[84:85], v214 offset:41216\n"
    "s_add_u32 s72, s65, 983040\n"
    "v_mfma_f32_16x16x32_bf16 v[24:27], v[92:95], a[120:123], v[24:27]\n"
    "ds_read_b64_tr_b16 v[86:87], v214 offset:45312\n"
    "v_mfma_f32_16x16x32_bf16 v[32:35], v[96:99], a[112:115], v[32:35]\n"
    "ds_read_b64_tr_b16 v[88:89], v215 offset:33024\n"
    "buffer_load_dwordx4 v210, s[84:87], s72 offen offset:0 lds\n"
    "v_mfma_f32_16x16x32_bf16 v[32:35], v[100:103], a[120:123], v[32:35]\n"
    "ds_read_b64_tr_b16 v[90:91], v215 offset:37120\n"
    "v_mfma_f32_16x16x32_bf16 v[36:39], v[96:99], a[116:119], v[36:39]\n"
    "ds_read_b64_tr_b16 v[92:93], v215 offset:41216\n"
    "buffer_load_dwordx4 v211, s[84:87], s72 offen offset:1024 lds\n"
    "v_mfma_f32_16x16x32_bf16 v[36:39], v[100:103], a[124:127], v[36:39]\n"
    "ds_read_b64_tr_b16 v[94:95], v215 offset:45312\n"
    "v_mfma_f32_16x16x32_bf16 v[44:47], v[104:107], a[116:119], v[44:47]\n"
    "ds_read_b64_tr_b16 v[96:97], v214 offset:33280\n"
    "buffer_load_dwordx4 v212, s[84:87], s72 offen offset:2048 lds\n"
    "v_mfma_f32_16x16x32_bf16 v[44:47], v[108:111], a[124:127], v[44:47]\n"
    "ds_read_b64_tr_b16 v[98:99], v214 offset:37376\n"
    "v_mfma_f32_16x16x32_bf16 v[40:43], v[104:107], a[112:115], v[40:43]\n"
    "ds_read_b64_tr_b16 v[100:101], v214 offset:41472\n"
    "buffer_load_dwordx4 v213, s[84:87], s72 offen offset:3072 lds\n"
    "v_mfma_f32_16x16x32_bf16 v[40:43], v[108:111], a[120:123], v[40:43]\n"
    "ds_read_b64_tr_b16 v[102:103], v214 offset:45568\n"
    "s_waitcnt vmcnt(20)\n"
    "s_waitcnt lgkmcnt(7)\n"
    "v_mfma_f32_16x16x32_bf16 v[0:3], v[64:67], a[128:131], v[0:3]\n"
    "ds_read_b64_tr_b16 v[104:105], v215 offset:33280\n"
    "v_mfma_f32_16x16x32_bf16 v[0:3], v[68:71], a[136:139], v[0:3]\n"
    "ds_read_b64_tr_b16 v[106:107], v215 offset:37376\n"
    "buffer_load_dwordx4 a[176:179], v209, s[52:55], s41 offen offset:2048 sc1\n"
    "v_mfma_f32_16x16x32_bf16 v[4:7], v[64:67], a[132:135], v[4:7]\n"
    "ds_read_b64_tr_b16 v[108:109], v215 offset:41472\n"
    "v_mfma_f32_16x16x32_bf16 v[4:7], v[68:71], a[140:143], v[4:7]\n"
    "ds_read_b64_tr_b16 v[110:111], v215 offset:45568\n"
    "buffer_load_dwordx4 a[184:187], v209, s[52:55], s41 offen offset:3072 sc1\n"
    "v_mfma_f32_16x16x32_bf16 v[12:15], v[72:75], a[132:135], v[12:15]\n"
    "ds_read_b64_tr_b16 v[64:65], v214 offset:49152\n"
    "v_mfma_f32_16x16x32_bf16 v[12:15], v[76:79], a[140:143], v[12:15]\n"
    "ds_read_b64_tr_b16 v[66:67], v214 offset:53248\n"
    "v_mfma_f32_16x16x32_bf16 v[8:11], v[72:75], a[128:131], v[8:11]\n"
    "ds_read_b64_tr_b16 v[68:69], v214 offset:57344\n"
    "buffer_load_dwordx4 a[180:183], v226, s[52:55], s41 offen offset:2048 sc1\n"
    "v_mfma_f32_16x16x32_bf16 v[8:11], v[76:79], a[136:139], v[8:11]\n"
    "ds_read_b64_tr_b16 v[70:71], v214 offset:61440\n"
    "v_mfma_f32_16x16x32_bf16 v[16:19], v[80:83], a[128:131], v[16:19]\n"
    "ds_read_b64_tr_b16 v[72:73], v215 offset:49152\n"
    "buffer_load_dwordx4 a[188:191], v226, s[52:55], s41 offen offset:3072 sc1\n"
    "v_mfma_f32_16x16x32_bf16 v[16:19], v[84:87], a[136:139], v[16:19]\n"
    "ds_read_b64_tr_b16 v[74:75], v215 offset:53248\n"
    "v_mfma_f32_16x16x32_bf16 v[20:23], v[80:83], a[132:135], v[20:23]\n"
    "ds_read_b64_tr_b16 v[76:77], v215 offset:57344\n"
    "s_add_u32 m0, s66, 147456\n"
    "v_mfma_f32_16x16x32_bf16 v[20:23], v[84:87], a[140:143], v[20:23]\n"
    "ds_read_b64_tr_b16 v[78:79], v215 offset:61440\n"
    "s_waitcnt lgkmcnt(8)\n"
    "v_mfma_f32_16x16x32_bf16 v[28:31], v[88:91], a[132:135], v[28:31]\n"
    "ds_read_b64_tr_b16 v[80:81], v214 offset:49408\n"
    "v_mfma_f32_16x16x32_bf16 v[28:31], v[92:95], a[140:143], v[28:31]\n"
    "ds_read_b64_tr_b16 v[82:83], v214 offset:53504\n"
    "s_add_u32 s72, s65, 1064960\n"
    "v_mfma_f32_16x16x32_bf16 v[24:27], v[88:91], a[128:131], v[24:27]\n"
    "ds_read_b64_tr_b16 v[84:85], v214 offset:57600\n"
    "v_mfma_f32_16x16x32_bf16 v[24:27], v[92:95], a[136:139], v[24:27]\n"
    "ds_read_b64_tr_b16 v[86:87], v214 offset:61696\n"
    "buffer_load_dwordx4 v210, s[84:87], s72 offen offset:0 lds\n"
    "v_mfma_f32_16x16x32_bf16 v[32:35], v[96:99], a[128:131], v[32:35]\n"
    "ds_read_b64_tr_b16 v[88:89], v215 offset:49408\n"
    "v_mfma_f32_16x16x32_bf16 v[32:35], v[100:103], a[136:139], v[32:35]\n"
    "ds_read_b64_tr_b16 v[90:91], v215 offset:53504\n"
    "v_mfma_f32_16x16x32_bf16 v[36:39], v[96:99], a[132:135], v[36:39]\n"
    "ds_read_b64_tr_b16 v[92:93], v215 offset:57600\n"
    "buffer_load_dwordx4 v211, s[84:87], s72 offen offset:1024 lds\n"
    "v_mfma_f32_16x16x32_bf16 v[36:39], v[100:103], a[140:143], v[36:39]\n"
    "ds_read_b64_tr_b16 v[94:95], v215 offset:61696\n"
    "v_mfma_f32_16x16x32_bf16 v[44:47], v[104:107], a[132:135], v[44:47]\n"
    "ds_read_b64_tr_b16 v[96:97], v214 offset:49664\n"
    "buffer_load_dwordx4 v212, s[84:87], s72 offen offset:2048 lds\n"
    "v_mfma_f32_16x16x32_bf16 v[44:47], v[108:111], a[140:143], v[44:47]\n"
    "ds_read_b64_tr_b16 v[98:99], v214 offset:53760\n"
    "v_mfma_f32_16x16x32_bf16 v[40:43], v[104:107], a[128:131], v[40:43]\n"
    "ds_read_b64_tr_b16 v[100:101], v214 offset:57856\n"
    "buffer_load_dwordx4 v213, s[84:87], s72 offen offset:3072 lds\n"
    "v_mfma_f32_16x16x32_bf16 v[40:43], v[108:111], a[136:139], v[40:43]\n"
    "ds_read_b64_tr_b16 v[102:103], v214 offset:61952\n"
    "s_waitcnt vmcnt(16)\n"
    "s_barrier\n"
    "s_waitcnt lgkmcnt(7)\n"
    "v_mfma_f32_16x16x32_bf16 v[0:3], v[64:67], a[144:147], v[0:3]\n"
    "ds_read_b64_tr_b16 v[104:105], v215 offset:49664\n"
    "v_mfma_f32_16x16x32_bf16 v[0:3], v[68:71], a[152:155], v[0:3]\n"
    "ds_read_b64_tr_b16 v[106:107], v215 offset:53760\n"
    "s_add_u32 s41, s40, 24576\n"
    "v_mfma_f32_16x16x32_bf16 v[4:7], v[64:67], a[148:151], v[4:7]\n"
    "ds_read_b64_tr_b16 v[108:109], v215 offset:57856\n"
    "v_mfma_f32_16x16x32_bf16 v[4:7], v[68:71], a[156:159], v[4:7]\n"
    "ds_read_b64_tr_b16 v[110:111], v215 offset:61952\n"
    "buffer_load_dwordx4 a[192:195], v209, s[52:55], s41 offen offset:0 sc1\n"
    "v_mfma_f32_16x16x32_bf16 v[12:15], v[72:75], a[148:151], v[12:15]\n"
    "ds_read_b64_tr_b16 v[64:65], v216 offset:32768\n"
    "v_mfma_f32_16x16x32_bf16 v[12:15], v[76:79], a[156:159], v[12:15]\n"
    "ds_read_b64_tr_b16 v[66:67], v216 offset:36864\n"
    "buffer_load_dwordx4 a[200:203], v209, s[52:55], s41 offen offset:1024 sc1\n"
    "v_mfma_f32_16x16x32_bf16 v[8:11], v[72:75], a[144:147], v[8:11]\n"
    "ds_read_b64_tr_b16 v[68:69], v216 offset:40960\n"
    "v_mfma_f32_16x16x32_bf16 v[8:11], v[76:79], a[152:155], v[8:11]\n"
    "ds_read_b64_tr_b16 v[70:71], v216 offset:45056\n"
    "buffer_load_dwordx4 a[196:199], v226, s[52:55], s41 offen offset:0 sc1\n"
    "v_mfma_f32_16x16x32_bf16 v[16:19], v[80:83], a[144:147], v[16:19]\n"
    "ds_read_b64_tr_b16 v[72:73], v217 offset:32768\n"
    "v_mfma_f32_16x16x32_bf16 v[16:19], v[84:87], a[152:155], v[16:19]\n"
    "ds_read_b64_tr_b16 v[74:75], v217 offset:36864\n"
    "buffer_load_dwordx4 a[204:207], v226, s[52:55], s41 offen offset:1024 sc1\n"
    "v_mfma_f32_16x16x32_bf16 v[20:23], v[80:83], a[148:151], v[20:23]\n"
    "ds_read_b64_tr_b16 v[76:77], v217 offset:40960\n"
    "v_mfma_f32_16x16x32_bf16 v[20:23], v[84:87], a[156:159], v[20:23]\n"
    "ds_read_b64_tr_b16 v[78:79], v217 offset:45056\n"
    "s_waitcnt lgkmcnt(8)\n"
    "v_mfma_f32_16x16x32_bf16 v[28:31], v[88:91], a[148:151], v[28:31]\n"
    "ds_read_b64_tr_b16 v[80:81], v216 offset:33024\n"
    "s_add_u32 m0, s66, 65536\n"
    "v_mfma_f32_16x16x32_bf16 v[28:31], v[92:95], a[156:159], v[28:31]\n"
    "ds_read_b64_tr_b16 v[82:83], v216 offset:37120\n"
    "v_mfma_f32_16x16x32_bf16 v[24:27], v[88:91], a[144:147], v[24:27]\n"
    "ds_read_b64_tr_b16 v[84:85], v216 offset:41216\n"
    "s_add_u32 s72, s65, 1146880\n"
    "v_mfma_f32_16x16x32_bf16 v[24:27], v[92:95], a[152:155], v[24:27]\n"
    "ds_read_b64_tr_b16 v[86:87], v216 offset:45312\n"
    "v_mfma_f32_16x16x32_bf16 v[32:35], v[96:99], a[144:147], v[32:35]\n"
    "ds_read_b64_tr_b16 v[88:89], v217 offset:33024\n"
    "buffer_load_dwordx4 v210, s[84:87], s72 offen offset:0 lds\n"
    "v_mfma_f32_16x16x32_bf16 v[32:35], v[100:103], a[152:155], v[32:35]\n"
    "ds_read_b64_tr_b16 v[90:91], v217 offset:37120\n"
    "v_mfma_f32_16x16x32_bf16 v[36:39], v[96:99], a[148:151], v[36:39]\n"
    "ds_read_b64_tr_b16 v[92:93], v217 offset:41216\n"
    "buffer_load_dwordx4 v211, s[84:87], s72 offen offset:1024 lds\n"
    "v_mfma_f32_16x16x32_bf16 v[36:39], v[100:103], a[156:159], v[36:39]\n"
    "ds_read_b64_tr_b16 v[94:95], v217 offset:45312\n"
    "v_mfma_f32_16x16x32_bf16 v[44:47], v[104:107], a[148:151], v[44:47]\n"
    "ds_read_b64_tr_b16 v[96:97], v216 offset:33280\n"
    "buffer_load_dwordx4 v212, s[84:87], s72 offen offset:2048 lds\n"
    "v_mfma_f32_16x16x32_bf16 v[44:47], v[108:111], a[156:159], v[44:47]\n"
    "ds_read_b64_tr_b16 v[98:99], v216 offset:37376\n"
    "v_mfma_f32_16x16x32_bf16 v[40:43], v[104:107], a[144:147], v[40:43]\n"
    "ds_read_b64_tr_b16 v[100:101], v216 offset:41472\n"
    "buffer_load_dwordx4 v213, s[84:87], s72 offen offset:3072 lds\n"
    "v_mfma_f32_16x16x32_bf16 v[40:43], v[108:111], a[152:155], v[40:43]\n"
    "ds_read_b64_tr_b16 v[102:103], v216 offset:45568\n"
    "s_waitcnt vmcnt(20)\n"
    "s_waitcnt lgkmcnt(7)\n"
    "v_mfma_f32_16x16x32_bf16 v[0:3], v[64:67], a[160:163], v[0:3]\n"
    "ds_read_b64_tr_b16 v[104:105], v217 offset:33280\n"
    "v_mfma_f32_16x16x32_bf16 v[0:3], v[68:71], a[168:171], v[0:3]\n"
    "ds_read_b64_tr_b16 v[106:107], v217 offset:37376\n"
    "buffer_load_dwordx4 a[208:211], v209, s[52:55], s41 offen offset:2048 sc1\n"
    "v_mfma_f32_16x16x32_bf16 v[4:7], v[64:67], a[164:167], v[4:7]\n"
    "ds_read_b64_tr_b16 v[108:109], v217 offset:41472\n"
    "v_mfma_f32_16x16x32_bf16 v[4:7], v[68:71], a[172:175], v[4:7]\n"
    "ds_read_b64_tr_b16 v[110:111], v217 offset:45568\n"
    "buffer_load_dwordx4 a[216:219], v209, s[52:55], s41 offen offset:3072 sc1\n"
    "v_mfma_f32_16x16x32_bf16 v[12:15], v[72:75], a[164:167], v[12:15]\n"
    "ds_read_b64_tr_b16 v[64:65], v216 offset:49152\n"
    "v_mfma_f32_16x16x32_bf16 v[12:15], v[76:79], a[172:175], v[12:15]\n"
    "ds_read_b64_tr_b16 v[66:67], v216 offset:53248\n"
    "v_mfma_f32_16x16x32_bf16 v[8:11], v[72:75], a[160:163], v[8:11]\n"
    "ds_read_b64_tr_b16 v[68:69], v216 offset:57344\n"
    "buffer_load_dwordx4 a[212:215], v226, s[52:55], s41 offen offset:2048 sc1\n"
    "v_mfma_f32_16x16x32_bf16 v[8:11], v[76:79], a[168:171], v[8:11]\n"
    "ds_read_b64_tr_b16 v[70:71], v216 offset:61440\n"
    "v_mfma_f32_16x16x32_bf16 v[16:19], v[80:83], a[160:163], v[16:19]\n"
    "ds_read_b64_tr_b16 v[72:73], v217 offset:49152\n"
    "buffer_load_dwordx4 a[220:223], v226, s[52:55], s41 offen offset:3072 sc1\n"
    "v_mfma_f32_16x16x32_bf16 v[16:19], v[84:87], a[168:171], v[16:19]\n"
    "ds_read_b64_tr_b16 v[74:75], v217 offset:53248\n"
    "v_mfma_f32_16x16x32_bf16 v[20:23], v[80:83], a[164:167], v[20:23]\n"
    "ds_read_b64_tr_b16 v[76:77], v217 offset:57344\n"
    "s_add_u32 m0, s66, 81920\n"
    "v_mfma_f32_16x16x32_bf16 v[20:23], v[84:87], a[172:175], v[20:23]\n"
    "ds_read_b64_tr_b16 v[78:79], v217 offset:61440\n"
    "s_waitcnt lgkmcnt(8)\n"
    "v_mfma_f32_16x16x32_bf16 v[28:31], v[88:91], a[164:167], v[28:31]\n"
    "ds_read_b64_tr_b16 v[80:81], v216 offset:49408\n"
    "v_mfma_f32_16x16x32_bf16 v[28:31], v[92:95], a[172:175], v[28:31]\n"
    "ds_read_b64_tr_b16 v[82:83], v216 offset:53504\n"
    "s_add_u32 s72, s65, 1228800\n"
    "v_mfma_f32_16x16x32_bf16 v[24:27], v[88:91], a[160:163], v[24:27]\n"
    "ds_read_b64_tr_b16 v[84:85], v216 offset:57600\n"
    "v_mfma_f32_16x16x32_bf16 v[24:27], v[92:95], a[168:171], v[24:27]\n"
    "ds_read_b64_tr_b16 v[86:87], v216 offset:61696\n"
    "buffer_load_dwordx4 v210, s[84:87], s72 offen offset:0 lds\n"
    "v_mfma_f32_16x16x32_bf16 v[32:35], v[96:99], a[160:163], v[32:35]\n"
    "ds_read_b64_tr_b16 v[88:89], v217 offset:49408\n"
    "v_mfma_f32_16x16x32_bf16 v[32:35], v[100:103], a[168:171], v[32:35]\n"
    "ds_read_b64_tr_b16 v[90:91], v217 offset:53504\n"
    "v_mfma_f32_16x16x32_bf16 v[36:39], v[96:99], a[164:167], v[36:39]\n"
    "ds_read_b64_tr_b16 v[92:93], v217 offset:57600\n"
    "buffer_load_dwordx4 v211, s[84:87], s72 offen offset:1024 lds\n"
    "v_mfma_f32_16x16x32_bf16 v[36:39], v[100:103], a[172:175], v[36:39]\n"
    "ds_read_b64_tr_b16 v[94:95], v217 offset:61696\n"
    "v_mfma_f32_16x16x32_bf16 v[44:47], v[104:107], a[164:167], v[44:47]\n"
    "ds_read_b64_tr_b16 v[96:97], v216 offset:49664\n"
    "buffer_load_dwordx4 v212, s[84:87], s72 offen offset:2048 lds\n"
    "v_mfma_f32_16x16x32_bf16 v[44:47], v[108:111], a[172:175], v[44:47]\n"
    "ds_read_b64_tr_b16 v[98:99], v216 offset:53760\n"
    "v_mfma_f32_16x16x32_bf16 v[40:43], v[104:107], a[160:163], v[40:43]\n"
    "ds_read_b64_tr_b16 v[100:101], v216 offset:57856\n"
    "buffer_load_dwordx4 v213, s[84:87], s72 offen offset:3072 lds\n"
    "v_mfma_f32_16x16x32_bf16 v[40:43], v[108:111], a[168:171], v[40:43]\n"
    "ds_read_b64_tr_b16 v[102:103], v216 offset:61952\n"
    "s_waitcnt vmcnt(16)\n"
    "s_barrier\n"
    "s_waitcnt lgkmcnt(7)\n"
    "v_mfma_f32_16x16x32_bf16 v[0:3], v[64:67], a[176:179], v[0:3]\n"
    "ds_read_b64_tr_b16 v[104:105], v217 offset:49664\n"
    "v_mfma_f32_16x16x32_bf16 v[0:3], v[68:71], a[184:187], v[0:3]\n"
    "ds_read_b64_tr_b16 v[106:107], v217 offset:53760\n"
    "v_mfma_f32_16x16x32_bf16 v[4:7], v[64:67], a[180:183], v[4:7]\n"
    "ds_read_b64_tr_b16 v[108:109], v217 offset:57856\n"
    "s_add_u32 s41, s40, 28672\n"
    "v_mfma_f32_16x16x32_bf16 v[4:7], v[68:71], a[188:191], v[4:7]\n"
    "ds_read_b64_tr_b16 v[110:111], v217 offset:61952\n"
    "v_mfma_f32_16x16x32_bf16 v[12:15], v[72:75], a[180:183], v[12:15]\n"
    "ds_read_b64_tr_b16 v[64:65], v218 offset:0\n"
    "v_mfma_f32_16x16x32_bf16 v[12:15], v[76:79], a[188:191], v[12:15]\n"
    "ds_read_b64_tr_b16 v[66:67], v218 offset:4096\n"
    "v_mfma_f32_16x16x32_bf16 v[8:11], v[72:75], a[176:179], v[8:11]\n"
    "ds_read_b64_tr_b16 v[68:69], v218 offset:8192\n"
    "v_mfma_f32_16x16x32_bf16 v[8:11], v[76:79], a[184:187], v[8:11]\n"
    "ds_read_b64_tr_b16 v[70:71], v218 offset:12288\n"
    "buffer_load_dwordx4 a[224:227], v209, s[52:55], s41 offen offset:0 sc1\n"
    "v_mfma_f32_16x16x32_bf16 v[16:19], v[80:83], a[176:179], v[16:19]\n"
    "ds_read_b64_tr_b16 v[72:73], v219 offset:0\n"
    "v_mfma_f32_16x16x32_bf16 v[16:19], v[84:87], a[184:187], v[16:19]\n"
    "ds_read_b64_tr_b16 v[74:75], v219 offset:4096\n"
    "v_mfma_f32_16x16x32_bf16 v[20:23], v[80:83], a[180:183], v[20:23]\n"
    "ds_read_b64_tr_b16 v[76:77], v219 offset:8192\n"
    "v_mfma_f32_16x16x32_bf16 v[20:23], v[84:87], a[188:191], v[20:23]\n"
    "ds_read_b64_tr_b16 v[78:79], v219 offset:12288\n"
    "s_waitcnt lgkmcnt(9)\n"
    "v_mfma_f32_16x16x32_bf16 v[28:31], v[88:91], a[180:183], v[28:31]\n"
    "ds_read_b64_tr_b16 v[80:81], v218 offset:256\n"
    "buffer_load_dwordx4 a[232:235], v209, s[52:55], s41 offen offset:1024 sc1\n"
    "v_mfma_f32_16x16x32_bf16 v[28:31], v[92:95], a[188:191], v[28:31]\n"
    "ds_read_b64_tr_b16 v[82:83], v218 offset:4352\n"
    "v_mfma_f32_16x16x32_bf16 v[24:27], v[88:91], a[176:179], v[24:27]\n"
    "ds_read_b64_tr_b16 v[84:85], v218 offset:8448\n"
    "v_mfma_f32_16x16x32_bf16 v[24:27], v[92:95], a[184:187], v[24:27]\n"
    "ds_read_b64_tr_b16 v[86:87], v218 offset:12544\n"
    "v_mfma_f32_16x16x32_bf16 v[32:35], v[96:99], a[176:179], v[32:35]\n"
    "ds_read_b64_tr_b16 v[88:89], v219 offset:256\n"
    "buffer_load_dwordx4 a[228:231], v226, s[52:55], s41 offen offset:0 sc1\n"
    "v_mfma_f32_16x16x32_bf16 v[32:35], v[100:103], a[184:187], v[32:35]\n"
    "ds_read_b64_tr_b16 v[90:91], v219 offset:4352\n"
    "v_mfma_f32_16x16x32_bf16 v[36:39], v[96:99], a[180:183], v[36:39]\n"
    "ds_read_b64_tr_b16 v[92:93], v219 offset:8448\n"
    "v_mfma_f32_16x16x32_bf16 v[36:39], v[100:103], a[188:191], v[36:39]\n"
    "ds_read_b64_tr_b16 v[94:95], v219 offset:12544\n"
    "v_mfma_f32_16x16x32_bf16 v[44:47], v[104:107], a[180:183], v[44:47]\n"
    "ds_read_b64_tr_b16 v[96:97], v218 offset:512\n"
    "s_waitcnt lgkmcnt(9)\n"
    "v_mfma_f32_16x16x32_bf16 v[44:47], v[108:111], a[188:191], v[44:47]\n"
    "ds_read_b64_tr_b16 v[98:99], v218 offset:4608\n"
    "buffer_load_dwordx4 a[236:239], v226, s[52:55], s41 offen offset:1024 sc1\n"
    "v_mfma_f32_16x16x32_bf16 v[40:43], v[104:107], a[176:179], v[40:43]\n"
    "ds_read_b64_tr_b16 v[100:101], v218 offset:8704\n"
    "v_mfma_f32_16x16x32_bf16 v[40:43], v[108:111], a[184:187], v[40:43]\n"
    "ds_read_b64_tr_b16 v[102:103], v218 offset:12800\n"
    "s_waitcnt vmcnt(16)\n"
    "v_mfma_f32_16x16x32_bf16 v[0:3], v[64:67], a[192:195], v[0:3]\n"
    "ds_read_b64_tr_b16 v[104:105], v219 offset:512\n"
    "v_mfma_f32_16x16x32_bf16 v[0:3], v[68:71], a[200:203], v[0:3]\n"
    "ds_read_b64_tr_b16 v[106:107], v219 offset:4608\n"
    "v_mfma_f32_16x16x32_bf16 v[4:7], v[64:67], a[196:199], v[4:7]\n"
    "ds_read_b64_tr_b16 v[108:109], v219 offset:8704\n"
    "v_mfma_f32_16x16x32_bf16 v[4:7], v[68:71], a[204:207], v[4:7]\n"
    "ds_read_b64_tr_b16 v[110:111], v219 offset:12800\n"
    "buffer_load_dwordx4 a[240:243], v209, s[52:55], s41 offen offset:2048 sc1\n"
    "v_mfma_f32_16x16x32_bf16 v[12:15], v[72:75], a[196:199], v[12:15]\n"
    "ds_read_b64_tr_b16 v[64:65], v218 offset:16384\n"
    "v_mfma_f32_16x16x32_bf16 v[12:15], v[76:79], a[204:207], v[12:15]\n"
    "ds_read_b64_tr_b16 v[66:67], v218 offset:20480\n"
    "v_mfma_f32_16x16x32_bf16 v[8:11], v[72:75], a[192:195], v[8:11]\n"
    "ds_read_b64_tr_b16 v[68:69], v218 offset:24576\n"
    "v_mfma_f32_16x16x32_bf16 v[8:11], v[76:79], a[200:203], v[8:11]\n"
    "ds_read_b64_tr_b16 v[70:71], v218 offset:28672\n"
    "s_waitcnt lgkmcnt(9)\n"
    "v_mfma_f32_16x16x32_bf16 v[16:19], v[80:83], a[192:195], v[16:19]\n"
    "ds_read_b64_tr_b16 v[72:73], v219 offset:16384\n"
    "v_mfma_f32_16x16x32_bf16 v[16:19], v[84:87], a[200:203], v[16:19]\n"
    "ds_read_b64_tr_b16 v[74:75], v219 offset:20480\n"
    "buffer_load_dwordx4 a[248:251], v209, s[52:55], s41 offen offset:3072 sc1\n"
    "v_mfma_f32_16x16x32_bf16 v[20:23], v[80:83], a[196:199], v[20:23]\n"
    "ds_read_b64_tr_b16 v[76:77], v219 offset:24576\n"
    "v_mfma_f32_16x16x32_bf16 v[20:23], v[84:87], a[204:207], v[20:23]\n"
    "ds_read_b64_tr_b16 v[78:79], v219 offset:28672\n"
    "v_mfma_f32_16x16x32_bf16 v[28:31], v[88:91], a[196:199], v[28:31]\n"
    "ds_read_b64_tr_b16 v[80:81], v218 offset:16640\n"
    "v_mfma_f32_16x16x32_bf16 v[28:31], v[92:95], a[204:207], v[28:31]\n"
    "ds_read_b64_tr_b16 v[82:83], v218 offset:20736\n"
    "v_mfma_f32_16x16x32_bf16 v[24:27], v[88:91], a[192:195], v[24:27]\n"
    "ds_read_b64_tr_b16 v[84:85], v218 offset:24832\n"
    "v_mfma_f32_16x16x32_bf16 v[24:27], v[92:95], a[200:203], v[24:27]\n"
    "ds_read_b64_tr_b16 v[86:87], v218 offset:28928\n"
    "buffer_load_dwordx4 a[244:247], v226, s[52:55], s41 offen offset:2048 sc1\n"
    "v_mfma_f32_16x16x32_bf16 v[32:35], v[96:99], a[192:195], v[32:35]\n"
    "ds_read_b64_tr_b16 v[88:89], v219 offset:16640\n"
    "s_waitcnt lgkmcnt(9)\n"
    "v_mfma_f32_16x16x32_bf16 v[32:35], v[100:103], a[200:203], v[32:35]\n"
    "ds_read_b64_tr_b16 v[90:91], v219 offset:20736\n"
    "v_mfma_f32_16x16x32_bf16 v[36:39], v[96:99], a[196:199], v[36:39]\n"
    "ds_read_b64_tr_b16 v[92:93], v219 offset:24832\n"
    "v_mfma_f32_16x16x32_bf16 v[36:39], v[100:103], a[204:207], v[36:39]\n"
    "ds_read_b64_tr_b16 v[94:95], v219 offset:28928\n"
    "v_mfma_f32_16x16x32_bf16 v[44:47], v[104:107], a[196:199], v[44:47]\n"
    "ds_read_b64_tr_b16 v[96:97], v218 offset:16896\n"
    "v_mfma_f32_16x16x32_bf16 v[44:47], v[108:111], a[204:207], v[44:47]\n"
    "ds_read_b64_tr_b16 v[98:99], v218 offset:20992\n"
    "buffer_load_dwordx4 a[252:255], v226, s[52:55], s41 offen offset:3072 sc1\n"
    "v_mfma_f32_16x16x32_bf16 v[40:43], v[104:107], a[192:195], v[40:43]\n"
    "ds_read_b64_tr_b16 v[100:101], v218 offset:25088\n"
    "v_mfma_f32_16x16x32_bf16 v[40:43], v[108:111], a[200:203], v[40:43]\n"
    "ds_read_b64_tr_b16 v[102:103], v218 offset:29184\n"
    "s_waitcnt vmcnt(8)\n"
    "s_barrier\n"
    "v_mfma_f32_16x16x32_bf16 v[0:3], v[64:67], a[208:211], v[0:3]\n"
    "ds_read_b64_tr_b16 v[104:105], v219 offset:16896\n"
    "v_mfma_f32_16x16x32_bf16 v[0:3], v[68:71], a[216:219], v[0:3]\n"
    "ds_read_b64_tr_b16 v[106:107], v219 offset:20992\n"
    "v_mfma_f32_16x16x32_bf16 v[4:7], v[64:67], a[212:215], v[4:7]\n"
    "ds_read_b64_tr_b16 v[108:109], v219 offset:25088\n"
    "v_mfma_f32_16x16x32_bf16 v[4:7], v[68:71], a[220:223], v[4:7]\n"
    "ds_read_b64_tr_b16 v[110:111], v219 offset:29184\n"
    "s_waitcnt lgkmcnt(8)\n"
    "v_mfma_f32_16x16x32_bf16 v[12:15], v[72:75], a[212:215], v[12:15]\n"
    "ds_read_b64_tr_b16 v[64:65], v216 offset:0\n"
    "v_mfma_f32_16x16x32_bf16 v[12:15], v[76:79], a[220:223], v[12:15]\n"
    "ds_read_b64_tr_b16 v[66:67], v216 offset:4096\n"
    "v_mfma_f32_16x16x32_bf16 v[8:11], v[72:75], a[208:211], v[8:11]\n"
    "ds_read_b64_tr_b16 v[68:69], v216 offset:8192\n"
    "v_mfma_f32_16x16x32_bf16 v[8:11], v[76:79], a[216:219], v[8:11]\n"
    "ds_read_b64_tr_b16 v[70:71], v216 offset:12288\n"
    "v_mfma_f32_16x16x32_bf16 v[16:19], v[80:83], a[208:211], v[16:19]\n"
    "ds_read_b64_tr_b16 v[72:73], v217 offset:0\n"
    "v_mfma_f32_16x16x32_bf16 v[16:19], v[84:87], a[216:219], v[16:19]\n"
    "ds_read_b64_tr_b16 v[74:75], v217 offset:4096\n"
    "v_mfma_f32_16x16x32_bf16 v[20:23], v[80:83], a[212:215], v[20:23]\n"
    "ds_read_b64_tr_b16 v[76:77], v217 offset:8192\n"
    "v_mfma_f32_16x16x32_bf16 v[20:23], v[84:87], a[220:223], v[20:23]\n"
    "ds_read_b64_tr_b16 v[78:79], v217 offset:12288\n"
    "v_mfma_f32_16x16x32_bf16 v[28:31], v[88:91], a[212:215], v[28:31]\n"
    "ds_read_b64_tr_b16 v[80:81], v216 offset:256\n"
    "v_mfma_f32_16x16x32_bf16 v[28:31], v[92:95], a[220:223], v[28:31]\n"
    "ds_read_b64_tr_b16 v[82:83], v216 offset:4352\n"
    "v_mfma_f32_16x16x32_bf16 v[24:27], v[88:91], a[208:211], v[24:27]\n"
    "ds_read_b64_tr_b16 v[84:85], v216 offset:8448\n"
    "v_mfma_f32_16x16x32_bf16 v[24:27], v[92:95], a[216:219], v[24:27]\n"
    "ds_read_b64_tr_b16 v[86:87], v216 offset:12544\n"
    "s_waitcnt lgkmcnt(10)\n"
    "v_mfma_f32_16x16x32_bf16 v[32:35], v[96:99], a[208:211], v[32:35]\n"
    "ds_read_b64_tr_b16 v[88:89], v217 offset:256\n"
    "v_mfma_f32_16x16x32_bf16 v[32:35], v[100:103], a[216:219], v[32:35]\n"
    "ds_read_b64_tr_b16 v[90:91], v217 offset:4352\n"
    "v_mfma_f32_16x16x32_bf16 v[36:39], v[96:99], a[212:215], v[36:39]\n"
    "ds_read_b64_tr_b16 v[92:93], v217 offset:8448\n"
    "v_mfma_f32_16x16x32_bf16 v[36:39], v[100:103], a[220:223], v[36:39]\n"
    "ds_read_b64_tr_b16 v[94:95], v217 offset:12544\n"
    "v_mfma_f32_16x16x32_bf16 v[44:47], v[104:107], a[212:215], v[44:47]\n"
    "ds_read_b64_tr_b16 v[96:97], v216 offset:512\n"
    "v_mfma_f32_16x16x32_bf16 v[44:47], v[108:111], a[220:223], v[44:47]\n"
    "ds_read_b64_tr_b16 v[98:99], v216 offset:4608\n"
    "v_mfma_f32_16x16x32_bf16 v[40:43], v[104:107], a[208:211], v[40:43]\n"
    "ds_read_b64_tr_b16 v[100:101], v216 offset:8704\n"
    "v_mfma_f32_16x16x32_bf16 v[40:43], v[108:111], a[216:219], v[40:43]\n"
    "ds_read_b64_tr_b16 v[102:103], v216 offset:12800\n"
    "s_waitcnt vmcnt(4)\n"
    "v_mfma_f32_16x16x32_bf16 v[0:3], v[64:67], a[224:227], v[0:3]\n"
    "ds_read_b64_tr_b16 v[104:105], v217 offset:512\n"
    "s_waitcnt lgkmcnt(9)\n"
    "v_mfma_f32_16x16x32_bf16 v[0:3], v[68:71], a[232:235], v[0:3]\n"
    "ds_read_b64_tr_b16 v[106:107], v217 offset:4608\n"
    "v_mfma_f32_16x16x32_bf16 v[4:7], v[64:67], a[228:231], v[4:7]\n"
    "ds_read_b64_tr_b16 v[108:109], v217 offset:8704\n"
    "v_mfma_f32_16x16x32_bf16 v[4:7], v[68:71], a[236:239], v[4:7]\n"
    "ds_read_b64_tr_b16 v[110:111], v217 offset:12800\n"
    "v_mfma_f32_16x16x32_bf16 v[12:15], v[72:75], a[228:231], v[12:15]\n"
    "ds_read_b64_tr_b16 v[64:65], v216 offset:16384\n"
    "v_mfma_f32_16x16x32_bf16 v[12:15], v[76:79], a[236:239], v[12:15]\n"
    "ds_read_b64_tr_b16 v[66:67], v216 offset:20480\n"
    "v_mfma_f32_16x16x32_bf16 v[8:11], v[72:75], a[224:227], v[8:11]\n"
    "ds_read_b64_tr_b16 v[68:69], v216 offset:24576\n"
    "v_mfma_f32_16x16x32_bf16 v[8:11], v[76:79], a[232:235], v[8:11]\n"
    "ds_read_b64_tr_b16 v[70:71], v216 offset:28672\n"
    "v_mfma_f32_16x16x32_bf16 v[16:19], v[80:83], a[224:227], v[16:19]\n"
    "ds_read_b64_tr_b16 v[72:73], v217 offset:16384\n"
    "v_mfma_f32_16x16x32_bf16 v[16:19], v[84:87], a[232:235], v[16:19]\n"
    "ds_read_b64_tr_b16 v[74:75], v217 offset:20480\n"
    "v_mfma_f32_16x16x32_bf16 v[20:23], v[80:83], a[228:231], v[20:23]\n"
    "ds_read_b64_tr_b16 v[76:77], v217 offset:24576\n"
    "v_mfma_f32_16x16x32_bf16 v[20:23], v[84:87], a[236:239], v[20:23]\n"
    "ds_read_b64_tr_b16 v[78:79], v217 offset:28672\n"
    "s_waitcnt lgkmcnt(10)\n"
    "v_mfma_f32_16x16x32_bf16 v[28:31], v[88:91], a[228:231], v[28:31]\n"
    "ds_read_b64_tr_b16 v[80:81], v216 offset:16640\n"
    "v_mfma_f32_16x16x32_bf16 v[28:31], v[92:95], a[236:239], v[28:31]\n"
    "ds_read_b64_tr_b16 v[82:83], v216 offset:20736\n"
    "v_mfma_f32_16x16x32_bf16 v[24:27], v[88:91], a[224:227], v[24:27]\n"
    "ds_read_b64_tr_b16 v[84:85], v216 offset:24832\n"
    "v_mfma_f32_16x16x32_bf16 v[24:27], v[92:95], a[232:235], v[24:27]\n"
    "ds_read_b64_tr_b16 v[86:87], v216 offset:28928\n"
    "v_mfma_f32_16x16x32_bf16 v[32:35], v[96:99], a[224:227], v[32:35]\n"
    "ds_read_b64_tr_b16 v[88:89], v217 offset:16640\n"
    "v_mfma_f32_16x16x32_bf16 v[32:35], v[100:103], a[232:235], v[32:35]\n"
    "ds_read_b64_tr_b16 v[90:91], v217 offset:20736\n"
    "v_mfma_f32_16x16x32_bf16 v[36:39], v[96:99], a[228:231], v[36:39]\n"
    "ds_read_b64_tr_b16 v[92:93], v217 offset:24832\n"
    "v_mfma_f32_16x16x32_bf16 v[36:39], v[100:103], a[236:239], v[36:39]\n"
    "ds_read_b64_tr_b16 v[94:95], v217 offset:28928\n"
    "v_mfma_f32_16x16x32_bf16 v[44:47], v[104:107], a[228:231], v[44:47]\n"
    "ds_read_b64_tr_b16 v[96:97], v216 offset:16896\n"
    "s_waitcnt lgkmcnt(9)\n"
    "v_mfma_f32_16x16x32_bf16 v[44:47], v[108:111], a[236:239], v[44:47]\n"
    "ds_read_b64_tr_b16 v[98:99], v216 offset:20992\n"
    "v_mfma_f32_16x16x32_bf16 v[40:43], v[104:107], a[224:227], v[40:43]\n"
    "ds_read_b64_tr_b16 v[100:101], v216 offset:25088\n"
    "v_mfma_f32_16x16x32_bf16 v[40:43], v[108:111], a[232:235], v[40:43]\n"
    "ds_read_b64_tr_b16 v[102:103], v216 offset:29184\n"
    "s_waitcnt vmcnt(0)\n"
    "v_mfma_f32_16x16x32_bf16 v[0:3], v[64:67], a[240:243], v[0:3]\n"
    "ds_read_b64_tr_b16 v[104:105], v217 offset:16896\n"
    "v_mfma_f32_16x16x32_bf16 v[0:3], v[68:71], a[248:251], v[0:3]\n"
    "ds_read_b64_tr_b16 v[106:107], v217 offset:20992\n"
    "v_mfma_f32_16x16x32_bf16 v[4:7], v[64:67], a[244:247], v[4:7]\n"
    "ds_read_b64_tr_b16 v[108:109], v217 offset:25088\n"
    "v_mfma_f32_16x16x32_bf16 v[4:7], v[68:71], a[252:255], v[4:7]\n"
    "ds_read_b64_tr_b16 v[110:111], v217 offset:29184\n"
    "v_mfma_f32_16x16x32_bf16 v[12:15], v[72:75], a[244:247], v[12:15]\n"
    "ds_read_b64_tr_b16 v[128:129], v214 offset:768\n"
    "v_mfma_f32_16x16x32_bf16 v[12:15], v[76:79], a[252:255], v[12:15]\n"
    "ds_read_b64_tr_b16 v[130:131], v214 offset:4864\n"
    "v_mfma_f32_16x16x32_bf16 v[8:11], v[72:75], a[240:243], v[8:11]\n"
    "ds_read_b64_tr_b16 v[132:133], v214 offset:8960\n"
    "v_mfma_f32_16x16x32_bf16 v[8:11], v[76:79], a[248:251], v[8:11]\n"
    "ds_read_b64_tr_b16 v[134:135], v214 offset:13056\n"
    "s_waitcnt lgkmcnt(9)\n"
    "v_mfma_f32_16x16x32_bf16 v[16:19], v[80:83], a[240:243], v[16:19]\n"
    "ds_read_b64_tr_b16 v[144:145], v214 offset:17152\n"
    "v_mfma_f32_16x16x32_bf16 v[16:19], v[84:87], a[248:251], v[16:19]\n"
    "ds_read_b64_tr_b16 v[146:147], v214 offset:21248\n"
    "v_mfma_f32_16x16x32_bf16 v[20:23], v[80:83], a[244:247], v[20:23]\n"
    "ds_read_b64_tr_b16 v[148:149], v214 offset:25344\n"
    "v_cvt_pk_bf16_f32 v0, v0, v1\n"
    "v_mfma_f32_16x16x32_bf16 v[20:23], v[84:87], a[252:255], v[20:23]\n"
    "ds_read_b64_tr_b16 v[150:151], v214 offset:29440\n"
    "v_cvt_pk_bf16_f32 v1, v2, v3\n"
    "v_mfma_f32_16x16x32_bf16 v[28:31], v[88:91], a[244:247], v[28:31]\n"
    "ds_read_b64_tr_b16 v[136:137], v215 offset:768\n"
    "v_cvt_pk_bf16_f32 v2, v4, v5\n"
    "v_mfma_f32_16x16x32_bf16 v[28:31], v[92:95], a[252:255], v[28:31]\n"
    "ds_read_b64_tr_b16 v[138:139], v215 offset:4864\n"
    "v_cvt_pk_bf16_f32 v3, v6, v7\n"
    "v_mfma_f32_16x16x32_bf16 v[24:27], v[88:91], a[240:243], v[24:27]\n"
    "ds_read_b64_tr_b16 v[140:141], v215 offset:8960\n"
    "buffer_store_dwordx4 v[0:3], v209, s[52:55], s61 offen offset:0 sc1\n"
    "v_mfma_f32_16x16x32_bf16 v[24:27], v[92:95], a[248:251], v[24:27]\n"
    "ds_read_b64_tr_b16 v[142:143], v215 offset:13056\n"
    "v_cvt_pk_bf16_f32 v8, v8, v9\n"
    "v_mfma_f32_16x16x32_bf16 v[32:35], v[96:99], a[240:243], v[32:35]\n"
    "ds_read_b64_tr_b16 v[152:153], v215 offset:17152\n"
    "s_waitcnt lgkmcnt(7)\n"
    "v_mfma_f32_16x16x32_bf16 v[32:35], v[100:103], a[248:251], v[32:35]\n"
    "ds_read_b64_tr_b16 v[154:155], v215 offset:21248\n"
    "v_mfma_f32_16x16x32_bf16 v[36:39], v[96:99], a[244:247], v[36:39]\n"
    "ds_read_b64_tr_b16 v[156:157], v215 offset:25344\n"
    "v_cvt_pk_bf16_f32 v9, v10, v11\n"
    "v_mfma_f32_16x16x32_bf16 v[36:39], v[100:103], a[252:255], v[36:39]\n"
    "ds_read_b64_tr_b16 v[158:159], v215 offset:29440\n"
    "v_cvt_pk_bf16_f32 v10, v12, v13\n"
    "v_mfma_f32_16x16x32_bf16 v[44:47], v[104:107], a[244:247], v[44:47]\n"
    "v_cvt_pk_bf16_f32 v11, v14, v15\n"
    "v_mfma_f32_16x16x32_bf16 v[44:47], v[108:111], a[252:255], v[44:47]\n"
    "buffer_store_dwordx4 v[8:11], v209, s[52:55], s61 offen offset:1024 sc1\n"
    "v_mfma_f32_16x16x32_bf16 v[40:43], v[104:107], a[240:243], v[40:43]\n"
    "v_cvt_pk_bf16_f32 v16, v16, v17\n"
    "v_mfma_f32_16x16x32_bf16 v[40:43], v[108:111], a[248:251], v[40:43]\n"
    "v_cvt_pk_bf16_f32 v17, v18, v19\n"
    "v_mfma_f32_16x16x32_bf16 v[48:51], v[128:131], a[96:99], v[48:51]\n"
    "ds_read_b64_tr_b16 v[160:161], v214 offset:33536\n"
    "v_mfma_f32_16x16x32_bf16 v[48:51], v[132:135], a[104:107], v[48:51]\n"
    "ds_read_b64_tr_b16 v[162:163], v214 offset:37632\n"
    "v_mfma_f32_16x16x32_bf16 v[48:51], v[144:147], a[112:115], v[48:51]\n"
    "ds_read_b64_tr_b16 v[164:165], v214 offset:41728\n"
    "v_cvt_pk_bf16_f32 v18, v20, v21\n"
    "v_cvt_pk_bf16_f32 v19, v22, v23\n"
    "s_waitcnt lgkmcnt(4)\n"
    "v_mfma_f32_16x16x32_bf16 v[48:51], v[148:151], a[120:123], v[48:51]\n"
    "ds_read_b64_tr_b16 v[166:167], v214 offset:45824\n"
    "buffer_store_dwordx4 v[16:19], v209, s[52:55], s62 offen offset:0 sc1\n"
    "v_cvt_pk_bf16_f32 v24, v24, v25\n"
    "v_mfma_f32_16x16x32_bf16 v[52:55], v[148:151], a[124:127], v[52:55]\n"
    "ds_read_b64_tr_b16 v[176:177], v214 offset:49920\n"
    "v_cvt_pk_bf16_f32 v25, v26, v27\n"
    "v_cvt_pk_bf16_f32 v26, v28, v29\n"
    "v_mfma_f32_16x16x32_bf16 v[52:55], v[144:147], a[116:119], v[52:55]\n"
    "ds_read_b64_tr_b16 v[178:179], v214 offset:54016\n"
    "v_cvt_pk_bf16_f32 v27, v30, v31\n"
    "buffer_store_dwordx4 v[24:27], v209, s[52:55], s62 offen offset:1024 sc1\n"
    "v_mfma_f32_16x16x32_bf16 v[52:55], v[132:135], a[108:111], v[52:55]\n"
    "ds_read_b64_tr_b16 v[180:181], v214 offset:58112\n"
    "v_cvt_pk_bf16_f32 v32, v32, v33\n"
    "v_cvt_pk_bf16_f32 v33, v34, v35\n"
    "v_mfma_f32_16x16x32_bf16 v[52:55], v[128:131], a[100:103], v[52:55]\n"
    "ds_read_b64_tr_b16 v[182:183], v214 offset:62208\n"
    "v_cvt_pk_bf16_f32 v34, v36, v37\n"
    "v_cvt_pk_bf16_f32 v35, v38, v39\n"
    "v_mfma_f32_16x16x32_bf16 v[60:63], v[136:139], a[100:103], v[60:63]\n"
    "ds_read_b64_tr_b16 v[168:169], v215 offset:33536\n"
    "buffer_store_dwordx4 v[32:35], v209, s[52:55], s63 offen offset:0 sc1\n"
    "v_cvt_pk_bf16_f32 v40, v40, v41\n"
    "v_mfma_f32_16x16x32_bf16 v[60:63], v[140:143], a[108:111], v[60:63]\n"
    "ds_read_b64_tr_b16 v[170:171], v215 offset:37632\n"
    "v_cvt_pk_bf16_f32 v41, v42, v43\n"
    "v_cvt_pk_bf16_f32 v42, v44, v45\n"
    "v_mfma_f32_16x16x32_bf16 v[60:63], v[152:155], a[116:119], v[60:63]\n"
    "ds_read_b64_tr_b16 v[172:173], v215 offset:41728\n"
    "v_cvt_pk_bf16_f32 v43, v46, v47\n"
    "buffer_store_dwordx4 v[40:43], v209, s[52:55], s63 offen offset:1024 sc1\n"
    "s_waitcnt lgkmcnt(5)\n"
    "v_mfma_f32_16x16x32_bf16 v[60:63], v[156:159], a[124:127], v[60:63]\n"
    "ds_read_b64_tr_b16 v[174:175], v215 offset:45824\n"
    "v_mfma_f32_16x16x32_bf16 v[56:59], v[156:159], a[120:123], v[56:59]\n"
    "ds_read_b64_tr_b16 v[184:185], v215 offset:49920\n"
    "v_mfma_f32_16x16x32_bf16 v[56:59], v[152:155], a[112:115], v[56:59]\n"
    "ds_read_b64_tr_b16 v[186:187], v215 offset:54016\n"
    "v_mfma_f32_16x16x32_bf16 v[56:59], v[140:143], a[104:107], v[56:59]\n"
    "ds_read_b64_tr_b16 v[188:189], v215 offset:58112\n"
    "v_mfma_f32_16x16x32_bf16 v[56:59], v[136:139], a[96:99], v[56:59]\n"
    "ds_read_b64_tr_b16 v[190:191], v215 offset:62208\n"
    "v_mfma_f32_16x16x32_bf16 v[48:51], v[160:163], a[128:131], v[48:51]\n"
    "ds_read_b64_tr_b16 v[128:129], v216 offset:33536\n"
    "v_mfma_f32_16x16x32_bf16 v[48:51], v[164:167], a[136:139], v[48:51]\n"
    "ds_read_b64_tr_b16 v[130:131], v216 offset:37632\n"
    "v_mfma_f32_16x16x32_bf16 v[48:51], v[176:179], a[144:147], v[48:51]\n"
    "ds_read_b64_tr_b16 v[132:133], v216 offset:41728\n"
    "s_waitcnt lgkmcnt(8)\n"
    "v_mfma_f32_16x16x32_bf16 v[48:51], v[180:183], a[152:155], v[48:51]\n"
    "ds_read_b64_tr_b16 v[134:135], v216 offset:45824\n"
    "v_mfma_f32_16x16x32_bf16 v[52:55], v[180:183], a[156:159], v[52:55]\n"
    "ds_read_b64_tr_b16 v[144:145], v216 offset:49920\n"
    "v_mfma_f32_16x16x32_bf16 v[52:55], v[176:179], a[148:151], v[52:55]\n"
    "ds_read_b64_tr_b16 v[146:147], v216 offset:54016\n"
    "v_mfma_f32_16x16x32_bf16 v[52:55], v[164:167], a[140:143], v[52:55]\n"
    "ds_read_b64_tr_b16 v[148:149], v216 offset:58112\n"
    "v_mfma_f32_16x16x32_bf16 v[52:55], v[160:163], a[132:135], v[52:55]\n"
    "ds_read_b64_tr_b16 v[150:151], v216 offset:62208\n"
    "v_mfma_f32_16x16x32_bf16 v[60:63], v[168:171], a[132:135], v[60:63]\n"
    "ds_read_b64_tr_b16 v[136:137], v217 offset:33536\n"
    "s_waitcnt lgkmcnt(9)\n"
    "v_mfma_f32_16x16x32_bf16 v[60:63], v[172:175], a[140:143], v[60:63]\n"
    "ds_read_b64_tr_b16 v[138:139], v217 offset:37632\n"
    "v_mfma_f32_16x16x32_bf16 v[60:63], v[184:187], a[148:151], v[60:63]\n"
    "ds_read_b64_tr_b16 v[140:141], v217 offset:41728\n"
    "v_mfma_f32_16x16x32_bf16 v[60:63], v[188:191], a[156:159], v[60:63]\n"
    "ds_read_b64_tr_b16 v[142:143], v217 offset:45824\n"
    "v_mfma_f32_16x16x32_bf16 v[56:59], v[188:191], a[152:155], v[56:59]\n"
    "ds_read_b64_tr_b16 v[152:153], v217 offset:49920\n"
    "v_mfma_f32_16x16x32_bf16 v[56:59], v[184:187], a[144:147], v[56:59]\n"
    "ds_read_b64_tr_b16 v[154:155], v217 offset:54016\n"
    "v_mfma_f32_16x16x32_bf16 v[56:59], v[172:175], a[136:139], v[56:59]\n"
    "ds_read_b64_tr_b16 v[156:157], v217 offset:58112\n"
    "v_mfma_f32_16x16x32_bf16 v[56:59], v[168:171], a[128:131], v[56:59]\n"
    "ds_read_b64_tr_b16 v[158:159], v217 offset:62208\n"
    "s_waitcnt vmcnt(0) lgkmcnt(0)\n"
    "s_barrier\n"
    "s_cmp_lg_u32 %[w], 0\n"
    "s_cbranch_scc1 NOFLAG2F_%=\n"
    "s_mov_b64 exec, 1\n"
    "global_store_dwordx2 v223, v[228:229], s[28:29] offset:0 sc1\n"
    "s_mov_b64 exec, -1\n"
    "s_nop 3\n"
    "NOFLAG2F_%=:\n"
    "v_mfma_f32_16x16x32_bf16 v[48:51], v[128:131], a[160:163], v[48:51]\n"
    "ds_read_b64_tr_b16 v[160:161], v218 offset:768\n"
    "v_mfma_f32_16x16x32_bf16 v[48:51], v[132:135], a[168:171], v[48:51]\n"
    "ds_read_b64_tr_b16 v[162:163], v218 offset:4864\n"
    "v_mfma_f32_16x16x32_bf16 v[48:51], v[144:147], a[176:179], v[48:51]\n"
    "ds_read_b64_tr_b16 v[164:165], v218 offset:8960\n"
    "v_mfma_f32_16x16x32_bf16 v[48:51], v[148:151], a[184:187], v[48:51]\n"
    "ds_read_b64_tr_b16 v[166:167], v218 offset:13056\n"
    "v_mfma_f32_16x16x32_bf16 v[52:55], v[148:151], a[188:191], v[52:55]\n"
    "ds_read_b64_tr_b16 v[176:177], v218 offset:17152\n"
    "v_mfma_f32_16x16x32_bf16 v[52:55], v[144:147], a[180:183], v[52:55]\n"
    "ds_read_b64_tr_b16 v[178:179], v218 offset:21248\n"
    "v_mfma_f32_16x16x32_bf16 v[52:55], v[132:135], a[172:175], v[52:55]\n"
    "ds_read_b64_tr_b16 v[180:181], v218 offset:25344\n"
    "v_mfma_f32_16x16x32_bf16 v[52:55], v[128:131], a[164:167], v[52:55]\n"
    "ds_read_b64_tr_b16 v[182:183], v218 offset:29440\n"
    "v_mfma_f32_16x16x32_bf16 v[60:63], v[136:139], a[164:167], v[60:63]\n"
    "ds_read_b64_tr_b16 v[168:169], v219 offset:768\n"
    "v_mfma_f32_16x16x32_bf16 v[60:63], v[140:143], a[172:175], v[60:63]\n"
    "ds_read_b64_tr_b16 v[170:171], v219 offset:4864\n"
    "v_mfma_f32_16x16x32_bf16 v[60:63], v[152:155], a[180:183], v[60:63]\n"
    "ds_read_b64_tr_b16 v[172:173], v219 offset:8960\n"
    "v_mfma_f32_16x16x32_bf16 v[60:63], v[156:159], a[188:191], v[60:63]\n"
    "ds_read_b64_tr_b16 v[174:175], v219 offset:13056\n"
    "v_mfma_f32_16x16x32_bf16 v[56:59], v[156:159], a[184:187], v[56:59]\n"
    "ds_read_b64_tr_b16 v[184:185], v219 offset:17152\n"
    "v_mfma_f32_16x16x32_bf16 v[56:59], v[152:155], a[176:179], v[56:59]\n"
    "ds_read_b64_tr_b16 v[186:187], v219 offset:21248\n"
    "v_mfma_f32_16x16x32_bf16 v[56:59], v[140:143], a[168:171], v[56:59]\n"
    "ds_read_b64_tr_b16 v[188:189], v219 offset:25344\n"
    "v_mfma_f32_16x16x32_bf16 v[56:59], v[136:139], a[160:163], v[56:59]\n"
    "ds_read_b64_tr_b16 v[190:191], v219 offset:29440\n"
    "s_waitcnt lgkmcnt(10)\n"
    "v_mfma_f32_16x16x32_bf16 v[48:51], v[160:163], a[192:195], v[48:51]\n"
    "ds_read_b64_tr_b16 v[128:129], v216 offset:768\n"
    "v_mfma_f32_16x16x32_bf16 v[48:51], v[164:167], a[200:203], v[48:51]\n"
    "ds_read_b64_tr_b16 v[130:131], v216 offset:4864\n"
    "v_mfma_f32_16x16x32_bf16 v[48:51], v[176:179], a[208:211], v[48:51]\n"
    "ds_read_b64_tr_b16 v[132:133], v216 offset:8960\n"
    "s_waitcnt lgkmcnt(9)\n"
    "v_mfma_f32_16x16x32_bf16 v[48:51], v[180:183], a[216:219], v[48:51]\n"
    "ds_read_b64_tr_b16 v[134:135], v216 offset:13056\n"
    "v_mfma_f32_16x16x32_bf16 v[52:55], v[180:183], a[220:223], v[52:55]\n"
    "ds_read_b64_tr_b16 v[144:145], v216 offset:17152\n"
    "v_mfma_f32_16x16x32_bf16 v[52:55], v[176:179], a[212:215], v[52:55]\n"
    "ds_read_b64_tr_b16 v[146:147], v216 offset:21248\n"
    "v_mfma_f32_16x16x32_bf16 v[52:55], v[164:167], a[204:207], v[52:55]\n"
    "ds_read_b64_tr_b16 v[148:149], v216 offset:25344\n"
    "v_mfma_f32_16x16x32_bf16 v[52:55], v[160:163], a[196:199], v[52:55]\n"
    "ds_read_b64_tr_b16 v[150:151], v216 offset:29440\n"
    "v_mfma_f32_16x16x32_bf16 v[60:63], v[168:171], a[196:199], v[60:63]\n"
    "ds_read_b64_tr_b16 v[136:137], v217 offset:768\n"
    "s_waitcnt lgkmcnt(9)\n"
    "v_mfma_f32_16x16x32_bf16 v[60:63], v[172:175], a[204:207], v[60:63]\n"
    "ds_read_b64_tr_b16 v[138:139], v217 offset:4864\n"
    "v_mfma_f32_16x16x32_bf16 v[60:63], v[184:187], a[212:215], v[60:63]\n"
    "ds_read_b64_tr_b16 v[140:141], v217 offset:8960\n"
    "v_mfma_f32_16x16x32_bf16 v[60:63], v[188:191], a[220:223], v[60:63]\n"
    "ds_read_b64_tr_b16 v[142:143], v217 offset:13056\n"
    "global_load_dwordx2 v[230:231], v221, s[28:29] sc1\n"
    "v_mfma_f32_16x16x32_bf16 v[56:59], v[188:191], a[216:219], v[56:59]\n"
    "ds_read_b64_tr_b16 v[152:153], v217 offset:17152\n"
    "v_mfma_f32_16x16x32_bf16 v[56:59], v[184:187], a[208:211], v[56:59]\n"
    "ds_read_b64_tr_b16 v[154:155], v217 offset:21248\n"
    "v_mfma_f32_16x16x32_bf16 v[56:59], v[172:175], a[200:203], v[56:59]\n"
    "ds_read_b64_tr_b16 v[156:157], v217 offset:25344\n"
    "v_mfma_f32_16x16x32_bf16 v[56:59], v[168:171], a[192:195], v[56:59]\n"
    "ds_read_b64_tr_b16 v[158:159], v217 offset:29440\n"
    "s_waitcnt lgkmcnt(9)\n"
    "v_mfma_f32_16x16x32_bf16 v[48:51], v[128:131], a[224:227], v[48:51]\n"
    "v_mfma_f32_16x16x32_bf16 v[48:51], v[132:135], a[232:235], v[48:51]\n"
    "v_mfma_f32_16x16x32_bf16 v[48:51], v[144:147], a[240:243], v[48:51]\n"
    "s_waitcnt lgkmcnt(7)\n"
    "v_mfma_f32_16x16x32_bf16 v[48:51], v[148:151], a[248:251], v[48:51]\n"
    "v_mfma_f32_16x16x32_bf16 v[52:55], v[148:151], a[252:255], v[52:55]\n"
    "v_mfma_f32_16x16x32_bf16 v[52:55], v[144:147], a[244:247], v[52:55]\n"
    "v_mfma_f32_16x16x32_bf16 v[52:55], v[132:135], a[236:239], v[52:55]\n"
    "v_mfma_f32_16x16x32_bf16 v[52:55], v[128:131], a[228:231], v[52:55]\n"
    "s_waitcnt lgkmcnt(4)\n"
    "v_mfma_f32_16x16x32_bf16 v[60:63], v[136:139], a[228:231], v[60:63]\n"
    "v_mfma_f32_16x16x32_bf16 v[60:63], v[140:143], a[236:239], v[60:63]\n"
    "s_waitcnt lgkmcnt(2)\n"
    "v_mfma_f32_16x16x32_bf16 v[60:63], v[152:155], a[244:247], v[60:63]\n"
    "s_waitcnt lgkmcnt(0)\n"
    "v_mfma_f32_16x16x32_bf16 v[60:63], v[156:159], a[252:255], v[60:63]\n"
    "v_mfma_f32_16x16x32_bf16 v[56:59], v[156:159], a[248:251], v[56:59]\n"
    "v_mfma_f32_16x16x32_bf16 v[56:59], v[152:155], a[240:243], v[56:59]\n"
    "v_mfma_f32_16x16x32_bf16 v[56:59], v[140:143], a[232:235], v[56:59]\n"
    "v_mfma_f32_16x16x32_bf16 v[56:59], v[136:139], a[224:227], v[56:59]\n"
    "s_waitcnt vmcnt(0)\n"
    "v_cmp_ne_u64 vcc, v[230:231], v[228:229]\n"
    "s_and_b32 s73, vcc_lo, 7\n"
    "s_cbranch_scc1 P2SF_%=\n"
    "P2BF_%=:\n"
    "buffer_load_dwordx4 v[64:67], v209, s[52:55], s67 offen offset:0 sc1\n"
    "buffer_load_dwordx4 v[68:71], v209, s[52:55], s67 offen offset:1024 sc1\n"
    "buffer_load_dwordx4 v[72:75], v209, s[52:55], s68 offen offset:0 sc1\n"
    "buffer_load_dwordx4 v[76:79], v209, s[52:55], s68 offen offset:1024 sc1\n"
    "buffer_load_dwordx4 v[80:83], v209, s[52:55], s69 offen offset:0 sc1\n"
    "buffer_load_dwordx4 v[84:87], v209, s[52:55], s69 offen offset:1024 sc1\n"
    "s_waitcnt vmcnt(4)\n"
    "v_lshlrev_b32 v100, 16, v64\n"
    "v_and_b32 v101, 0xffff0000, v64\n"
    "v_pk_add_f32 v[48:49], v[48:49], v[100:101]\n"
    "v_lshlrev_b32 v102, 16, v65\n"
    "v_and_b32 v103, 0xffff0000, v65\n"
    "v_pk_add_f32 v[50:51], v[50:51], v[102:103]\n"
    "v_lshlrev_b32 v104, 16, v66\n"
    "v_and_b32 v105, 0xffff0000, v66\n"
    "v_pk_add_f32 v[52:53], v[52:53], v[104:105]\n"
    "v_lshlrev_b32 v106, 16, v67\n"
    "v_and_b32 v107, 0xffff0000, v67\n"
    "v_pk_add_f32 v[54:55], v[54:55], v[106:107]\n"
    "v_lshlrev_b32 v100, 16, v68\n"
    "v_and_b32 v101, 0xffff0000, v68\n"
    "v_pk_add_f32 v[56:57], v[56:57], v[100:101]\n"
    "v_lshlrev_b32 v102, 16, v69\n"
    "v_and_b32 v103, 0xffff0000, v69\n"
    "v_pk_add_f32 v[58:59], v[58:59], v[102:103]\n"
    "v_lshlrev_b32 v104, 16, v70\n"
    "v_and_b32 v105, 0xffff0000, v70\n"
    "v_pk_add_f32 v[60:61], v[60:61], v[104:105]\n"
    "v_lshlrev_b32 v106, 16, v71\n"
    "v_and_b32 v107, 0xffff0000, v71\n"
    "v_pk_add_f32 v[62:63], v[62:63], v[106:107]\n"
    "s_waitcnt vmcnt(2)\n"
    "v_lshlrev_b32 v100, 16, v72\n"
    "v_and_b32 v101, 0xffff0000, v72\n"
    "v_pk_add_f32 v[48:49], v[48:49], v[100:101]\n"
    "v_lshlrev_b32 v102, 16, v73\n"
    "v_and_b32 v103, 0xffff0000, v73\n"
    "v_pk_add_f32 v[50:51], v[50:51], v[102:103]\n"
    "v_lshlrev_b32 v104, 16, v74\n"
    "v_and_b32 v105, 0xffff0000, v74\n"
    "v_pk_add_f32 v[52:53], v[52:53], v[104:105]\n"
    "v_lshlrev_b32 v106, 16, v75\n"
    "v_and_b32 v107, 0xffff0000, v75\n"
    "v_pk_add_f32 v[54:55], v[54:55], v[106:107]\n"
    "v_lshlrev_b32 v100, 16, v76\n"
    "v_and_b32 v101, 0xffff0000, v76\n"
    "v_pk_add_f32 v[56:57], v[56:57], v[100:101]\n"
    "v_lshlrev_b32 v102, 16, v77\n"
    "v_and_b32 v103, 0xffff0000, v77\n"
    "v_pk_add_f32 v[58:59], v[58:59], v[102:103]\n"
    "v_lshlrev_b32 v104, 16, v78\n"
    "v_and_b32 v105, 0xffff0000, v78\n"
    "v_pk_add_f32 v[60:61], v[60:61], v[104:105]\n"
    "v_lshlrev_b32 v106, 16, v79\n"
    "v_and_b32 v107, 0xffff0000, v79\n"
    "v_pk_add_f32 v[62:63], v[62:63], v[106:107]\n"
    "s_waitcnt vmcnt(0)\n"
    "v_lshlrev_b32 v100, 16, v80\n"
    "v_and_b32 v101, 0xffff0000, v80\n"
    "v_pk_add_f32 v[48:49], v[48:49], v[100:101]\n"
    "v_lshlrev_b32 v102, 16, v81\n"
    "v_and_b32 v103, 0xffff0000, v81\n"
    "v_pk_add_f32 v[50:51], v[50:51], v[102:103]\n"
    "v_lshlrev_b32 v104, 16, v82\n"
    "v_and_b32 v105, 0xffff0000, v82\n"
    "v_pk_add_f32 v[52:53], v[52:53], v[104:105]\n"
    "v_lshlrev_b32 v106, 16, v83\n"
    "v_and_b32 v107, 0xffff0000, v83\n"
    "v_pk_add_f32 v[54:55], v[54:55], v[106:107]\n"
    "v_lshlrev_b32 v100, 16, v84\n"
    "v_and_b32 v101, 0xffff0000, v84\n"
    "v_pk_add_f32 v[56:57], v[56:57], v[100:101]\n"
    "v_lshlrev_b32 v102, 16, v85\n"
    "v_and_b32 v103, 0xffff0000, v85\n"
    "v_pk_add_f32 v[58:59], v[58:59], v[102:103]\n"
    "v_lshlrev_b32 v104, 16, v86\n"
    "v_and_b32 v105, 0xffff0000, v86\n"
    "v_pk_add_f32 v[60:61], v[60:61], v[104:105]\n"
    "v_lshlrev_b32 v106, 16, v87\n"
    "v_and_b32 v107, 0xffff0000, v87\n"
    "v_pk_add_f32 v[62:63], v[62:63], v[106:107]\n"
    "v_cvt_pk_bf16_f32 v48, v48, v49\n"
    "v_cvt_pk_bf16_f32 v49, v50, v51\n"
    "buffer_store_dwordx2 v[48:49], v224, s[36:39], s43 offen offset:0\n"
    "v_cvt_pk_bf16_f32 v52, v52, v53\n"
    "v_cvt_pk_bf16_f32 v53, v54, v55\n"
    "buffer_store_dwordx2 v[52:53], v225, s[36:39], s43 offen offset:0\n"
    "v_cvt_pk_bf16_f32 v56, v56, v57\n"
    "v_cvt_pk_bf16_f32 v57, v58, v59\n"
    "buffer_store_dwordx2 v[56:57], v224, s[36:39], s43 offen offset:32\n"
    "v_cvt_pk_bf16_f32 v60, v60, v61\n"
    "v_cvt_pk_bf16_f32 v61, v62, v63\n"
    "buffer_store_dwordx2 v[60:61], v225, s[36:39], s43 offen offset:32\n"
    "s_branch ENDF_%=\n"
    "P1SF_%=:\n"
    "s_mov_b32 s42, 0x40000\n"
    "P1LF_%=:\n"
    "global_load_dwordx2 v[230:231], v220, s[28:29] offset:0 sc1\n"
    "s_waitcnt vmcnt(0)\n"
    "s_sub_u32 s42, s42, 1\n"
    "s_cbranch_scc1 P1BF_%=\n"
    "v_cmp_ne_u64 vcc, v[230:231], v[228:229]\n"
    "s_and_b32 s73, vcc_lo, 255\n"
    "s_cbranch_scc1 P1LF_%=\n"
    "s_branch P1BF_%=\n"
    "P2SF_%=:\n"
    "s_mov_b32 s42, 0x40000\n"
    "P2LF_%=:\n"
    "global_load_dwordx2 v[230:231], v221, s[28:29] offset:0 sc1\n"
    "s_waitcnt vmcnt(0)\n"
    "s_sub_u32 s42, s42, 1\n"
    "s_cbranch_scc1 P2BF_%=\n"
    "v_cmp_ne_u64 vcc, v[230:231], v[228:229]\n"
    "s_and_b32 s73, vcc_lo, 7\n"
    "s_cbranch_scc1 P2LF_%=\n"
    "s_branch P2BF_%=\n"
    "IDLEF_%=:\n"
    "s_lshl_b32 s72, %[rk], 3\n"
    "s_add_u32 s72, s72, %[xcd]\n"
    "s_lshl_b32 s72, s72, 2\n"
    "s_add_u32 s72, s72, %[w]\n"
    "s_cmp_lg_u32 s72, 992\n"
    "s_cbranch_scc1 ENDF_%=\n"
    "v_lshrrev_b32 v100, 3, v208\n"
    "v_lshlrev_b32 v100, 7, v100\n"
    "v_and_b32 v101, 7, v208\n"
    "v_lshl_add_u32 v100, v101, 3, v100\n"
    "v_add_u32 v100, 41943040, v100\n"
    "s_mov_b32 s42, 0x40000\n"
    "PE0LF_%=:\n"
    "global_load_dwordx2 v[230:231], v100, s[28:29] offset:0 sc1\n"
    "s_waitcnt vmcnt(0)\n"
    "s_sub_u32 s42, s42, 1\n"
    "s_cbranch_scc1 PE0XF_%=\n"
    "v_cmp_ne_u64 vcc, v[230:231], v[228:229]\n"
    "s_or_b32 s73, vcc_lo, vcc_hi\n"
    "s_cbranch_scc1 PE0LF_%=\n"
    "PE0XF_%=:\n"
    "v_add_u32 v100, 1024, v100\n"
    "s_mov_b32 s42, 0x40000\n"
    "PE1LF_%=:\n"
    "global_load_dwordx2 v[230:231], v100, s[28:29] offset:0 sc1\n"
    "s_waitcnt vmcnt(0)\n"
    "s_sub_u32 s42, s42, 1\n"
    "s_cbranch_scc1 PE1XF_%=\n"
    "v_cmp_ne_u64 vcc, v[230:231], v[228:229]\n"
    "s_or_b32 s73, vcc_lo, vcc_hi\n"
    "s_cbranch_scc1 PE1LF_%=\n"
    "PE1XF_%=:\n"
    "v_add_u32 v100, 1024, v100\n"
    "s_mov_b32 s42, 0x40000\n"
    "PE2LF_%=:\n"
    "global_load_dwordx2 v[230:231], v100, s[28:29] offset:0 sc1\n"
    "s_waitcnt vmcnt(0)\n"
    "s_sub_u32 s42, s42, 1\n"
    "s_cbranch_scc1 PE2XF_%=\n"
    "v_cmp_ne_u64 vcc, v[230:231], v[228:229]\n"
    "s_or_b32 s73, vcc_lo, vcc_hi\n"
    "s_cbranch_scc1 PE2LF_%=\n"
    "PE2XF_%=:\n"
    "v_add_u32 v100, 1024, v100\n"
    "s_mov_b32 s42, 0x40000\n"
    "PE3LF_%=:\n"
    "global_load_dwordx2 v[230:231], v100, s[28:29] offset:0 sc1\n"
    "s_waitcnt vmcnt(0)\n"
    "s_sub_u32 s42, s42, 1\n"
    "s_cbranch_scc1 PE3XF_%=\n"
    "v_cmp_ne_u64 vcc, v[230:231], v[228:229]\n"
    "s_or_b32 s73, vcc_lo, vcc_hi\n"
    "s_cbranch_scc1 PE3LF_%=\n"
    "PE3XF_%=:\n"
    "v_add_u32 v100, 1024, v100\n"
    "v_mov_b32 v100, 42008576\n"
    "s_mov_b64 exec, 1\n"
    "global_store_dwordx2 v100, v[228:229], s[28:29] sc1\n"
    "s_mov_b64 exec, -1\n"
    "ENDF_%=:\n"
    "s_endpgm\n"
    "SPIN0_%=:\n"
    "s_sleep 1\n"
    "s_memrealtime s[92:93]\n"
    "s_waitcnt lgkmcnt(0)\n"
    "s_lshl_b32 s92, s92, 5\n"
    "s_sub_u32 s92, s80, s92\n"
    "s_cmp_gt_i32 s92, 64\n"
    "s_cbranch_scc1 SPIN0_%=\n"
    "s_branch BACK0_%=\n"
    "SPIN1_%=:\n"
    "s_sleep 1\n"
    "s_memrealtime s[92:93]\n"
    "s_waitcnt lgkmcnt(0)\n"
    "s_lshl_b32 s92, s92, 5\n"
    "s_sub_u32 s92, s80, s92\n"
    "s_cmp_gt_i32 s92, 64\n"
    "s_cbranch_scc1 SPIN1_%=\n"
    "s_branch BACK1_%=\n"
    "SPIN2_%=:\n"
    "s_sleep 1\n"
    "s_memrealtime s[92:93]\n"
    "s_waitcnt lgkmcnt(0)\n"
    "s_lshl_b32 s92, s92, 5\n"
    "s_sub_u32 s92, s80, s92\n"
    "s_cmp_gt_i32 s92, 64\n"
    "s_cbranch_scc1 SPIN2_%=\n"
    "s_branch BACK2_%=\n"
    "SLOW_%=:\n"
    "s_mov_b32 %[e0], s78\n"
    "s_mov_b32 %[e1], s79\n"
    "s_waitcnt vmcnt(0)\n"
    "s_nop 1\n"
    : [e0] "=&s"(e0), [e1] "=&s"(e1), [ka] "+s"(ka), [w] "+s"(w), [b] "+s"(b), [h] "+s"(h)
    : [rk] "s"(rk), [xcd] "s"(xcd)
    : "v0", "v1", "v2", "v3", "v4", "v5", "v6", "v7", "v8", "v9", "v10", "v11", "v12", "v13", "v14", "v15", "v16", "v17", "v18", "v19", "v20", "v21", "v22", "v23", "v24", "v25", "v26", "v27", "v28", "v29", "v30", "v31", "v32", "v33", "v34", "v35", "v36", "v37", "v38", "v39", "v40", "v41", "v42", "v43", "v44", "v45", "v46", "v47", "v48", "v49", "v50", "v51", "v52", "v53", "v54", "v55", "v56", "v57", "v58", "v59", "v60", "v61", "v62", "v63", "v64", "v65", "v66", "v67", "v68", "v69", "v70", "v71", "v72", "v73", "v74", "v75", "v76", "v77", "v78", "v79", "v80", "v81", "v82", "v83", "v84", "v85", "v86", "v87", "v88", "v89", "v90", "v91", "v92", "v93", "v94", "v95", "v96", "v97", "v98", "v99", "v100", "v101", "v102", "v103", "v104", "v105", "v106", "v107", "v108", "v109", "v110", "v111", "v112", "v113", "v114", "v115", "v116", "v117", "v118", "v119", "v120", "v121", "v122", "v123", "v124", "v125", "v126", "v127", "v128", "v129", "v130", "v131", "v132", "v133", "v134", "v135", "v136", "v137", "v138", "v139", "v140", "v141", "v142", "v143", "v144", "v145", "v146", "v147", "v148", "v149", "v150", "v151", "v152", "v153", "v154", "v155", "v156", "v157", "v158", "v159", "v160", "v161", "v162", "v163", "v164", "v165", "v166", "v167", "v168", "v169", "v170", "v171", "v172", "v173", "v174", "v175", "v176", "v177", "v178", "v179", "v180", "v181", "v182", "v183", "v184", "v185", "v186", "v187", "v188", "v189", "v190", "v191", "v192", "v193", "v194", "v195", "v196", "v197", "v198", "v199", "v200", "v201", "v202", "v203", "v204", "v205", "v206", "v207", "v208", "v209", "v210", "v211", "v212", "v213", "v214", "v215", "v216", "v217", "v218", "v219", "v220", "v221", "v222", "v223", "v224", "v225", "v226", "v227", "v228", "v229", "v230", "v231", "v232", "v233", "v234", "v235", "v236", "v237", "v238", "v239", "v240", "v241", "v242", "v243", "v244", "v245", "v246", "v247", "v248", "v249", "v250", "v251", "v252", "v253", "v254", "v255", "a0", "a1", "a2", "a3", "a4", "a5", "a6", "a7", "a8", "a9", "a10", "a11", "a12", "a13", "a14", "a15", "a16", "a17", "a18", "a19", "a20", "a21", "a22", "a23", "a24", "a25", "a26", "a27", "a28", "a29", "a30", "a31", "a32", "a33", "a34", "a35", "a36", "a37", "a38", "a39", "a40", "a41", "a42", "a43", "a44", "a45", "a46", "a47", "a48", "a49", "a50", "a51", "a52", "a53", "a54", "a55", "a56", "a57", "a58", "a59", "a60", "a61", "a62", "a63", "a64", "a65", "a66", "a67", "a68", "a69", "a70", "a71", "a72", "a73", "a74", "a75", "a76", "a77", "a78", "a79", "a80", "a81", "a82", "a83", "a84", "a85", "a86", "a87", "a88", "a89", "a90", "a91", "a92", "a93", "a94", "a95", "a96", "a97", "a98", "a99", "a100", "a101", "a102", "a103", "a104", "a105", "a106", "a107", "a108", "a109", "a110", "a111", "a112", "a113", "a114", "a115", "a116", "a117", "a118", "a119", "a120", "a121", "a122", "a123", "a124", "a125", "a126", "a127", "a128", "a129", "a130", "a131", "a132", "a133", "a134", "a135", "a136", "a137", "a138", "a139", "a140", "a141", "a142", "a143", "a144", "a145", "a146", "a147", "a148", "a149", "a150", "a151", "a152", "a153", "a154", "a155", "a156", "a157", "a158", "a159", "a160", "a161", "a162", "a163", "a164", "a165", "a166", "a167", "a168", "a169", "a170", "a171", "a172", "a173", "a174", "a175", "a176", "a177", "a178", "a179", "a180", "a181", "a182", "a183", "a184", "a185", "a186", "a187", "a188", "a189", "a190", "a191", "a192", "a193", "a194", "a195", "a196", "a197", "a198", "a199", "a200", "a201", "a202", "a203", "a204", "a205", "a206", "a207", "a208", "a209", "a210", "a211", "a212", "a213", "a214", "a215", "a216", "a217", "a218", "a219", "a220", "a221", "a222", "a223", "a224", "a225", "a226", "a227", "a228", "a229", "a230", "a231", "a232", "a233", "a234", "a235", "a236", "a237", "a238", "a239", "a240", "a241", "a242", "a243", "a244", "a245", "a246", "a247", "a248", "a249", "a250", "a251", "a252", "a253", "a254", "a255", "s16", "s17", "s18", "s19", "s20", "s21", "s22", "s23", "s24", "s25", "s26", "s27", "s28", "s29", "s36", "s37", "s38", "s39", "s40", "s41", "s42", "s43", "s44", "s45", "s46", "s47", "s48", "s49", "s50", "s51", "s52", "s53", "s54", "s55", "s56", "s57", "s58", "s59", "s60", "s61", "s62", "s63", "s64", "s65", "s66", "s67", "s68", "s69", "s70", "s71", "s72", "s73", "s74", "s75", "s76", "s77", "s78", "s79", "s80", "s81", "s82", "s83", "s84", "s85", "s86", "s87", "s88", "s89", "s90", "s91", "s92", "s93", "s94", "s95", "s96", "s97", "s98", "s99", "vcc", "scc", "memory");
  typedef __attribute__((address_space(1))) char* gptr;
  x = (const u16*)(gptr)ka[0]; kc = (const u16*)(gptr)ka[1]; vc = (const u16*)(gptr)ka[2]; wq = (const u16*)(gptr)ka[3];
  ows = (char*)(gptr)ka[6];
  slow_path(x, kc, vc, wq, ows, b, h, w);
  asm volatile(
    "s_load_dwordx4 s[24:27], %[ka], 0x20\n"
    "s_load_dwordx2 s[28:29], %[ka], 0x30\n"
    "s_waitcnt lgkmcnt(0)\n"
    "v_mbcnt_lo_u32_b32 v208, -1, 0\n"
    "v_mbcnt_hi_u32_b32 v208, -1, v208\n"
    "v_lshlrev_b32 v209, 4, v208\n"
    "v_mov_b32 v228, %[e0]\n"
    "v_mov_b32 v229, %[e1]\n"
    "s_lshr_b32 s89, %[xcd], 1\n"
    "s_mul_i32 s90, %[rk], 43\n"
    "s_lshr_b32 s90, s90, 8\n"
    "s_mul_i32 s72, s89, 1310720\n"
    "s_lshl_b32 s73, s90, 8\n"
    "s_add_u32 s72, s72, s73\n"
    "s_mov_b64 s[84:85], s[24:25]\n"
    "s_add_u32 s84, s84, s72\n"
    "s_addc_u32 s85, s85, 0\n"
    "s_and_b32 s85, s85, 0xffff\n"
    "s_mov_b32 s72, 1309696\n"
    "s_cmp_lt_u32 %[rk], 30\n"
    "s_cselect_b32 s86, s72, 0\n"
    "s_mov_b32 s87, 0x20000\n"
    "s_mov_b32 s88, 0\n"
    "v_bfe_u32 v254, v208, 2, 2\n"
    "v_mul_u32_u24 v254, 1280, v254\n"
    "v_lshrrev_b32 v255, 4, v208\n"
    "s_add_u32 s72, s89, 1\n"
    "v_add_u32 v255, s72, v255\n"
    "v_and_b32 v255, 3, v255\n"
    "v_lshl_add_u32 v254, v255, 6, v254\n"
    "v_and_b32 v255, 3, v208\n"
    "v_lshl_add_u32 v210, v255, 4, v254\n"
    "v_xor_b32 v255, 32, v210\n"
    "v_add_u32 v211, 4096, v255\n"
    "v_add_u32 v212, 8192, v210\n"
    "v_add_u32 v213, 12288, v255\n"
    "s_lshl_b32 s66, %[w], 12\n"
    "s_mul_i32 s65, %[w], 20480\n"
    "s_mov_b64 s[52:53], s[28:29]\n"
    "s_and_b32 s53, s53, 0xffff\n"
    "s_mov_b32 s54, 41943040\n"
    "s_mov_b32 s55, 0x20000\n"
    "s_mul_i32 s72, s90, 6\n"
    "s_sub_u32 s78, %[rk], s72\n"
    "s_and_b32 s72, %[xcd], 1\n"
    "s_mul_i32 s72, s72, 6\n"
    "s_add_u32 s78, s78, s72\n"
    "s_mul_i32 s79, s78, 5\n"
    "s_add_u32 s79, s79, s90\n"
    "s_lshl_b32 s72, s78, 2\n"
    "s_add_u32 s72, s72, %[w]\n"
    "s_mul_i32 s43, s72, 40960\n"
    "s_mul_i32 s74, s72, 43\n"
    "s_lshr_b32 s74, s74, 8\n"
    "s_lshl_b32 s74, s74, 2\n"
    "s_add_u32 s74, s74, s89\n"
    "s_lshl_b32 s74, s74, 7\n"
    "s_add_u32 s74, s74, 41943040\n"
    "s_lshl_b32 s40, s72, 18\n"
    "s_lshl_b32 s73, s89, 15\n"
    "s_add_u32 s40, s40, s73\n"
    "s_lshl_b32 s72, s90, 8\n"
    "s_add_u32 s43, s43, s72\n"
    "s_lshl_b32 s72, s89, 6\n"
    "s_add_u32 s43, s43, s72\n"
    "s_lshl_b32 s78, s79, 17\n"
    "s_add_u32 s78, s78, 16777216\n"
    "s_lshl_b32 s73, %[w], 11\n"
    "s_add_u32 s78, s78, s73\n"
    "s_add_u32 s72, s89, 1\n"
    "s_and_b32 s72, s72, 3\n"
    "s_lshl_b32 s61, s72, 2\n"
    "s_add_u32 s61, s61, s89\n"
    "s_lshl_b32 s61, s61, 13\n"
    "s_add_u32 s61, s61, s78\n"
    "s_lshl_b32 s67, s89, 2\n"
    "s_add_u32 s67, s67, s72\n"
    "s_lshl_b32 s67, s67, 13\n"
    "s_add_u32 s67, s67, s78\n"
    "s_add_u32 s72, s89, 2\n"
    "s_and_b32 s72, s72, 3\n"
    "s_lshl_b32 s62, s72, 2\n"
    "s_add_u32 s62, s62, s89\n"
    "s_lshl_b32 s62, s62, 13\n"
    "s_add_u32 s62, s62, s78\n"
    "s_lshl_b32 s68, s89, 2\n"
    "s_add_u32 s68, s68, s72\n"
    "s_lshl_b32 s68, s68, 13\n"
    "s_add_u32 s68, s68, s78\n"
    "s_add_u32 s72, s89, 3\n"
    "s_and_b32 s72, s72, 3\n"
    "s_lshl_b32 s63, s72, 2\n"
    "s_add_u32 s63, s63, s89\n"
    "s_lshl_b32 s63, s63, 13\n"
    "s_add_u32 s63, s63, s78\n"
    "s_lshl_b32 s69, s89, 2\n"
    "s_add_u32 s69, s69, s72\n"
    "s_lshl_b32 s69, s69, 13\n"
    "s_add_u32 s69, s69, s78\n"
    "s_lshl_b32 s75, s79, 7\n"
    "s_add_u32 s75, s75, 41951232\n"
    "s_lshl_b32 s76, s89, 3\n"
    "s_add_u32 s76, s76, s75\n"
    "s_lshl_b32 s77, %[b], 2\n"
    "s_add_u32 s77, s77, s89\n"
    "s_lshl_b32 s77, s77, 7\n"
    "s_and_b32 s72, %[h], 7\n"
    "s_lshl_b32 s72, s72, 3\n"
    "s_add_u32 s77, s77, s72\n"
    "s_add_u32 s77, s77, 41943040\n"
    "s_add_u32 s73, s89, 1\n"
    "s_mov_b64 s[36:37], s[26:27]\n"
    "s_and_b32 s37, s37, 0xffff\n"
    "s_mov_b32 s38, 1966080\n"
    "s_mov_b32 s39, 0x20000\n"
    "v_mov_b32 v222, s77\n"
    "v_mov_b32 v223, s76\n"
    "v_and_b32 v220, 7, v208\n"
    "v_lshlrev_b32 v220, 3, v220\n"
    "v_add_u32 v220, s74, v220\n"
    "v_and_b32 v221, 3, v208\n"
    "v_min_u32 v221, 2, v221\n"
    "v_add_u32 v221, s73, v221\n"
    "v_and_b32 v221, 3, v221\n"
    "v_lshlrev_b32 v221, 3, v221\n"
    "v_add_u32 v221, s75, v221\n"
    "v_bfe_u32 v216, v208, 2, 2\n"
    "v_lshrrev_b32 v217, 4, v208\n"
    "v_and_b32 v218, 3, v208\n"
    "v_and_b32 v219, 1, v217\n"
    "v_lshlrev_b32 v219, 5, v219\n"
    "v_lshl_add_u32 v219, v216, 6, v219\n"
    "v_lshl_add_u32 v219, v218, 3, v219\n"
    "v_lshl_add_u32 v214, v217, 10, v219\n"
    "v_xor_b32 v215, 32, v214\n"
    "v_and_b32 v224, 15, v208\n"
    "v_mul_u32_u24 v224, 1280, v224\n"
    "v_lshl_add_u32 v224, v217, 3, v224\n"
    "v_add_u32 v225, 20480, v224\n"
    "v_add_u32 v216, 65536, v214\n"
    "v_add_u32 v217, 65536, v215\n"
    "v_add_u32 v218, 131072, v214\n"
    "v_add_u32 v219, 131072, v215\n"
    "v_add_u32 v226, 131072, v209\n"
    "s_waitcnt lgkmcnt(0)\n"
    "s_waitcnt vmcnt(0)\n"
    "s_barrier\n"
    "s_cmp_lg_u32 %[w], 0\n"
    "s_cbranch_scc1 NOFLAGR_%=\n"
    "s_mov_b64 exec, 1\n"
    "global_store_dwordx2 v222, v[228:229], s[28:29] sc1\n"
    "s_mov_b64 exec, -1\n"
    "NOFLAGR_%=:\n"
    "s_cmp_lt_u32 %[rk], 30\n"
    "s_cbranch_scc0 IDLER_%=\n"
    "s_add_u32 m0, s66, 98304\n"
    "s_add_u32 s72, s65, 0\n"
    "buffer_load_dwordx4 v210, s[84:87], s72 offen offset:0 lds\n"
    "buffer_load_dwordx4 v211, s[84:87], s72 offen offset:1024 lds\n"
    "buffer_load_dwordx4 v212, s[84:87], s72 offen offset:2048 lds\n"
    "buffer_load_dwordx4 v213, s[84:87], s72 offen offset:3072 lds\n"
    "s_add_u32 m0, s66, 114688\n"
    "s_add_u32 s72, s65, 81920\n"
    "buffer_load_dwordx4 v210, s[84:87], s72 offen offset:0 lds\n"
    "buffer_load_dwordx4 v211, s[84:87], s72 offen offset:1024 lds\n"
    "buffer_load_dwordx4 v212, s[84:87], s72 offen offset:2048 lds\n"
    "buffer_load_dwordx4 v213, s[84:87], s72 offen offset:3072 lds\n"
    "s_add_u32 m0, s66, 131072\n"
    "s_add_u32 s72, s65, 163840\n"
    "buffer_load_dwordx4 v210, s[84:87], s72 offen offset:0 lds\n"
    "buffer_load_dwordx4 v211, s[84:87], s72 offen offset:1024 lds\n"
    "buffer_load_dwordx4 v212, s[84:87], s72 offen offset:2048 lds\n"
    "buffer_load_dwordx4 v213, s[84:87], s72 offen offset:3072 lds\n"
    "global_load_dwordx2 v[230:231], v220, s[28:29] sc1\n"
    "s_add_u32 m0, s66, 147456\n"
    "s_add_u32 s72, s65, 245760\n"
    "buffer_load_dwordx4 v210, s[84:87], s72 offen offset:0 lds\n"
    "buffer_load_dwordx4 v211, s[84:87], s72 offen offset:1024 lds\n"
    "buffer_load_dwordx4 v212, s[84:87], s72 offen offset:2048 lds\n"
    "buffer_load_dwordx4 v213, s[84:87], s72 offen offset:3072 lds\n"
    "s_add_u32 m0, s66, 65536\n"
    "s_add_u32 s72, s65, 327680\n"
    "buffer_load_dwordx4 v210, s[84:87], s72 offen offset:0 lds\n"
    "buffer_load_dwordx4 v211, s[84:87], s72 offen offset:1024 lds\n"
    "buffer_load_dwordx4 v212, s[84:87], s72 offen offset:2048 lds\n"
    "buffer_load_dwordx4 v213, s[84:87], s72 offen offset:3072 lds\n"
    "s_waitcnt vmcnt(13)\n"
    "s_barrier\n"
    "ds_read_b64_tr_b16 v[64:65], v216 offset:32768\n"
    "ds_read_b64_tr_b16 v[66:67], v216 offset:36864\n"
    "ds_read_b64_tr_b16 v[68:69], v216 offset:40960\n"
    "ds_read_b64_tr_b16 v[70:71], v216 offset:45056\n"
    "ds_read_b64_tr_b16 v[72:73], v217 offset:32768\n"
    "ds_read_b64_tr_b16 v[74:75], v217 offset:36864\n"
    "ds_read_b64_tr_b16 v[76:77], v217 offset:40960\n"
    "ds_read_b64_tr_b16 v[78:79], v217 offset:45056\n"
    "ds_read_b64_tr_b16 v[80:81], v216 offset:33024\n"
    "ds_read_b64_tr_b16 v[82:83], v216 offset:37120\n"
    "ds_read_b64_tr_b16 v[84:85], v216 offset:41216\n"
    "ds_read_b64_tr_b16 v[86:87], v216 offset:45312\n"
    "ds_read_b64_tr_b16 v[88:89], v217 offset:33024\n"
    "ds_read_b64_tr_b16 v[90:91], v217 offset:37120\n"
    "ds_read_b64_tr_b16 v[92:93], v217 offset:41216\n"
    "ds_read_b64_tr_b16 v[94:95], v217 offset:45312\n"
    "ds_read_b64_tr_b16 v[96:97], v216 offset:33280\n"
    "ds_read_b64_tr_b16 v[98:99], v216 offset:37376\n"
    "ds_read_b64_tr_b16 v[100:101], v216 offset:41472\n"
    "ds_read_b64_tr_b16 v[102:103], v216 offset:45568\n"
    "ds_read_b64_tr_b16 v[104:105], v217 offset:33280\n"
    "ds_read_b64_tr_b16 v[106:107], v217 offset:37376\n"
    "ds_read_b64_tr_b16 v[108:109], v217 offset:41472\n"
    "ds_read_b64_tr_b16 v[110:111], v217 offset:45568\n"
    "ds_read_b64_tr_b16 v[112:113], v216 offset:33536\n"
    "ds_read_b64_tr_b16 v[114:115], v216 offset:37632\n"
    "ds_read_b64_tr_b16 v[116:117], v216 offset:41728\n"
    "ds_read_b64_tr_b16 v[118:119], v216 offset:45824\n"
    "ds_read_b64_tr_b16 v[120:121], v217 offset:33536\n"
    "ds_read_b64_tr_b16 v[122:123], v217 offset:37632\n"
    "ds_read_b64_tr_b16 v[124:125], v217 offset:41728\n"
    "ds_read_b64_tr_b16 v[126:127], v217 offset:45824\n"
    "s_waitcnt vmcnt(8) lgkmcnt(0)\n"
    "v_cmp_ne_u64 vcc, v[230:231], v[228:229]\n"
    "s_and_b32 s73, vcc_lo, 255\n"
    "s_cbranch_scc1 P1SR_%=\n"
    "P1BR_%=:\n"
    "s_add_u32 s41, s40, 0\n"
    "buffer_load_dwordx4 a[0:3], v209, s[52:55], s41 offen offset:0 sc1\n"
    "buffer_load_dwordx4 a[8:11], v209, s[52:55], s41 offen offset:1024 sc1\n"
    "buffer_load_dwordx4 a[4:7], v226, s[52:55], s41 offen offset:0 sc1\n"
    "buffer_load_dwordx4 a[12:15], v226, s[52:55], s41 offen offset:1024 sc1\n"
    "buffer_load_dwordx4 a[16:19], v209, s[52:55], s41 offen offset:2048 sc1\n"
    "buffer_load_dwordx4 a[24:27], v209, s[52:55], s41 offen offset:3072 sc1\n"
    "buffer_load_dwordx4 a[20:23], v226, s[52:55], s41 offen offset:2048 sc1\n"
    "buffer_load_dwordx4 a[28:31], v226, s[52:55], s41 offen offset:3072 sc1\n"
    "s_add_u32 s41, s40, 4096\n"
    "buffer_load_dwordx4 a[32:35], v209, s[52:55], s41 offen offset:0 sc1\n"
    "buffer_load_dwordx4 a[40:43], v209, s[52:55], s41 offen offset:1024 sc1\n"
    "buffer_load_dwordx4 a[36:39], v226, s[52:55], s41 offen offset:0 sc1\n"
    "buffer_load_dwordx4 a[44:47], v226, s[52:55], s41 offen offset:1024 sc1\n"
    "s_add_u32 m0, s66, 81920\n"
    "s_add_u32 s72, s65, 409600\n"
    "buffer_load_dwordx4 v210, s[84:87], s72 offen offset:0 lds\n"
    "buffer_load_dwordx4 v211, s[84:87], s72 offen offset:1024 lds\n"
    "buffer_load_dwordx4 v212, s[84:87], s72 offen offset:2048 lds\n"
    "buffer_load_dwordx4 v213, s[84:87], s72 offen offset:3072 lds\n"
    "s_add_u32 m0, s66, 0\n"
    "s_add_u32 s72, s65, 491520\n"
    "buffer_load_dwordx4 v210, s[84:87], s72 offen offset:0 lds\n"
    "buffer_load_dwordx4 v211, s[84:87], s72 offen offset:1024 lds\n"
    "buffer_load_dwordx4 v212, s[84:87], s72 offen offset:2048 lds\n"
    "buffer_load_dwordx4 v213, s[84:87], s72 offen offset:3072 lds\n"
    "s_waitcnt vmcnt(16)\n"
    "v_mfma_f32_16x16x32_bf16 v[0:3], v[64:67], a[0:3], 0\n"
    "v_mfma_f32_16x16x32_bf16 v[0:3], v[68:71], a[8:11], v[0:3]\n"
    "v_mfma_f32_16x16x32_bf16 v[4:7], v[64:67], a[4:7], 0\n"
    "v_mfma_f32_16x16x32_bf16 v[4:7], v[68:71], a[12:15], v[4:7]\n"
    "v_mfma_f32_16x16x32_bf16 v[12:15], v[72:75], a[4:7], 0\n"
    "ds_read_b64_tr_b16 v[64:65], v216 offset:49152\n"
    "buffer_load_dwordx4 a[48:51], v209, s[52:55], s41 offen offset:2048 sc1\n"
    "v_mfma_f32_16x16x32_bf16 v[12:15], v[76:79], a[12:15], v[12:15]\n"
    "ds_read_b64_tr_b16 v[66:67], v216 offset:53248\n"
    "v_mfma_f32_16x16x32_bf16 v[8:11], v[72:75], a[0:3], 0\n"
    "ds_read_b64_tr_b16 v[68:69], v216 offset:57344\n"
    "v_mfma_f32_16x16x32_bf16 v[8:11], v[76:79], a[8:11], v[8:11]\n"
    "ds_read_b64_tr_b16 v[70:71], v216 offset:61440\n"
    "v_mfma_f32_16x16x32_bf16 v[16:19], v[80:83], a[0:3], 0\n"
    "ds_read_b64_tr_b16 v[72:73], v217 offset:49152\n"
    "v_mfma_f32_16x16x32_bf16 v[16:19], v[84:87], a[8:11], v[16:19]\n"
    "ds_read_b64_tr_b16 v[74:75], v217 offset:53248\n"
    "v_mfma_f32_16x16x32_bf16 v[20:23], v[80:83], a[4:7], 0\n"
    "ds_read_b64_tr_b16 v[76:77], v217 offset:57344\n"
    "v_mfma_f32_16x16x32_bf16 v[20:23], v[84:87], a[12:15], v[20:23]\n"
    "ds_read_b64_tr_b16 v[78:79], v217 offset:61440\n"
    "v_mfma_f32_16x16x32_bf16 v[28:31], v[88:91], a[4:7], 0\n"
    "ds_read_b64_tr_b16 v[80:81], v216 offset:49408\n"
    "buffer_load_dwordx4 a[56:59], v209, s[52:55], s41 offen offset:3072 sc1\n"
    "v_mfma_f32_16x16x32_bf16 v[28:31], v[92:95], a[12:15], v[28:31]\n"
    "ds_read_b64_tr_b16 v[82:83], v216 offset:53504\n"
    "v_mfma_f32_16x16x32_bf16 v[24:27], v[88:91], a[0:3], 0\n"
    "ds_read_b64_tr_b16 v[84:85], v216 offset:57600\n"
    "v_mfma_f32_16x16x32_bf16 v[24:27], v[92:95], a[8:11], v[24:27]\n"
    "ds_read_b64_tr_b16 v[86:87], v216 offset:61696\n"
    "v_mfma_f32_16x16x32_bf16 v[32:35], v[96:99], a[0:3], 0\n"
    "ds_read_b64_tr_b16 v[88:89], v217 offset:49408\n"
    "v_mfma_f32_16x16x32_bf16 v[32:35], v[100:103], a[8:11], v[32:35]\n"
    "ds_read_b64_tr_b16 v[90:91], v217 offset:53504\n"
    "v_mfma_f32_16x16x32_bf16 v[36:39], v[96:99], a[4:7], 0\n"
    "ds_read_b64_tr_b16 v[92:93], v217 offset:57600\n"
    "v_mfma_f32_16x16x32_bf16 v[36:39], v[100:103], a[12:15], v[36:39]\n"
    "ds_read_b64_tr_b16 v[94:95], v217 offset:61696\n"
    "v_mfma_f32_16x16x32_bf16 v[44:47], v[104:107], a[4:7], 0\n"
    "ds_read_b64_tr_b16 v[96:97], v216 offset:49664\n"
    "buffer_load_dwordx4 a[52:55], v226, s[52:55], s41 offen offset:2048 sc1\n"
    "v_mfma_f32_16x16x32_bf16 v[44:47], v[108:111], a[12:15], v[44:47]\n"
    "ds_read_b64_tr_b16 v[98:99], v216 offset:53760\n"
    "v_mfma_f32_16x16x32_bf16 v[40:43], v[104:107], a[0:3], 0\n"
    "ds_read_b64_tr_b16 v[100:101], v216 offset:57856\n"
    "v_mfma_f32_16x16x32_bf16 v[40:43], v[108:111], a[8:11], v[40:43]\n"
    "ds_read_b64_tr_b16 v[102:103], v216 offset:61952\n"
    "v_mfma_f32_16x16x32_bf16 v[48:51], v[112:115], a[0:3], 0\n"
    "ds_read_b64_tr_b16 v[104:105], v217 offset:49664\n"
    "v_mfma_f32_16x16x32_bf16 v[48:51], v[116:119], a[8:11], v[48:51]\n"
    "ds_read_b64_tr_b16 v[106:107], v217 offset:53760\n"
    "v_mfma_f32_16x16x32_bf16 v[52:55], v[112:115], a[4:7], 0\n"
    "ds_read_b64_tr_b16 v[108:109], v217 offset:57856\n"
    "v_mfma_f32_16x16x32_bf16 v[52:55], v[116:119], a[12:15], v[52:55]\n"
    "ds_read_b64_tr_b16 v[110:111], v217 offset:61952\n"
    "v_mfma_f32_16x16x32_bf16 v[60:63], v[120:123], a[4:7], 0\n"
    "ds_read_b64_tr_b16 v[112:113], v216 offset:49920\n"
    "buffer_load_dwordx4 a[60:63], v226, s[52:55], s41 offen offset:3072 sc1\n"
    "v_mfma_f32_16x16x32_bf16 v[60:63], v[124:127], a[12:15], v[60:63]\n"
    "ds_read_b64_tr_b16 v[114:115], v216 offset:54016\n"
    "v_mfma_f32_16x16x32_bf16 v[56:59], v[120:123], a[0:3], 0\n"
    "ds_read_b64_tr_b16 v[116:117], v216 offset:58112\n"
    "v_mfma_f32_16x16x32_bf16 v[56:59], v[124:127], a[8:11], v[56:59]\n"
    "ds_read_b64_tr_b16 v[118:119], v216 offset:62208\n"
    "s_waitcnt vmcnt(16)\n"
    "s_barrier\n"
    "s_waitcnt lgkmcnt(8)\n"
    "v_mfma_f32_16x16x32_bf16 v[0:3], v[64:67], a[16:19], v[0:3]\n"
    "ds_read_b64_tr_b16 v[120:121], v217 offset:49920\n"
    "v_mfma_f32_16x16x32_bf16 v[0:3], v[68:71], a[24:27], v[0:3]\n"
    "ds_read_b64_tr_b16 v[122:123], v217 offset:54016\n"
    "v_mfma_f32_16x16x32_bf16 v[4:7], v[64:67], a[20:23], v[4:7]\n"
    "ds_read_b64_tr_b16 v[124:125], v217 offset:58112\n"
    "v_mfma_f32_16x16x32_bf16 v[4:7], v[68:71], a[28:31], v[4:7]\n"
    "ds_read_b64_tr_b16 v[126:127], v217 offset:62208\n"
    "s_add_u32 s41, s40, 8192\n"
    "v_mfma_f32_16x16x32_bf16 v[12:15], v[72:75], a[20:23], v[12:15]\n"
    "ds_read_b64_tr_b16 v[64:65], v218 offset:0\n"
    "v_mfma_f32_16x16x32_bf16 v[12:15], v[76:79], a[28:31], v[12:15]\n"
    "ds_read_b64_tr_b16 v[66:67], v218 offset:4096\n"
    "v_mfma_f32_16x16x32_bf16 v[8:11], v[72:75], a[16:19], v[8:11]\n"
    "ds_read_b64_tr_b16 v[68:69], v218 offset:8192\n"
    "v_mfma_f32_16x16x32_bf16 v[8:11], v[76:79], a[24:27], v[8:11]\n"
    "ds_read_b64_tr_b16 v[70:71], v218 offset:12288\n"
    "v_mfma_f32_16x16x32_bf16 v[16:19], v[80:83], a[16:19], v[16:19]\n"
    "ds_read_b64_tr_b16 v[72:73], v219 offset:0\n"
    "v_mfma_f32_16x16x32_bf16 v[16:19], v[84:87], a[24:27], v[16:19]\n"
    "ds_read_b64_tr_b16 v[74:75], v219 offset:4096\n"
    "buffer_load_dwordx4 a[64:67], v209, s[52:55], s41 offen offset:0 sc1\n"
    "v_mfma_f32_16x16x32_bf16 v[20:23], v[80:83], a[20:23], v[20:23]\n"
    "ds_read_b64_tr_b16 v[76:77], v219 offset:8192\n"
    "v_mfma_f32_16x16x32_bf16 v[20:23], v[84:87], a[28:31], v[20:23]\n"
    "ds_read_b64_tr_b16 v[78:79], v219 offset:12288\n"
    "v_mfma_f32_16x16x32_bf16 v[28:31], v[88:91], a[20:23], v[28:31]\n"
    "ds_read_b64_tr_b16 v[80:81], v218 offset:256\n"
    "v_mfma_f32_16x16x32_bf16 v[28:31], v[92:95], a[28:31], v[28:31]\n"
    "ds_read_b64_tr_b16 v[82:83], v218 offset:4352\n"
    "v_mfma_f32_16x16x32_bf16 v[24:27], v[88:91], a[16:19], v[24:27]\n"
    "ds_read_b64_tr_b16 v[84:85], v218 offset:8448\n"
    "v_mfma_f32_16x16x32_bf16 v[24:27], v[92:95], a[24:27], v[24:27]\n"
    "ds_read_b64_tr_b16 v[86:87], v218 offset:12544\n"
    "v_mfma_f32_16x16x32_bf16 v[32:35], v[96:99], a[16:19], v[32:35]\n"
    "ds_read_b64_tr_b16 v[88:89], v219 offset:256\n"
    "buffer_load_dwordx4 a[72:75], v209, s[52:55], s41 offen offset:1024 sc1\n"
    "v_mfma_f32_16x16x32_bf16 v[32:35], v[100:103], a[24:27], v[32:35]\n"
    "ds_read_b64_tr_b16 v[90:91], v219 offset:4352\n"
    "v_mfma_f32_16x16x32_bf16 v[36:39], v[96:99], a[20:23], v[36:39]\n"
    "ds_read_b64_tr_b16 v[92:93], v219 offset:8448\n"
    "v_mfma_f32_16x16x32_bf16 v[36:39], v[100:103], a[28:31], v[36:39]\n"
    "ds_read_b64_tr_b16 v[94:95], v219 offset:12544\n"
    "s_waitcnt lgkmcnt(9)\n"
    "v_mfma_f32_16x16x32_bf16 v[44:47], v[104:107], a[20:23], v[44:47]\n"
    "ds_read_b64_tr_b16 v[96:97], v218 offset:512\n"
    "v_mfma_f32_16x16x32_bf16 v[44:47], v[108:111], a[28:31], v[44:47]\n"
    "ds_read_b64_tr_b16 v[98:99], v218 offset:4608\n"
    "v_mfma_f32_16x16x32_bf16 v[40:43], v[104:107], a[16:19], v[40:43]\n"
    "ds_read_b64_tr_b16 v[100:101], v218 offset:8704\n"
    "buffer_load_dwordx4 a[68:71], v226, s[52:55], s41 offen offset:0 sc1\n"
    "v_mfma_f32_16x16x32_bf16 v[40:43], v[108:111], a[24:27], v[40:43]\n"
    "ds_read_b64_tr_b16 v[102:103], v218 offset:12800\n"
    "v_mfma_f32_16x16x32_bf16 v[48:51], v[112:115], a[16:19], v[48:51]\n"
    "ds_read_b64_tr_b16 v[104:105], v219 offset:512\n"
    "v_mfma_f32_16x16x32_bf16 v[48:51], v[116:119], a[24:27], v[48:51]\n"
    "ds_read_b64_tr_b16 v[106:107], v219 offset:4608\n"
    "v_mfma_f32_16x16x32_bf16 v[52:55], v[112:115], a[20:23], v[52:55]\n"
    "ds_read_b64_tr_b16 v[108:109], v219 offset:8704\n"
    "v_mfma_f32_16x16x32_bf16 v[52:55], v[116:119], a[28:31], v[52:55]\n"
    "ds_read_b64_tr_b16 v[110:111], v219 offset:12800\n"
    "v_mfma_f32_16x16x32_bf16 v[60:63], v[120:123], a[20:23], v[60:63]\n"
    "ds_read_b64_tr_b16 v[112:113], v218 offset:768\n"
    "buffer_load_dwordx4 a[76:79], v226, s[52:55], s41 offen offset:1024 sc1\n"
    "v_mfma_f32_16x16x32_bf16 v[60:63], v[124:127], a[28:31], v[60:63]\n"
    "ds_read_b64_tr_b16 v[114:115], v218 offset:4864\n"
    "v_mfma_f32_16x16x32_bf16 v[56:59], v[120:123], a[16:19], v[56:59]\n"
    "ds_read_b64_tr_b16 v[116:117], v218 offset:8960\n"
    "v_mfma_f32_16x16x32_bf16 v[56:59], v[124:127], a[24:27], v[56:59]\n"
    "ds_read_b64_tr_b16 v[118:119], v218 offset:13056\n"
    "s_waitcnt vmcnt(16)\n"
    "v_mfma_f32_16x16x32_bf16 v[0:3], v[64:67], a[32:35], v[0:3]\n"
    "ds_read_b64_tr_b16 v[120:121], v219 offset:768\n"
    "v_mfma_f32_16x16x32_bf16 v[0:3], v[68:71], a[40:43], v[0:3]\n"
    "ds_read_b64_tr_b16 v[122:123], v219 offset:4864\n"
    "buffer_load_dwordx4 a[80:83], v209, s[52:55], s41 offen offset:2048 sc1\n"
    "v_mfma_f32_16x16x32_bf16 v[4:7], v[64:67], a[36:39], v[4:7]\n"
    "ds_read_b64_tr_b16 v[124:125], v219 offset:8960\n"
    "v_mfma_f32_16x16x32_bf16 v[4:7], v[68:71], a[44:47], v[4:7]\n"
    "ds_read_b64_tr_b16 v[126:127], v219 offset:13056\n"
    "v_mfma_f32_16x16x32_bf16 v[12:15], v[72:75], a[36:39], v[12:15]\n"
    "ds_read_b64_tr_b16 v[64:65], v218 offset:16384\n"
    "buffer_load_dwordx4 a[88:91], v209, s[52:55], s41 offen offset:3072 sc1\n"
    "s_waitcnt lgkmcnt(8)\n"
    "v_mfma_f32_16x16x32_bf16 v[12:15], v[76:79], a[44:47], v[12:15]\n"
    "ds_read_b64_tr_b16 v[66:67], v218 offset:20480\n"
    "v_mfma_f32_16x16x32_bf16 v[8:11], v[72:75], a[32:35], v[8:11]\n"
    "ds_read_b64_tr_b16 v[68:69], v218 offset:24576\n"
    "v_mfma_f32_16x16x32_bf16 v[8:11], v[76:79], a[40:43], v[8:11]\n"
    "ds_read_b64_tr_b16 v[70:71], v218 offset:28672\n"
    "v_mfma_f32_16x16x32_bf16 v[16:19], v[80:83], a[32:35], v[16:19]\n"
    "ds_read_b64_tr_b16 v[72:73], v219 offset:16384\n"
    "buffer_load_dwordx4 a[84:87], v226, s[52:55], s41 offen offset:2048 sc1\n"
    "v_mfma_f32_16x16x32_bf16 v[16:19], v[84:87], a[40:43], v[16:19]\n"
    "ds_read_b64_tr_b16 v[74:75], v219 offset:20480\n"
    "v_mfma_f32_16x16x32_bf16 v[20:23], v[80:83], a[36:39], v[20:23]\n"
    "ds_read_b64_tr_b16 v[76:77], v219 offset:24576\n"
    "v_mfma_f32_16x16x32_bf16 v[20:23], v[84:87], a[44:47], v[20:23]\n"
    "ds_read_b64_tr_b16 v[78:79], v219 offset:28672\n"
    "buffer_load_dwordx4 a[92:95], v226, s[52:55], s41 offen offset:3072 sc1\n"
    "v_mfma_f32_16x16x32_bf16 v[28:31], v[88:91], a[36:39], v[28:31]\n"
    "ds_read_b64_tr_b16 v[80:81], v218 offset:16640\n"
    "v_mfma_f32_16x16x32_bf16 v[28:31], v[92:95], a[44:47], v[28:31]\n"
    "ds_read_b64_tr_b16 v[82:83], v218 offset:20736\n"
    "v_mfma_f32_16x16x32_bf16 v[24:27], v[88:91], a[32:35], v[24:27]\n"
    "ds_read_b64_tr_b16 v[84:85], v218 offset:24832\n"
    "s_add_u32 m0, s66, 16384\n"
    "v_mfma_f32_16x16x32_bf16 v[24:27], v[92:95], a[40:43], v[24:27]\n"
    "ds_read_b64_tr_b16 v[86:87], v218 offset:28928\n"
    "v_mfma_f32_16x16x32_bf16 v[32:35], v[96:99], a[32:35], v[32:35]\n"
    "ds_read_b64_tr_b16 v[88:89], v219 offset:16640\n"
    "v_mfma_f32_16x16x32_bf16 v[32:35], v[100:103], a[40:43], v[32:35]\n"
    "ds_read_b64_tr_b16 v[90:91], v219 offset:20736\n"
    "s_add_u32 s72, s65, 573440\n"
    "v_mfma_f32_16x16x32_bf16 v[36:39], v[96:99], a[36:39], v[36:39]\n"
    "ds_read_b64_tr_b16 v[92:93], v219 offset:24832\n"
    "v_mfma_f32_16x16x32_bf16 v[36:39], v[100:103], a[44:47], v[36:39]\n"
    "ds_read_b64_tr_b16 v[94:95], v219 offset:28928\n"
    "v_mfma_f32_16x16x32_bf16 v[44:47], v[104:107], a[36:39], v[44:47]\n"
    "ds_read_b64_tr_b16 v[96:97], v218 offset:16896\n"
    "buffer_load_dwordx4 v210, s[84:87], s72 offen offset:0 lds\n"
    "v_mfma_f32_16x16x32_bf16 v[44:47], v[108:111], a[44:47], v[44:47]\n"
    "ds_read_b64_tr_b16 v[98:99], v218 offset:20992\n"
    "v_mfma_f32_16x16x32_bf16 v[40:43], v[104:107], a[32:35], v[40:43]\n"
    "ds_read_b64_tr_b16 v[100:101], v218 offset:25088\n"
    "v_mfma_f32_16x16x32_bf16 v[40:43], v[108:111], a[40:43], v[40:43]\n"
    "ds_read_b64_tr_b16 v[102:103], v218 offset:29184\n"
    "s_waitcnt lgkmcnt(9)\n"
    "v_mfma_f32_16x16x32_bf16 v[48:51], v[112:115], a[32:35], v[48:51]\n"
    "ds_read_b64_tr_b16 v[104:105], v219 offset:16896\n"
    "buffer_load_dwordx4 v211, s[84:87], s72 offen offset:1024 lds\n"
    "v_mfma_f32_16x16x32_bf16 v[48:51], v[116:119], a[40:43], v[48:51]\n"
    "ds_read_b64_tr_b16 v[106:107], v219 offset:20992\n"
    "v_mfma_f32_16x16x32_bf16 v[52:55], v[112:115], a[36:39], v[52:55]\n"
    "ds_read_b64_tr_b16 v[108:109], v219 offset:25088\n"
    "v_mfma_f32_16x16x32_bf16 v[52:55], v[116:119], a[44:47], v[52:55]\n"
    "ds_read_b64_tr_b16 v[110:111], v219 offset:29184\n"
    "buffer_load_dwordx4 v212, s[84:87], s72 offen offset:2048 lds\n"
    "v_mfma_f32_16x16x32_bf16 v[60:63], v[120:123], a[36:39], v[60:63]\n"
    "ds_read_b64_tr_b16 v[112:113], v218 offset:17152\n"
    "v_mfma_f32_16x16x32_bf16 v[60:63], v[124:127], a[44:47], v[60:63]\n"
    "ds_read_b64_tr_b16 v[114:115], v218 offset:21248\n"
    "v_mfma_f32_16x16x32_bf16 v[56:59], v[120:123], a[32:35], v[56:59]\n"
    "ds_read_b64_tr_b16 v[116:117], v218 offset:25344\n"
    "buffer_load_dwordx4 v213, s[84:87], s72 offen offset:3072 lds\n"
    "v_mfma_f32_16x16x32_bf16 v[56:59], v[124:127], a[40:43], v[56:59]\n"
    "ds_read_b64_tr_b16 v[118:119], v218 offset:29440\n"
    "s_waitcnt vmcnt(12)\n"
    "s_barrier\n"
    "v_mfma_f32_16x16x32_bf16 v[0:3], v[64:67], a[48:51], v[0:3]\n"
    "ds_read_b64_tr_b16 v[120:121], v219 offset:17152\n"
    "v_mfma_f32_16x16x32_bf16 v[0:3], v[68:71], a[56:59], v[0:3]\n"
    "ds_read_b64_tr_b16 v[122:123], v219 offset:21248\n"
    "s_add_u32 s41, s40, 12288\n"
    "v_mfma_f32_16x16x32_bf16 v[4:7], v[64:67], a[52:55], v[4:7]\n"
    "ds_read_b64_tr_b16 v[124:125], v219 offset:25344\n"
    "v_mfma_f32_16x16x32_bf16 v[4:7], v[68:71], a[60:63], v[4:7]\n"
    "ds_read_b64_tr_b16 v[126:127], v219 offset:29440\n"
    "v_mfma_f32_16x16x32_bf16 v[12:15], v[72:75], a[52:55], v[12:15]\n"
    "ds_read_b64_tr_b16 v[64:65], v216 offset:0\n"
    "buffer_load_dwordx4 a[96:99], v209, s[52:55], s41 offen offset:0 sc1\n"
    "v_mfma_f32_16x16x32_bf16 v[12:15], v[76:79], a[60:63], v[12:15]\n"
    "ds_read_b64_tr_b16 v[66:67], v216 offset:4096\n"
    "v_mfma_f32_16x16x32_bf16 v[8:11], v[72:75], a[48:51], v[8:11]\n"
    "ds_read_b64_tr_b16 v[68:69], v216 offset:8192\n"
    "v_mfma_f32_16x16x32_bf16 v[8:11], v[76:79], a[56:59], v[8:11]\n"
    "ds_read_b64_tr_b16 v[70:71], v216 offset:12288\n"
    "buffer_load_dwordx4 a[104:107], v209, s[52:55], s41 offen offset:1024 sc1\n"
    "v_mfma_f32_16x16x32_bf16 v[16:19], v[80:83], a[48:51], v[16:19]\n"
    "ds_read_b64_tr_b16 v[72:73], v217 offset:0\n"
    "s_waitcnt lgkmcnt(8)\n"
    "v_mfma_f32_16x16x32_bf16 v[16:19], v[84:87], a[56:59], v[16:19]\n"
    "ds_read_b64_tr_b16 v[74:75], v217 offset:4096\n"
    "v_mfma_f32_16x16x32_bf16 v[20:23], v[80:83], a[52:55], v[20:23]\n"
    "ds_read_b64_tr_b16 v[76:77], v217 offset:8192\n"
    "buffer_load_dwordx4 a[100:103], v226, s[52:55], s41 offen offset:0 sc1\n"
    "v_mfma_f32_16x16x32_bf16 v[20:23], v[84:87], a[60:63], v[20:23]\n"
    "ds_read_b64_tr_b16 v[78:79], v217 offset:12288\n"
    "v_mfma_f32_16x16x32_bf16 v[28:31], v[88:91], a[52:55], v[28:31]\n"
    "ds_read_b64_tr_b16 v[80:81], v216 offset:256\n"
    "v_mfma_f32_16x16x32_bf16 v[28:31], v[92:95], a[60:63], v[28:31]\n"
    "ds_read_b64_tr_b16 v[82:83], v216 offset:4352\n"
    "buffer_load_dwordx4 a[108:111], v226, s[52:55], s41 offen offset:1024 sc1\n"
    "v_mfma_f32_16x16x32_bf16 v[24:27], v[88:91], a[48:51], v[24:27]\n"
    "ds_read_b64_tr_b16 v[84:85], v216 offset:8448\n"
    "v_mfma_f32_16x16x32_bf16 v[24:27], v[92:95], a[56:59], v[24:27]\n"
    "ds_read_b64_tr_b16 v[86:87], v216 offset:12544\n"
    "v_mfma_f32_16x16x32_bf16 v[32:35], v[96:99], a[48:51], v[32:35]\n"
    "ds_read_b64_tr_b16 v[88:89], v217 offset:256\n"
    "s_add_u32 m0, s66, 32768\n"
    "v_mfma_f32_16x16x32_bf16 v[32:35], v[100:103], a[56:59], v[32:35]\n"
    "ds_read_b64_tr_b16 v[90:91], v217 offset:4352\n"
    "v_mfma_f32_16x16x32_bf16 v[36:39], v[96:99], a[52:55], v[36:39]\n"
    "ds_read_b64_tr_b16 v[92:93], v217 offset:8448\n"
    "s_add_u32 s72, s65, 655360\n"
    "v_mfma_f32_16x16x32_bf16 v[36:39], v[100:103], a[60:63], v[36:39]\n"
    "ds_read_b64_tr_b16 v[94:95], v217 offset:12544\n"
    "v_mfma_f32_16x16x32_bf16 v[44:47], v[104:107], a[52:55], v[44:47]\n"
    "ds_read_b64_tr_b16 v[96:97], v216 offset:512\n"
    "v_mfma_f32_16x16x32_bf16 v[44:47], v[108:111], a[60:63], v[44:47]\n"
    "ds_read_b64_tr_b16 v[98:99], v216 offset:4608\n"
    "buffer_load_dwordx4 v210, s[84:87], s72 offen offset:0 lds\n"
    "v_mfma_f32_16x16x32_bf16 v[40:43], v[104:107], a[48:51], v[40:43]\n"
    "ds_read_b64_tr_b16 v[100:101], v216 offset:8704\n"
    "v_mfma_f32_16x16x32_bf16 v[40:43], v[108:111], a[56:59], v[40:43]\n"
    "ds_read_b64_tr_b16 v[102:103], v216 offset:12800\n"
    "v_mfma_f32_16x16x32_bf16 v[48:51], v[112:115], a[48:51], v[48:51]\n"
    "ds_read_b64_tr_b16 v[104:105], v217 offset:512\n"
    "buffer_load_dwordx4 v211, s[84:87], s72 offen offset:1024 lds\n"
    "v_mfma_f32_16x16x32_bf16 v[48:51], v[116:119], a[56:59], v[48:51]\n"
    "ds_read_b64_tr_b16 v[106:107], v217 offset:4608\n"
    "v_mfma_f32_16x16x32_bf16 v[52:55], v[112:115], a[52:55], v[52:55]\n"
    "ds_read_b64_tr_b16 v[108:109], v217 offset:8704\n"
    "v_mfma_f32_16x16x32_bf16 v[52:55], v[116:119], a[60:63], v[52:55]\n"
    "ds_read_b64_tr_b16 v[110:111], v217 offset:12800\n"
    "buffer_load_dwordx4 v212, s[84:87], s72 offen offset:2048 lds\n"
    "s_waitcnt lgkmcnt(8)\n"
    "v_mfma_f32_16x16x32_bf16 v[60:63], v[120:123], a[52:55], v[60:63]\n"
    "ds_read_b64_tr_b16 v[112:113], v216 offset:768\n"
    "v_mfma_f32_16x16x32_bf16 v[60:63], v[124:127], a[60:63], v[60:63]\n"
    "ds_read_b64_tr_b16 v[114:115], v216 offset:4864\n"
    "v_mfma_f32_16x16x32_bf16 v[56:59], v[120:123], a[48:51], v[56:59]\n"
    "ds_read_b64_tr_b16 v[116:117], v216 offset:8960\n"
    "buffer_load_dwordx4 v213, s[84:87], s72 offen offset:3072 lds\n"
    "v_mfma_f32_16x16x32_bf16 v[56:59], v[124:127], a[56:59], v[56:59]\n"
    "ds_read_b64_tr_b16 v[118:119], v216 offset:13056\n"
    "s_waitcnt vmcnt(16)\n"
    "v_mfma_f32_16x16x32_bf16 v[0:3], v[64:67], a[64:67], v[0:3]\n"
    "ds_read_b64_tr_b16 v[120:121], v217 offset:768\n"
    "v_mfma_f32_16x16x32_bf16 v[0:3], v[68:71], a[72:75], v[0:3]\n"
    "ds_read_b64_tr_b16 v[122:123], v217 offset:4864\n"
    "buffer_load_dwordx4 a[112:115], v209, s[52:55], s41 offen offset:2048 sc1\n"
    "v_mfma_f32_16x16x32_bf16 v[4:7], v[64:67], a[68:71], v[4:7]\n"
    "ds_read_b64_tr_b16 v[124:125], v217 offset:8960\n"
    "v_mfma_f32_16x16x32_bf16 v[4:7], v[68:71], a[76:79], v[4:7]\n"
    "ds_read_b64_tr_b16 v[126:127], v217 offset:13056\n"
    "v_mfma_f32_16x16x32_bf16 v[12:15], v[72:75], a[68:71], v[12:15]\n"
    "ds_read_b64_tr_b16 v[64:65], v216 offset:16384\n"
    "buffer_load_dwordx4 a[120:123], v209, s[52:55], s41 offen offset:3072 sc1\n"
    "v_mfma_f32_16x16x32_bf16 v[12:15], v[76:79], a[76:79], v[12:15]\n"
    "ds_read_b64_tr_b16 v[66:67], v216 offset:20480\n"
    "v_mfma_f32_16x16x32_bf16 v[8:11], v[72:75], a[64:67], v[8:11]\n"
    "ds_read_b64_tr_b16 v[68:69], v216 offset:24576\n"
    "v_mfma_f32_16x16x32_bf16 v[8:11], v[76:79], a[72:75], v[8:11]\n"
    "ds_read_b64_tr_b16 v[70:71], v216 offset:28672\n"
    "v_mfma_f32_16x16x32_bf16 v[16:19], v[80:83], a[64:67], v[16:19]\n"
    "ds_read_b64_tr_b16 v[72:73], v217 offset:16384\n"
    "buffer_load_dwordx4 a[116:119], v226, s[52:55], s41 offen offset:2048 sc1\n"
    "v_mfma_f32_16x16x32_bf16 v[16:19], v[84:87], a[72:75], v[16:19]\n"
    "ds_read_b64_tr_b16 v[74:75], v217 offset:20480\n"
    "v_mfma_f32_16x16x32_bf16 v[20:23], v[80:83], a[68:71], v[20:23]\n"
    "ds_read_b64_tr_b16 v[76:77], v217 offset:24576\n"
    "v_mfma_f32_16x16x32_bf16 v[20:23], v[84:87], a[76:79], v[20:23]\n"
    "ds_read_b64_tr_b16 v[78:79], v217 offset:28672\n"
    "buffer_load_dwordx4 a[124:127], v226, s[52:55], s41 offen offset:3072 sc1\n"
    "v_mfma_f32_16x16x32_bf16 v[28:31], v[88:91], a[68:71], v[28:31]\n"
    "ds_read_b64_tr_b16 v[80:81], v216 offset:16640\n"
    "v_mfma_f32_16x16x32_bf16 v[28:31], v[92:95], a[76:79], v[28:31]\n"
    "ds_read_b64_tr_b16 v[82:83], v216 offset:20736\n"
    "v_mfma_f32_16x16x32_bf16 v[24:27], v[88:91], a[64:67], v[24:27]\n"
    "ds_read_b64_tr_b16 v[84:85], v216 offset:24832\n"
    "s_add_u32 m0, s66, 49152\n"
    "v_mfma_f32_16x16x32_bf16 v[24:27], v[92:95], a[72:75], v[24:27]\n"
    "ds_read_b64_tr_b16 v[86:87], v216 offset:28928\n"
    "s_waitcnt lgkmcnt(8)\n"
    "v_mfma_f32_16x16x32_bf16 v[32:35], v[96:99], a[64:67], v[32:35]\n"
    "ds_read_b64_tr_b16 v[88:89], v217 offset:16640\n"
    "v_mfma_f32_16x16x32_bf16 v[32:35], v[100:103], a[72:75], v[32:35]\n"
    "ds_read_b64_tr_b16 v[90:91], v217 offset:20736\n"
    "s_add_u32 s72, s65, 737280\n"
    "v_mfma_f32_16x16x32_bf16 v[36:39], v[96:99], a[68:71], v[36:39]\n"
    "ds_read_b64_tr_b16 v[92:93], v217 offset:24832\n"
    "v_mfma_f32_16x16x32_bf16 v[36:39], v[100:103], a[76:79], v[36:39]\n"
    "ds_read_b64_tr_b16 v[94:95], v217 offset:28928\n"
    "v_mfma_f32_16x16x32_bf16 v[44:47], v[104:107], a[68:71], v[44:47]\n"
    "ds_read_b64_tr_b16 v[96:97], v216 offset:16896\n"
    "buffer_load_dwordx4 v210, s[84:87], s72 offen offset:0 lds\n"
    "v_mfma_f32_16x16x32_bf16 v[44:47], v[108:111], a[76:79], v[44:47]\n"
    "ds_read_b64_tr_b16 v[98:99], v216 offset:20992\n"
    "v_mfma_f32_16x16x32_bf16 v[40:43], v[104:107], a[64:67], v[40:43]\n"
    "ds_read_b64_tr_b16 v[100:101], v216 offset:25088\n"
    "v_mfma_f32_16x16x32_bf16 v[40:43], v[108:111], a[72:75], v[40:43]\n"
    "ds_read_b64_tr_b16 v[102:103], v216 offset:29184\n"
    "v_mfma_f32_16x16x32_bf16 v[48:51], v[112:115], a[64:67], v[48:51]\n"
    "ds_read_b64_tr_b16 v[104:105], v217 offset:16896\n"
    "buffer_load_dwordx4 v211, s[84:87], s72 offen offset:1024 lds\n"
    "v_mfma_f32_16x16x32_bf16 v[48:51], v[116:119], a[72:75], v[48:51]\n"
    "ds_read_b64_tr_b16 v[106:107], v217 offset:20992\n"
    "v_mfma_f32_16x16x32_bf16 v[52:55], v[112:115], a[68:71], v[52:55]\n"
    "ds_read_b64_tr_b16 v[108:109], v217 offset:25088\n"
    "v_mfma_f32_16x16x32_bf16 v[52:55], v[116:119], a[76:79], v[52:55]\n"
    "ds_read_b64_tr_b16 v[110:111], v217 offset:29184\n"
    "buffer_load_dwordx4 v212, s[84:87], s72 offen offset:2048 lds\n"
    "v_mfma_f32_16x16x32_bf16 v[60:63], v[120:123], a[68:71], v[60:63]\n"
    "ds_read_b64_tr_b16 v[112:113], v216 offset:17152\n"
    "v_mfma_f32_16x16x32_bf16 v[60:63], v[124:127], a[76:79], v[60:63]\n"
    "ds_read_b64_tr_b16 v[114:115], v216 offset:21248\n"
    "v_mfma_f32_16x16x32_bf16 v[56:59], v[120:123], a[64:67], v[56:59]\n"
    "ds_read_b64_tr_b16 v[116:117], v216 offset:25344\n"
    "buffer_load_dwordx4 v213, s[84:87], s72 offen offset:3072 lds\n"
    "v_mfma_f32_16x16x32_bf16 v[56:59], v[124:127], a[72:75], v[56:59]\n"
    "ds_read_b64_tr_b16 v[118:119], v216 offset:29440\n"
    "s_waitcnt vmcnt(16)\n"
    "s_barrier\n"
    "v_mfma_f32_16x16x32_bf16 v[0:3], v[64:67], a[80:83], v[0:3]\n"
    "ds_read_b64_tr_b16 v[120:121], v217 offset:17152\n"
    "v_mfma_f32_16x16x32_bf16 v[0:3], v[68:71], a[88:91], v[0:3]\n"
    "ds_read_b64_tr_b16 v[122:123], v217 offset:21248\n"
    "s_add_u32 s41, s40, 16384\n"
    "v_mfma_f32_16x16x32_bf16 v[4:7], v[64:67], a[84:87], v[4:7]\n"
    "ds_read_b64_tr_b16 v[124:125], v217 offset:25344\n"
    "v_mfma_f32_16x16x32_bf16 v[4:7], v[68:71], a[92:95], v[4:7]\n"
    "ds_read_b64_tr_b16 v[126:127], v217 offset:29440\n"
    "s_waitcnt lgkmcnt(8)\n"
    "v_mfma_f32_16x16x32_bf16 v[12:15], v[72:75], a[84:87], v[12:15]\n"
    "ds_read_b64_tr_b16 v[64:65], v214 offset:0\n"
    "buffer_load_dwordx4 a[128:131], v209, s[52:55], s41 offen offset:0 sc1\n"
    "v_mfma_f32_16x16x32_bf16 v[12:15], v[76:79], a[92:95], v[12:15]\n"
    "ds_read_b64_tr_b16 v[66:67], v214 offset:4096\n"
    "v_mfma_f32_16x16x32_bf16 v[8:11], v[72:75], a[80:83], v[8:11]\n"
    "ds_read_b64_tr_b16 v[68:69], v214 offset:8192\n"
    "v_mfma_f32_16x16x32_bf16 v[8:11], v[76:79], a[88:91], v[8:11]\n"
    "ds_read_b64_tr_b16 v[70:71], v214 offset:12288\n"
    "buffer_load_dwordx4 a[136:139], v209, s[52:55], s41 offen offset:1024 sc1\n"
    "v_mfma_f32_16x16x32_bf16 v[16:19], v[80:83], a[80:83], v[16:19]\n"
    "ds_read_b64_tr_b16 v[72:73], v215 offset:0\n"
    "v_mfma_f32_16x16x32_bf16 v[16:19], v[84:87], a[88:91], v[16:19]\n"
    "ds_read_b64_tr_b16 v[74:75], v215 offset:4096\n"
    "v_mfma_f32_16x16x32_bf16 v[20:23], v[80:83], a[84:87], v[20:23]\n"
    "ds_read_b64_tr_b16 v[76:77], v215 offset:8192\n"
    "buffer_load_dwordx4 a[132:135], v226, s[52:55], s41 offen offset:0 sc1\n"
    "v_mfma_f32_16x16x32_bf16 v[20:23], v[84:87], a[92:95], v[20:23]\n"
    "ds_read_b64_tr_b16 v[78:79], v215 offset:12288\n"
    "v_mfma_f32_16x16x32_bf16 v[28:31], v[88:91], a[84:87], v[28:31]\n"
    "ds_read_b64_tr_b16 v[80:81], v214 offset:256\n"
    "v_mfma_f32_16x16x32_bf16 v[28:31], v[92:95], a[92:95], v[28:31]\n"
    "ds_read_b64_tr_b16 v[82:83], v214 offset:4352\n"
    "buffer_load_dwordx4 a[140:143], v226, s[52:55], s41 offen offset:1024 sc1\n"
    "v_mfma_f32_16x16x32_bf16 v[24:27], v[88:91], a[80:83], v[24:27]\n"
    "ds_read_b64_tr_b16 v[84:85], v214 offset:8448\n"
    "v_mfma_f32_16x16x32_bf16 v[24:27], v[92:95], a[88:91], v[24:27]\n"
    "ds_read_b64_tr_b16 v[86:87], v214 offset:12544\n"
    "v_mfma_f32_16x16x32_bf16 v[32:35], v[96:99], a[80:83], v[32:35]\n"
    "ds_read_b64_tr_b16 v[88:89], v215 offset:256\n"
    "s_add_u32 m0, s66, 98304\n"
    "v_mfma_f32_16x16x32_bf16 v[32:35], v[100:103], a[88:91], v[32:35]\n"
    "ds_read_b64_tr_b16 v[90:91], v215 offset:4352\n"
    "v_mfma_f32_16x16x32_bf16 v[36:39], v[96:99], a[84:87], v[36:39]\n"
    "ds_read_b64_tr_b16 v[92:93], v215 offset:8448\n"
    "s_add_u32 s72, s65, 819200\n"
    "v_mfma_f32_16x16x32_bf16 v[36:39], v[100:103], a[92:95], v[36:39]\n"
    "ds_read_b64_tr_b16 v[94:95], v215 offset:12544\n"
    "v_mfma_f32_16x16x32_bf16 v[44:47], v[104:107], a[84:87], v[44:47]\n"
    "ds_read_b64_tr_b16 v[96:97], v214 offset:512\n"
    "v_mfma_f32_16x16x32_bf16 v[44:47], v[108:111], a[92:95], v[44:47]\n"
    "ds_read_b64_tr_b16 v[98:99], v214 offset:4608\n"
    "buffer_load_dwordx4 v210, s[84:87], s72 offen offset:0 lds\n"
    "v_mfma_f32_16x16x32_bf16 v[40:43], v[104:107], a[80:83], v[40:43]\n"
    "ds_read_b64_tr_b16 v[100:101], v214 offset:8704\n"
    "v_mfma_f32_16x16x32_bf16 v[40:43], v[108:111], a[88:91], v[40:43]\n"
    "ds_read_b64_tr_b16 v[102:103], v214 offset:12800\n"
    "s_waitcnt lgkmcnt(8)\n"
    "v_mfma_f32_16x16x32_bf16 v[48:51], v[112:115], a[80:83], v[48:51]\n"
    "ds_read_b64_tr_b16 v[104:105], v215 offset:512\n"
    "buffer_load_dwordx4 v211, s[84:87], s72 offen offset:1024 lds\n"
    "v_mfma_f32_16x16x32_bf16 v[48:51], v[116:119], a[88:91], v[48:51]\n"
    "ds_read_b64_tr_b16 v[106:107], v215 offset:4608\n"
    "v_mfma_f32_16x16x32_bf16 v[52:55], v[112:115], a[84:87], v[52:55]\n"
    "ds_read_b64_tr_b16 v[108:109], v215 offset:8704\n"
    "v_mfma_f32_16x16x32_bf16 v[52:55], v[116:119], a[92:95], v[52:55]\n"
    "ds_read_b64_tr_b16 v[110:111], v215 offset:12800\n"
    "buffer_load_dwordx4 v212, s[84:87], s72 offen offset:2048 lds\n"
    "v_mfma_f32_16x16x32_bf16 v[60:63], v[120:123], a[84:87], v[60:63]\n"
    "v_mfma_f32_16x16x32_bf16 v[60:63], v[124:127], a[92:95], v[60:63]\n"
    "v_mfma_f32_16x16x32_bf16 v[56:59], v[120:123], a[80:83], v[56:59]\n"
    "buffer_load_dwordx4 v213, s[84:87], s72 offen offset:3072 lds\n"
    "v_mfma_f32_16x16x32_bf16 v[56:59], v[124:127], a[88:91], v[56:59]\n"
    "s_waitcnt vmcnt(20)\n"
    "v_mfma_f32_16x16x32_bf16 v[0:3], v[64:67], a[96:99], v[0:3]\n"
    "v_mfma_f32_16x16x32_bf16 v[0:3], v[68:71], a[104:107], v[0:3]\n"
    "buffer_load_dwordx4 a[144:147], v209, s[52:55], s41 offen offset:2048 sc1\n"
    "v_mfma_f32_16x16x32_bf16 v[4:7], v[64:67], a[100:103], v[4:7]\n"
    "v_mfma_f32_16x16x32_bf16 v[4:7], v[68:71], a[108:111], v[4:7]\n"
    "buffer_load_dwordx4 a[152:155], v209, s[52:55], s41 offen offset:3072 sc1\n"
    "v_mfma_f32_16x16x32_bf16 v[12:15], v[72:75], a[100:103], v[12:15]\n"
    "ds_read_b64_tr_b16 v[64:65], v214 offset:16384\n"
    "v_mfma_f32_16x16x32_bf16 v[12:15], v[76:79], a[108:111], v[12:15]\n"
    "ds_read_b64_tr_b16 v[66:67], v214 offset:20480\n"
    "v_mfma_f32_16x16x32_bf16 v[8:11], v[72:75], a[96:99], v[8:11]\n"
    "ds_read_b64_tr_b16 v[68:69], v214 offset:24576\n"
    "buffer_load_dwordx4 a[148:151], v226, s[52:55], s41 offen offset:2048 sc1\n"
    "v_mfma_f32_16x16x32_bf16 v[8:11], v[76:79], a[104:107], v[8:11]\n"
    "ds_read_b64_tr_b16 v[70:71], v214 offset:28672\n"
    "v_mfma_f32_16x16x32_bf16 v[16:19], v[80:83], a[96:99], v[16:19]\n"
    "ds_read_b64_tr_b16 v[72:73], v215 offset:16384\n"
    "buffer_load_dwordx4 a[156:159], v226, s[52:55], s41 offen offset:3072 sc1\n"
    "v_mfma_f32_16x16x32_bf16 v[16:19], v[84:87], a[104:107], v[16:19]\n"
    "ds_read_b64_tr_b16 v[74:75], v215 offset:20480\n"
    "v_mfma_f32_16x16x32_bf16 v[20:23], v[80:83], a[100:103], v[20:23]\n"
    "ds_read_b64_tr_b16 v[76:77], v215 offset:24576\n"
    "s_add_u32 m0, s66, 114688\n"
    "v_mfma_f32_16x16x32_bf16 v[20:23], v[84:87], a[108:111], v[20:23]\n"
    "ds_read_b64_tr_b16 v[78:79], v215 offset:28672\n"
    "s_waitcnt lgkmcnt(8)\n"
    "v_mfma_f32_16x16x32_bf16 v[28:31], v[88:91], a[100:103], v[28:31]\n"
    "ds_read_b64_tr_b16 v[80:81], v214 offset:16640\n"
    "v_mfma_f32_16x16x32_bf16 v[28:31], v[92:95], a[108:111], v[28:31]\n"
    "ds_read_b64_tr_b16 v[82:83], v214 offset:20736\n"
    "s_add_u32 s72, s65, 901120\n"
    "v_mfma_f32_16x16x32_bf16 v[24:27], v[88:91], a[96:99], v[24:27]\n"
    "ds_read_b64_tr_b16 v[84:85], v214 offset:24832\n"
    "v_mfma_f32_16x16x32_bf16 v[24:27], v[92:95], a[104:107], v[24:27]\n"
    "ds_read_b64_tr_b16 v[86:87], v214 offset:28928\n"
    "buffer_load_dwordx4 v210, s[84:87], s72 offen offset:0 lds\n"
    "v_mfma_f32_16x16x32_bf16 v[32:35], v[96:99], a[96:99], v[32:35]\n"
    "ds_read_b64_tr_b16 v[88:89], v215 offset:16640\n"
    "v_mfma_f32_16x16x32_bf16 v[32:35], v[100:103], a[104:107], v[32:35]\n"
    "ds_read_b64_tr_b16 v[90:91], v215 offset:20736\n"
    "v_mfma_f32_16x16x32_bf16 v[36:39], v[96:99], a[100:103], v[36:39]\n"
    "ds_read_b64_tr_b16 v[92:93], v215 offset:24832\n"
    "buffer_load_dwordx4 v211, s[84:87], s72 offen offset:1024 lds\n"
    "v_mfma_f32_16x16x32_bf16 v[36:39], v[100:103], a[108:111], v[36:39]\n"
    "ds_read_b64_tr_b16 v[94:95], v215 offset:28928\n"
    "v_mfma_f32_16x16x32_bf16 v[44:47], v[104:107], a[100:103], v[44:47]\n"
    "ds_read_b64_tr_b16 v[96:97], v214 offset:16896\n"
    "buffer_load_dwordx4 v212, s[84:87], s72 offen offset:2048 lds\n"
    "v_mfma_f32_16x16x32_bf16 v[44:47], v[108:111], a[108:111], v[44:47]\n"
    "ds_read_b64_tr_b16 v[98:99], v214 offset:20992\n"
    "v_mfma_f32_16x16x32_bf16 v[40:43], v[104:107], a[96:99], v[40:43]\n"
    "ds_read_b64_tr_b16 v[100:101], v214 offset:25088\n"
    "buffer_load_dwordx4 v213, s[84:87], s72 offen offset:3072 lds\n"
    "v_mfma_f32_16x16x32_bf16 v[40:43], v[108:111], a[104:107], v[40:43]\n"
    "ds_read_b64_tr_b16 v[102:103], v214 offset:29184\n"
    "s_waitcnt vmcnt(16)\n"
    "s_barrier\n"
    "s_waitcnt lgkmcnt(7)\n"
    "v_mfma_f32_16x16x32_bf16 v[0:3], v[64:67], a[112:115], v[0:3]\n"
    "ds_read_b64_tr_b16 v[104:105], v215 offset:16896\n"
    "v_mfma_f32_16x16x32_bf16 v[0:3], v[68:71], a[120:123], v[0:3]\n"
    "ds_read_b64_tr_b16 v[106:107], v215 offset:20992\n"
    "s_add_u32 s41, s40, 20480\n"
    "v_mfma_f32_16x16x32_bf16 v[4:7], v[64:67], a[116:119], v[4:7]\n"
    "ds_read_b64_tr_b16 v[108:109], v215 offset:25088\n"
    "v_mfma_f32_16x16x32_bf16 v[4:7], v[68:71], a[124:127], v[4:7]\n"
    "ds_read_b64_tr_b16 v[110:111], v215 offset:29184\n"
    "buffer_load_dwordx4 a[160:163], v209, s[52:55], s41 offen offset:0 sc1\n"
    "v_mfma_f32_16x16x32_bf16 v[12:15], v[72:75], a[116:119], v[12:15]\n"
    "ds_read_b64_tr_b16 v[64:65], v214 offset:32768\n"
    "v_mfma_f32_16x16x32_bf16 v[12:15], v[76:79], a[124:127], v[12:15]\n"
    "ds_read_b64_tr_b16 v[66:67], v214 offset:36864\n"
    "buffer_load_dwordx4 a[168:171], v209, s[52:55], s41 offen offset:1024 sc1\n"
    "v_mfma_f32_16x16x32_bf16 v[8:11], v[72:75], a[112:115], v[8:11]\n"
    "ds_read_b64_tr_b16 v[68:69], v214 offset:40960\n"
    "v_mfma_f32_16x16x32_bf16 v[8:11], v[76:79], a[120:123], v[8:11]\n"
    "ds_read_b64_tr_b16 v[70:71], v214 offset:45056\n"
    "buffer_load_dwordx4 a[164:167], v226, s[52:55], s41 offen offset:0 sc1\n"
    "v_mfma_f32_16x16x32_bf16 v[16:19], v[80:83], a[112:115], v[16:19]\n"
    "ds_read_b64_tr_b16 v[72:73], v215 offset:32768\n"
    "v_mfma_f32_16x16x32_bf16 v[16:19], v[84:87], a[120:123], v[16:19]\n"
    "ds_read_b64_tr_b16 v[74:75], v215 offset:36864\n"
    "buffer_load_dwordx4 a[172:175], v226, s[52:55], s41 offen offset:1024 sc1\n"
    "v_mfma_f32_16x16x32_bf16 v[20:23], v[80:83], a[116:119], v[20:23]\n"
    "ds_read_b64_tr_b16 v[76:77], v215 offset:40960\n"
    "v_mfma_f32_16x16x32_bf16 v[20:23], v[84:87], a[124:127], v[20:23]\n"
    "ds_read_b64_tr_b16 v[78:79], v215 offset:45056\n"
    "s_waitcnt lgkmcnt(8)\n"
    "v_mfma_f32_16x16x32_bf16 v[28:31], v[88:91], a[116:119], v[28:31]\n"
    "ds_read_b64_tr_b16 v[80:81], v214 offset:33024\n"
    "s_add_u32 m0, s66, 131072\n"
    "v_mfma_f32_16x16x32_bf16 v[28:31], v[92:95], a[124:127], v[28:31]\n"
    "ds_read_b64_tr_b16 v[82:83], v214 offset:37120\n"
    "v_mfma_f32_16x16x32_bf16 v[24:27], v[88:91], a[112:115], v[24:27]\n"
    "ds_read_b64_tr_b16 v[84:85], v214 offset:41216\n"
    "s_add_u32 s72, s65, 983040\n"
    "v_mfma_f32_16x16x32_bf16 v[24:27], v[92:95], a[120:123], v[24:27]\n"
    "ds_read_b64_tr_b16 v[86:87], v214 offset:45312\n"
    "v_mfma_f32_16x16x32_bf16 v[32:35], v[96:99], a[112:115], v[32:35]\n"
    "ds_read_b64_tr_b16 v[88:89], v215 offset:33024\n"
    "buffer_load_dwordx4 v210, s[84:87], s72 offen offset:0 lds\n"
    "v_mfma_f32_16x16x32_bf16 v[32:35], v[100:103], a[120:123], v[32:35]\n"
    "ds_read_b64_tr_b16 v[90:91], v215 offset:37120\n"
    "v_mfma_f32_16x16x32_bf16 v[36:39], v[96:99], a[116:119], v[36:39]\n"
    "ds_read_b64_tr_b16 v[92:93], v215 offset:41216\n"
    "buffer_load_dwordx4 v211, s[84:87], s72 offen offset:1024 lds\n"
    "v_mfma_f32_16x16x32_bf16 v[36:39], v[100:103], a[124:127], v[36:39]\n"
    "ds_read_b64_tr_b16 v[94:95], v215 offset:45312\n"
    "v_mfma_f32_16x16x32_bf16 v[44:47], v[104:107], a[116:119], v[44:47]\n"
    "ds_read_b64_tr_b16 v[96:97], v214 offset:33280\n"
    "buffer_load_dwordx4 v212, s[84:87], s72 offen offset:2048 lds\n"
    "v_mfma_f32_16x16x32_bf16 v[44:47], v[108:111], a[124:127], v[44:47]\n"
    "ds_read_b64_tr_b16 v[98:99], v214 offset:37376\n"
    "v_mfma_f32_16x16x32_bf16 v[40:43], v[104:107], a[112:115], v[40:43]\n"
    "ds_read_b64_tr_b16 v[100:101], v214 offset:41472\n"
    "buffer_load_dwordx4 v213, s[84:87], s72 offen offset:3072 lds\n"
    "v_mfma_f32_16x16x32_bf16 v[40:43], v[108:111], a[120:123], v[40:43]\n"
    "ds_read_b64_tr_b16 v[102:103], v214 offset:45568\n"
    "s_waitcnt vmcnt(20)\n"
    "s_waitcnt lgkmcnt(7)\n"
    "v_mfma_f32_16x16x32_bf16 v[0:3], v[64:67], a[128:131], v[0:3]\n"
    "ds_read_b64_tr_b16 v[104:105], v215 offset:33280\n"
    "v_mfma_f32_16x16x32_bf16 v[0:3], v[68:71], a[136:139], v[0:3]\n"
    "ds_read_b64_tr_b16 v[106:107], v215 offset:37376\n"
    "buffer_load_dwordx4 a[176:179], v209, s[52:55], s41 offen offset:2048 sc1\n"
    "v_mfma_f32_16x16x32_bf16 v[4:7], v[64:67], a[132:135], v[4:7]\n"
    "ds_read_b64_tr_b16 v[108:109], v215 offset:41472\n"
    "v_mfma_f32_16x16x32_bf16 v[4:7], v[68:71], a[140:143], v[4:7]\n"
    "ds_read_b64_tr_b16 v[110:111], v215 offset:45568\n"
    "buffer_load_dwordx4 a[184:187], v209, s[52:55], s41 offen offset:3072 sc1\n"
    "v_mfma_f32_16x16x32_bf16 v[12:15], v[72:75], a[132:135], v[12:15]\n"
    "ds_read_b64_tr_b16 v[64:65], v214 offset:49152\n"
    "v_mfma_f32_16x16x32_bf16 v[12:15], v[76:79], a[140:143], v[12:15]\n"
    "ds_read_b64_tr_b16 v[66:67], v214 offset:53248\n"
    "v_mfma_f32_16x16x32_bf16 v[8:11], v[72:75], a[128:131], v[8:11]\n"
    "ds_read_b64_tr_b16 v[68:69], v214 offset:57344\n"
    "buffer_load_dwordx4 a[180:183], v226, s[52:55], s41 offen offset:2048 sc1\n"
    "v_mfma_f32_16x16x32_bf16 v[8:11], v[76:79], a[136:139], v[8:11]\n"
    "ds_read_b64_tr_b16 v[70:71], v214 offset:61440\n"
    "v_mfma_f32_16x16x32_bf16 v[16:19], v[80:83], a[128:131], v[16:19]\n"
    "ds_read_b64_tr_b16 v[72:73], v215 offset:49152\n"
    "buffer_load_dwordx4 a[188:191], v226, s[52:55], s41 offen offset:3072 sc1\n"
    "v_mfma_f32_16x16x32_bf16 v[16:19], v[84:87], a[136:139], v[16:19]\n"
    "ds_read_b64_tr_b16 v[74:75], v215 offset:53248\n"
    "v_mfma_f32_16x16x32_bf16 v[20:23], v[80:83], a[132:135], v[20:23]\n"
    "ds_read_b64_tr_b16 v[76:77], v215 offset:57344\n"
    "s_add_u32 m0, s66, 147456\n"
    "v_mfma_f32_16x16x32_bf16 v[20:23], v[84:87], a[140:143], v[20:23]\n"
    "ds_read_b64_tr_b16 v[78:79], v215 offset:61440\n"
    "s_waitcnt lgkmcnt(8)\n"
    "v_mfma_f32_16x16x32_bf16 v[28:31], v[88:91], a[132:135], v[28:31]\n"
    "ds_read_b64_tr_b16 v[80:81], v214 offset:49408\n"
    "v_mfma_f32_16x16x32_bf16 v[28:31], v[92:95], a[140:143], v[28:31]\n"
    "ds_read_b64_tr_b16 v[82:83], v214 offset:53504\n"
    "s_add_u32 s72, s65, 1064960\n"
    "v_mfma_f32_16x16x32_bf16 v[24:27], v[88:91], a[128:131], v[24:27]\n"
    "ds_read_b64_tr_b16 v[84:85], v214 offset:57600\n"
    "v_mfma_f32_16x16x32_bf16 v[24:27], v[92:95], a[136:139], v[24:27]\n"
    "ds_read_b64_tr_b16 v[86:87], v214 offset:61696\n"
    "buffer_load_dwordx4 v210, s[84:87], s72 offen offset:0 lds\n"
    "v_mfma_f32_16x16x32_bf16 v[32:35], v[96:99], a[128:131], v[32:35]\n"
    "ds_read_b64_tr_b16 v[88:89], v215 offset:49408\n"
    "v_mfma_f32_16x16x32_bf16 v[32:35], v[100:103], a[136:139], v[32:35]\n"
    "ds_read_b64_tr_b16 v[90:91], v215 offset:53504\n"
    "v_mfma_f32_16x16x32_bf16 v[36:39], v[96:99], a[132:135], v[36:39]\n"
    "ds_read_b64_tr_b16 v[92:93], v215 offset:57600\n"
    "buffer_load_dwordx4 v211, s[84:87], s72 offen offset:1024 lds\n"
    "v_mfma_f32_16x16x32_bf16 v[36:39], v[100:103], a[140:143], v[36:39]\n"
    "ds_read_b64_tr_b16 v[94:95], v215 offset:61696\n"
    "v_mfma_f32_16x16x32_bf16 v[44:47], v[104:107], a[132:135], v[44:47]\n"
    "ds_read_b64_tr_b16 v[96:97], v214 offset:49664\n"
    "buffer_load_dwordx4 v212, s[84:87], s72 offen offset:2048 lds\n"
    "v_mfma_f32_16x16x32_bf16 v[44:47], v[108:111], a[140:143], v[44:47]\n"
    "ds_read_b64_tr_b16 v[98:99], v214 offset:53760\n"
    "v_mfma_f32_16x16x32_bf16 v[40:43], v[104:107], a[128:131], v[40:43]\n"
    "ds_read_b64_tr_b16 v[100:101], v214 offset:57856\n"
    "buffer_load_dwordx4 v213, s[84:87], s72 offen offset:3072 lds\n"
    "v_mfma_f32_16x16x32_bf16 v[40:43], v[108:111], a[136:139], v[40:43]\n"
    "ds_read_b64_tr_b16 v[102:103], v214 offset:61952\n"
    "s_waitcnt vmcnt(16)\n"
    "s_barrier\n"
    "s_waitcnt lgkmcnt(7)\n"
    "v_mfma_f32_16x16x32_bf16 v[0:3], v[64:67], a[144:147], v[0:3]\n"
    "ds_read_b64_tr_b16 v[104:105], v215 offset:49664\n"
    "v_mfma_f32_16x16x32_bf16 v[0:3], v[68:71], a[152:155], v[0:3]\n"
    "ds_read_b64_tr_b16 v[106:107], v215 offset:53760\n"
    "s_add_u32 s41, s40, 24576\n"
    "v_mfma_f32_16x16x32_bf16 v[4:7], v[64:67], a[148:151], v[4:7]\n"
    "ds_read_b64_tr_b16 v[108:109], v215 offset:57856\n"
    "v_mfma_f32_16x16x32_bf16 v[4:7], v[68:71], a[156:159], v[4:7]\n"
    "ds_read_b64_tr_b16 v[110:111], v215 offset:61952\n"
    "buffer_load_dwordx4 a[192:195], v209, s[52:55], s41 offen offset:0 sc1\n"
    "v_mfma_f32_16x16x32_bf16 v[12:15], v[72:75], a[148:151], v[12:15]\n"
    "ds_read_b64_tr_b16 v[64:65], v216 offset:32768\n"
    "v_mfma_f32_16x16x32_bf16 v[12:15], v[76:79], a[156:159], v[12:15]\n"
    "ds_read_b64_tr_b16 v[66:67], v216 offset:36864\n"
    "buffer_load_dwordx4 a[200:203], v209, s[52:55], s41 offen offset:1024 sc1\n"
    "v_mfma_f32_16x16x32_bf16 v[8:11], v[72:75], a[144:147], v[8:11]\n"
    "ds_read_b64_tr_b16 v[68:69], v216 offset:40960\n"
    "v_mfma_f32_16x16x32_bf16 v[8:11], v[76:79], a[152:155], v[8:11]\n"
    "ds_read_b64_tr_b16 v[70:71], v216 offset:45056\n"
    "buffer_load_dwordx4 a[196:199], v226, s[52:55], s41 offen offset:0 sc1\n"
    "v_mfma_f32_16x16x32_bf16 v[16:19], v[80:83], a[144:147], v[16:19]\n"
    "ds_read_b64_tr_b16 v[72:73], v217 offset:32768\n"
    "v_mfma_f32_16x16x32_bf16 v[16:19], v[84:87], a[152:155], v[16:19]\n"
    "ds_read_b64_tr_b16 v[74:75], v217 offset:36864\n"
    "buffer_load_dwordx4 a[204:207], v226, s[52:55], s41 offen offset:1024 sc1\n"
    "v_mfma_f32_16x16x32_bf16 v[20:23], v[80:83], a[148:151], v[20:23]\n"
    "ds_read_b64_tr_b16 v[76:77], v217 offset:40960\n"
    "v_mfma_f32_16x16x32_bf16 v[20:23], v[84:87], a[156:159], v[20:23]\n"
    "ds_read_b64_tr_b16 v[78:79], v217 offset:45056\n"
    "s_waitcnt lgkmcnt(8)\n"
    "v_mfma_f32_16x16x32_bf16 v[28:31], v[88:91], a[148:151], v[28:31]\n"
    "ds_read_b64_tr_b16 v[80:81], v216 offset:33024\n"
    "s_add_u32 m0, s66, 65536\n"
    "v_mfma_f32_16x16x32_bf16 v[28:31], v[92:95], a[156:159], v[28:31]\n"
    "ds_read_b64_tr_b16 v[82:83], v216 offset:37120\n"
    "v_mfma_f32_16x16x32_bf16 v[24:27], v[88:91], a[144:147], v[24:27]\n"
    "ds_read_b64_tr_b16 v[84:85], v216 offset:41216\n"
    "s_add_u32 s72, s65, 1146880\n"
    "v_mfma_f32_16x16x32_bf16 v[24:27], v[92:95], a[152:155], v[24:27]\n"
    "ds_read_b64_tr_b16 v[86:87], v216 offset:45312\n"
    "v_mfma_f32_16x16x32_bf16 v[32:35], v[96:99], a[144:147], v[32:35]\n"
    "ds_read_b64_tr_b16 v[88:89], v217 offset:33024\n"
    "buffer_load_dwordx4 v210, s[84:87], s72 offen offset:0 lds\n"
    "v_mfma_f32_16x16x32_bf16 v[32:35], v[100:103], a[152:155], v[32:35]\n"
    "ds_read_b64_tr_b16 v[90:91], v217 offset:37120\n"
    "v_mfma_f32_16x16x32_bf16 v[36:39], v[96:99], a[148:151], v[36:39]\n"
    "ds_read_b64_tr_b16 v[92:93], v217 offset:41216\n"
    "buffer_load_dwordx4 v211, s[84:87], s72 offen offset:1024 lds\n"
    "v_mfma_f32_16x16x32_bf16 v[36:39], v[100:103], a[156:159], v[36:39]\n"
    "ds_read_b64_tr_b16 v[94:95], v217 offset:45312\n"
    "v_mfma_f32_16x16x32_bf16 v[44:47], v[104:107], a[148:151], v[44:47]\n"
    "ds_read_b64_tr_b16 v[96:97], v216 offset:33280\n"
    "buffer_load_dwordx4 v212, s[84:87], s72 offen offset:2048 lds\n"
    "v_mfma_f32_16x16x32_bf16 v[44:47], v[108:111], a[156:159], v[44:47]\n"
    "ds_read_b64_tr_b16 v[98:99], v216 offset:37376\n"
    "v_mfma_f32_16x16x32_bf16 v[40:43], v[104:107], a[144:147], v[40:43]\n"
    "ds_read_b64_tr_b16 v[100:101], v216 offset:41472\n"
    "buffer_load_dwordx4 v213, s[84:87], s72 offen offset:3072 lds\n"
    "v_mfma_f32_16x16x32_bf16 v[40:43], v[108:111], a[152:155], v[40:43]\n"
    "ds_read_b64_tr_b16 v[102:103], v216 offset:45568\n"
    "s_waitcnt vmcnt(20)\n"
    "s_waitcnt lgkmcnt(7)\n"
    "v_mfma_f32_16x16x32_bf16 v[0:3], v[64:67], a[160:163], v[0:3]\n"
    "ds_read_b64_tr_b16 v[104:105], v217 offset:33280\n"
    "v_mfma_f32_16x16x32_bf16 v[0:3], v[68:71], a[168:171], v[0:3]\n"
    "ds_read_b64_tr_b16 v[106:107], v217 offset:37376\n"
    "buffer_load_dwordx4 a[208:211], v209, s[52:55], s41 offen offset:2048 sc1\n"
    "v_mfma_f32_16x16x32_bf16 v[4:7], v[64:67], a[164:167], v[4:7]\n"
    "ds_read_b64_tr_b16 v[108:109], v217 offset:41472\n"
    "v_mfma_f32_16x16x32_bf16 v[4:7], v[68:71], a[172:175], v[4:7]\n"
    "ds_read_b64_tr_b16 v[110:111], v217 offset:45568\n"
    "buffer_load_dwordx4 a[216:219], v209, s[52:55], s41 offen offset:3072 sc1\n"
    "v_mfma_f32_16x16x32_bf16 v[12:15], v[72:75], a[164:167], v[12:15]\n"
    "ds_read_b64_tr_b16 v[64:65], v216 offset:49152\n"
    "v_mfma_f32_16x16x32_bf16 v[12:15], v[76:79], a[172:175], v[12:15]\n"
    "ds_read_b64_tr_b16 v[66:67], v216 offset:53248\n"
    "v_mfma_f32_16x16x32_bf16 v[8:11], v[72:75], a[160:163], v[8:11]\n"
    "ds_read_b64_tr_b16 v[68:69], v216 offset:57344\n"
    "buffer_load_dwordx4 a[212:215], v226, s[52:55], s41 offen offset:2048 sc1\n"
    "v_mfma_f32_16x16x32_bf16 v[8:11], v[76:79], a[168:171], v[8:11]\n"
    "ds_read_b64_tr_b16 v[70:71], v216 offset:61440\n"
    "v_mfma_f32_16x16x32_bf16 v[16:19], v[80:83], a[160:163], v[16:19]\n"
    "ds_read_b64_tr_b16 v[72:73], v217 offset:49152\n"
    "buffer_load_dwordx4 a[220:223], v226, s[52:55], s41 offen offset:3072 sc1\n"
    "v_mfma_f32_16x16x32_bf16 v[16:19], v[84:87], a[168:171], v[16:19]\n"
    "ds_read_b64_tr_b16 v[74:75], v217 offset:53248\n"
    "v_mfma_f32_16x16x32_bf16 v[20:23], v[80:83], a[164:167], v[20:23]\n"
    "ds_read_b64_tr_b16 v[76:77], v217 offset:57344\n"
    "s_add_u32 m0, s66, 81920\n"
    "v_mfma_f32_16x16x32_bf16 v[20:23], v[84:87], a[172:175], v[20:23]\n"
    "ds_read_b64_tr_b16 v[78:79], v217 offset:61440\n"
    "s_waitcnt lgkmcnt(8)\n"
    "v_mfma_f32_16x16x32_bf16 v[28:31], v[88:91], a[164:167], v[28:31]\n"
    "ds_read_b64_tr_b16 v[80:81], v216 offset:49408\n"
    "v_mfma_f32_16x16x32_bf16 v[28:31], v[92:95], a[172:175], v[28:31]\n"
    "ds_read_b64_tr_b16 v[82:83], v216 offset:53504\n"
    "s_add_u32 s72, s65, 1228800\n"
    "v_mfma_f32_16x16x32_bf16 v[24:27], v[88:91], a[160:163], v[24:27]\n"
    "ds_read_b64_tr_b16 v[84:85], v216 offset:57600\n"
    "v_mfma_f32_16x16x32_bf16 v[24:27], v[92:95], a[168:171], v[24:27]\n"
    "ds_read_b64_tr_b16 v[86:87], v216 offset:61696\n"
    "buffer_load_dwordx4 v210, s[84:87], s72 offen offset:0 lds\n"
    "v_mfma_f32_16x16x32_bf16 v[32:35], v[96:99], a[160:163], v[32:35]\n"
    "ds_read_b64_tr_b16 v[88:89], v217 offset:49408\n"
    "v_mfma_f32_16x16x32_bf16 v[32:35], v[100:103], a[168:171], v[32:35]\n"
    "ds_read_b64_tr_b16 v[90:91], v217 offset:53504\n"
    "v_mfma_f32_16x16x32_bf16 v[36:39], v[96:99], a[164:167], v[36:39]\n"
    "ds_read_b64_tr_b16 v[92:93], v217 offset:57600\n"
    "buffer_load_dwordx4 v211, s[84:87], s72 offen offset:1024 lds\n"
    "v_mfma_f32_16x16x32_bf16 v[36:39], v[100:103], a[172:175], v[36:39]\n"
    "ds_read_b64_tr_b16 v[94:95], v217 offset:61696\n"
    "v_mfma_f32_16x16x32_bf16 v[44:47], v[104:107], a[164:167], v[44:47]\n"
    "ds_read_b64_tr_b16 v[96:97], v216 offset:49664\n"
    "buffer_load_dwordx4 v212, s[84:87], s72 offen offset:2048 lds\n"
    "v_mfma_f32_16x16x32_bf16 v[44:47], v[108:111], a[172:175], v[44:47]\n"
    "ds_read_b64_tr_b16 v[98:99], v216 offset:53760\n"
    "v_mfma_f32_16x16x32_bf16 v[40:43], v[104:107], a[160:163], v[40:43]\n"
    "ds_read_b64_tr_b16 v[100:101], v216 offset:57856\n"
    "buffer_load_dwordx4 v213, s[84:87], s72 offen offset:3072 lds\n"
    "v_mfma_f32_16x16x32_bf16 v[40:43], v[108:111], a[168:171], v[40:43]\n"
    "ds_read_b64_tr_b16 v[102:103], v216 offset:61952\n"
    "s_waitcnt vmcnt(16)\n"
    "s_barrier\n"
    "s_waitcnt lgkmcnt(7)\n"
    "v_mfma_f32_16x16x32_bf16 v[0:3], v[64:67], a[176:179], v[0:3]\n"
    "ds_read_b64_tr_b16 v[104:105], v217 offset:49664\n"
    "v_mfma_f32_16x16x32_bf16 v[0:3], v[68:71], a[184:187], v[0:3]\n"
    "ds_read_b64_tr_b16 v[106:107], v217 offset:53760\n"
    "v_mfma_f32_16x16x32_bf16 v[4:7], v[64:67], a[180:183], v[4:7]\n"
    "ds_read_b64_tr_b16 v[108:109], v217 offset:57856\n"
    "s_add_u32 s41, s40, 28672\n"
    "v_mfma_f32_16x16x32_bf16 v[4:7], v[68:71], a[188:191], v[4:7]\n"
    "ds_read_b64_tr_b16 v[110:111], v217 offset:61952\n"
    "v_mfma_f32_16x16x32_bf16 v[12:15], v[72:75], a[180:183], v[12:15]\n"
    "ds_read_b64_tr_b16 v[64:65], v218 offset:0\n"
    "v_mfma_f32_16x16x32_bf16 v[12:15], v[76:79], a[188:191], v[12:15]\n"
    "ds_read_b64_tr_b16 v[66:67], v218 offset:4096\n"
    "v_mfma_f32_16x16x32_bf16 v[8:11], v[72:75], a[176:179], v[8:11]\n"
    "ds_read_b64_tr_b16 v[68:69], v218 offset:8192\n"
    "v_mfma_f32_16x16x32_bf16 v[8:11], v[76:79], a[184:187], v[8:11]\n"
    "ds_read_b64_tr_b16 v[70:71], v218 offset:12288\n"
    "buffer_load_dwordx4 a[224:227], v209, s[52:55], s41 offen offset:0 sc1\n"
    "v_mfma_f32_16x16x32_bf16 v[16:19], v[80:83], a[176:179], v[16:19]\n"
    "ds_read_b64_tr_b16 v[72:73], v219 offset:0\n"
    "v_mfma_f32_16x16x32_bf16 v[16:19], v[84:87], a[184:187], v[16:19]\n"
    "ds_read_b64_tr_b16 v[74:75], v219 offset:4096\n"
    "v_mfma_f32_16x16x32_bf16 v[20:23], v[80:83], a[180:183], v[20:23]\n"
    "ds_read_b64_tr_b16 v[76:77], v219 offset:8192\n"
    "v_mfma_f32_16x16x32_bf16 v[20:23], v[84:87], a[188:191], v[20:23]\n"
    "ds_read_b64_tr_b16 v[78:79], v219 offset:12288\n"
    "s_waitcnt lgkmcnt(9)\n"
    "v_mfma_f32_16x16x32_bf16 v[28:31], v[88:91], a[180:183], v[28:31]\n"
    "ds_read_b64_tr_b16 v[80:81], v218 offset:256\n"
    "buffer_load_dwordx4 a[232:235], v209, s[52:55], s41 offen offset:1024 sc1\n"
    "v_mfma_f32_16x16x32_bf16 v[28:31], v[92:95], a[188:191], v[28:31]\n"
    "ds_read_b64_tr_b16 v[82:83], v218 offset:4352\n"
    "v_mfma_f32_16x16x32_bf16 v[24:27], v[88:91], a[176:179], v[24:27]\n"
    "ds_read_b64_tr_b16 v[84:85], v218 offset:8448\n"
    "v_mfma_f32_16x16x32_bf16 v[24:27], v[92:95], a[184:187], v[24:27]\n"
    "ds_read_b64_tr_b16 v[86:87], v218 offset:12544\n"
    "v_mfma_f32_16x16x32_bf16 v[32:35], v[96:99], a[176:179], v[32:35]\n"
    "ds_read_b64_tr_b16 v[88:89], v219 offset:256\n"
    "buffer_load_dwordx4 a[228:231], v226, s[52:55], s41 offen offset:0 sc1\n"
    "v_mfma_f32_16x16x32_bf16 v[32:35], v[100:103], a[184:187], v[32:35]\n"
    "ds_read_b64_tr_b16 v[90:91], v219 offset:4352\n"
    "v_mfma_f32_16x16x32_bf16 v[36:39], v[96:99], a[180:183], v[36:39]\n"
    "ds_read_b64_tr_b16 v[92:93], v219 offset:8448\n"
    "v_mfma_f32_16x16x32_bf16 v[36:39], v[100:103], a[188:191], v[36:39]\n"
    "ds_read_b64_tr_b16 v[94:95], v219 offset:12544\n"
    "v_mfma_f32_16x16x32_bf16 v[44:47], v[104:107], a[180:183], v[44:47]\n"
    "ds_read_b64_tr_b16 v[96:97], v218 offset:512\n"
    "s_waitcnt lgkmcnt(9)\n"
    "v_mfma_f32_16x16x32_bf16 v[44:47], v[108:111], a[188:191], v[44:47]\n"
    "ds_read_b64_tr_b16 v[98:99], v218 offset:4608\n"
    "buffer_load_dwordx4 a[236:239], v226, s[52:55], s41 offen offset:1024 sc1\n"
    "v_mfma_f32_16x16x32_bf16 v[40:43], v[104:107], a[176:179], v[40:43]\n"
    "ds_read_b64_tr_b16 v[100:101], v218 offset:8704\n"
    "v_mfma_f32_16x16x32_bf16 v[40:43], v[108:111], a[184:187], v[40:43]\n"
    "ds_read_b64_tr_b16 v[102:103], v218 offset:12800\n"
    "s_waitcnt vmcnt(16)\n"
    "v_mfma_f32_16x16x32_bf16 v[0:3], v[64:67], a[192:195], v[0:3]\n"
    "ds_read_b64_tr_b16 v[104:105], v219 offset:512\n"
    "v_mfma_f32_16x16x32_bf16 v[0:3], v[68:71], a[200:203], v[0:3]\n"
    "ds_read_b64_tr_b16 v[106:107], v219 offset:4608\n"
    "v_mfma_f32_16x16x32_bf16 v[4:7], v[64:67], a[196:199], v[4:7]\n"
    "ds_read_b64_tr_b16 v[108:109], v219 offset:8704\n"
    "v_mfma_f32_16x16x32_bf16 v[4:7], v[68:71], a[204:207], v[4:7]\n"
    "ds_read_b64_tr_b16 v[110:111], v219 offset:12800\n"
    "buffer_load_dwordx4 a[240:243], v209, s[52:55], s41 offen offset:2048 sc1\n"
    "v_mfma_f32_16x16x32_bf16 v[12:15], v[72:75], a[196:199], v[12:15]\n"
    "ds_read_b64_tr_b16 v[64:65], v218 offset:16384\n"
    "v_mfma_f32_16x16x32_bf16 v[12:15], v[76:79], a[204:207], v[12:15]\n"
    "ds_read_b64_tr_b16 v[66:67], v218 offset:20480\n"
    "v_mfma_f32_16x16x32_bf16 v[8:11], v[72:75], a[192:195], v[8:11]\n"
    "ds_read_b64_tr_b16 v[68:69], v218 offset:24576\n"
    "v_mfma_f32_16x16x32_bf16 v[8:11], v[76:79], a[200:203], v[8:11]\n"
    "ds_read_b64_tr_b16 v[70:71], v218 offset:28672\n"
    "s_waitcnt lgkmcnt(9)\n"
    "v_mfma_f32_16x16x32_bf16 v[16:19], v[80:83], a[192:195], v[16:19]\n"
    "ds_read_b64_tr_b16 v[72:73], v219 offset:16384\n"
    "v_mfma_f32_16x16x32_bf16 v[16:19], v[84:87], a[200:203], v[16:19]\n"
    "ds_read_b64_tr_b16 v[74:75], v219 offset:20480\n"
    "buffer_load_dwordx4 a[248:251], v209, s[52:55], s41 offen offset:3072 sc1\n"
    "v_mfma_f32_16x16x32_bf16 v[20:23], v[80:83], a[196:199], v[20:23]\n"
    "ds_read_b64_tr_b16 v[76:77], v219 offset:24576\n"
    "v_mfma_f32_16x16x32_bf16 v[20:23], v[84:87], a[204:207], v[20:23]\n"
    "ds_read_b64_tr_b16 v[78:79], v219 offset:28672\n"
    "v_mfma_f32_16x16x32_bf16 v[28:31], v[88:91], a[196:199], v[28:31]\n"
    "ds_read_b64_tr_b16 v[80:81], v218 offset:16640\n"
    "v_mfma_f32_16x16x32_bf16 v[28:31], v[92:95], a[204:207], v[28:31]\n"
    "ds_read_b64_tr_b16 v[82:83], v218 offset:20736\n"
    "v_mfma_f32_16x16x32_bf16 v[24:27], v[88:91], a[192:195], v[24:27]\n"
    "ds_read_b64_tr_b16 v[84:85], v218 offset:24832\n"
    "v_mfma_f32_16x16x32_bf16 v[24:27], v[92:95], a[200:203], v[24:27]\n"
    "ds_read_b64_tr_b16 v[86:87], v218 offset:28928\n"
    "buffer_load_dwordx4 a[244:247], v226, s[52:55], s41 offen offset:2048 sc1\n"
    "v_mfma_f32_16x16x32_bf16 v[32:35], v[96:99], a[192:195], v[32:35]\n"
    "ds_read_b64_tr_b16 v[88:89], v219 offset:16640\n"
    "s_waitcnt lgkmcnt(9)\n"
    "v_mfma_f32_16x16x32_bf16 v[32:35], v[100:103], a[200:203], v[32:35]\n"
    "ds_read_b64_tr_b16 v[90:91], v219 offset:20736\n"
    "v_mfma_f32_16x16x32_bf16 v[36:39], v[96:99], a[196:199], v[36:39]\n"
    "ds_read_b64_tr_b16 v[92:93], v219 offset:24832\n"
    "v_mfma_f32_16x16x32_bf16 v[36:39], v[100:103], a[204:207], v[36:39]\n"
    "ds_read_b64_tr_b16 v[94:95], v219 offset:28928\n"
    "v_mfma_f32_16x16x32_bf16 v[44:47], v[104:107], a[196:199], v[44:47]\n"
    "ds_read_b64_tr_b16 v[96:97], v218 offset:16896\n"
    "v_mfma_f32_16x16x32_bf16 v[44:47], v[108:111], a[204:207], v[44:47]\n"
    "ds_read_b64_tr_b16 v[98:99], v218 offset:20992\n"
    "buffer_load_dwordx4 a[252:255], v226, s[52:55], s41 offen offset:3072 sc1\n"
    "v_mfma_f32_16x16x32_bf16 v[40:43], v[104:107], a[192:195], v[40:43]\n"
    "ds_read_b64_tr_b16 v[100:101], v218 offset:25088\n"
    "v_mfma_f32_16x16x32_bf16 v[40:43], v[108:111], a[200:203], v[40:43]\n"
    "ds_read_b64_tr_b16 v[102:103], v218 offset:29184\n"
    "s_waitcnt vmcnt(8)\n"
    "s_barrier\n"
    "v_mfma_f32_16x16x32_bf16 v[0:3], v[64:67], a[208:211], v[0:3]\n"
    "ds_read_b64_tr_b16 v[104:105], v219 offset:16896\n"
    "v_mfma_f32_16x16x32_bf16 v[0:3], v[68:71], a[216:219], v[0:3]\n"
    "ds_read_b64_tr_b16 v[106:107], v219 offset:20992\n"
    "v_mfma_f32_16x16x32_bf16 v[4:7], v[64:67], a[212:215], v[4:7]\n"
    "ds_read_b64_tr_b16 v[108:109], v219 offset:25088\n"
    "v_mfma_f32_16x16x32_bf16 v[4:7], v[68:71], a[220:223], v[4:7]\n"
    "ds_read_b64_tr_b16 v[110:111], v219 offset:29184\n"
    "s_waitcnt lgkmcnt(8)\n"
    "v_mfma_f32_16x16x32_bf16 v[12:15], v[72:75], a[212:215], v[12:15]\n"
    "ds_read_b64_tr_b16 v[64:65], v216 offset:0\n"
    "v_mfma_f32_16x16x32_bf16 v[12:15], v[76:79], a[220:223], v[12:15]\n"
    "ds_read_b64_tr_b16 v[66:67], v216 offset:4096\n"
    "v_mfma_f32_16x16x32_bf16 v[8:11], v[72:75], a[208:211], v[8:11]\n"
    "ds_read_b64_tr_b16 v[68:69], v216 offset:8192\n"
    "v_mfma_f32_16x16x32_bf16 v[8:11], v[76:79], a[216:219], v[8:11]\n"
    "ds_read_b64_tr_b16 v[70:71], v216 offset:12288\n"
    "v_mfma_f32_16x16x32_bf16 v[16:19], v[80:83], a[208:211], v[16:19]\n"
    "ds_read_b64_tr_b16 v[72:73], v217 offset:0\n"
    "v_mfma_f32_16x16x32_bf16 v[16:19], v[84:87], a[216:219], v[16:19]\n"
    "ds_read_b64_tr_b16 v[74:75], v217 offset:4096\n"
    "v_mfma_f32_16x16x32_bf16 v[20:23], v[80:83], a[212:215], v[20:23]\n"
    "ds_read_b64_tr_b16 v[76:77], v217 offset:8192\n"
    "v_mfma_f32_16x16x32_bf16 v[20:23], v[84:87], a[220:223], v[20:23]\n"
    "ds_read_b64_tr_b16 v[78:79], v217 offset:12288\n"
    "v_mfma_f32_16x16x32_bf16 v[28:31], v[88:91], a[212:215], v[28:31]\n"
    "ds_read_b64_tr_b16 v[80:81], v216 offset:256\n"
    "v_mfma_f32_16x16x32_bf16 v[28:31], v[92:95], a[220:223], v[28:31]\n"
    "ds_read_b64_tr_b16 v[82:83], v216 offset:4352\n"
    "v_mfma_f32_16x16x32_bf16 v[24:27], v[88:91], a[208:211], v[24:27]\n"
    "ds_read_b64_tr_b16 v[84:85], v216 offset:8448\n"
    "v_mfma_f32_16x16x32_bf16 v[24:27], v[92:95], a[216:219], v[24:27]\n"
    "ds_read_b64_tr_b16 v[86:87], v216 offset:12544\n"
    "s_waitcnt lgkmcnt(10)\n"
    "v_mfma_f32_16x16x32_bf16 v[32:35], v[96:99], a[208:211], v[32:35]\n"
    "ds_read_b64_tr_b16 v[88:89], v217 offset:256\n"
    "v_mfma_f32_16x16x32_bf16 v[32:35], v[100:103], a[216:219], v[32:35]\n"
    "ds_read_b64_tr_b16 v[90:91], v217 offset:4352\n"
    "v_mfma_f32_16x16x32_bf16 v[36:39], v[96:99], a[212:215], v[36:39]\n"
    "ds_read_b64_tr_b16 v[92:93], v217 offset:8448\n"
    "v_mfma_f32_16x16x32_bf16 v[36:39], v[100:103], a[220:223], v[36:39]\n"
    "ds_read_b64_tr_b16 v[94:95], v217 offset:12544\n"
    "v_mfma_f32_16x16x32_bf16 v[44:47], v[104:107], a[212:215], v[44:47]\n"
    "ds_read_b64_tr_b16 v[96:97], v216 offset:512\n"
    "v_mfma_f32_16x16x32_bf16 v[44:47], v[108:111], a[220:223], v[44:47]\n"
    "ds_read_b64_tr_b16 v[98:99], v216 offset:4608\n"
    "v_mfma_f32_16x16x32_bf16 v[40:43], v[104:107], a[208:211], v[40:43]\n"
    "ds_read_b64_tr_b16 v[100:101], v216 offset:8704\n"
    "v_mfma_f32_16x16x32_bf16 v[40:43], v[108:111], a[216:219], v[40:43]\n"
    "ds_read_b64_tr_b16 v[102:103], v216 offset:12800\n"
    "s_waitcnt vmcnt(4)\n"
    "v_mfma_f32_16x16x32_bf16 v[0:3], v[64:67], a[224:227], v[0:3]\n"
    "ds_read_b64_tr_b16 v[104:105], v217 offset:512\n"
    "s_waitcnt lgkmcnt(9)\n"
    "v_mfma_f32_16x16x32_bf16 v[0:3], v[68:71], a[232:235], v[0:3]\n"
    "ds_read_b64_tr_b16 v[106:107], v217 offset:4608\n"
    "v_mfma_f32_16x16x32_bf16 v[4:7], v[64:67], a[228:231], v[4:7]\n"
    "ds_read_b64_tr_b16 v[108:109], v217 offset:8704\n"
    "v_mfma_f32_16x16x32_bf16 v[4:7], v[68:71], a[236:239], v[4:7]\n"
    "ds_read_b64_tr_b16 v[110:111], v217 offset:12800\n"
    "v_mfma_f32_16x16x32_bf16 v[12:15], v[72:75], a[228:231], v[12:15]\n"
    "ds_read_b64_tr_b16 v[64:65], v216 offset:16384\n"
    "v_mfma_f32_16x16x32_bf16 v[12:15], v[76:79], a[236:239], v[12:15]\n"
    "ds_read_b64_tr_b16 v[66:67], v216 offset:20480\n"
    "v_mfma_f32_16x16x32_bf16 v[8:11], v[72:75], a[224:227], v[8:11]\n"
    "ds_read_b64_tr_b16 v[68:69], v216 offset:24576\n"
    "v_mfma_f32_16x16x32_bf16 v[8:11], v[76:79], a[232:235], v[8:11]\n"
    "ds_read_b64_tr_b16 v[70:71], v216 offset:28672\n"
    "v_mfma_f32_16x16x32_bf16 v[16:19], v[80:83], a[224:227], v[16:19]\n"
    "ds_read_b64_tr_b16 v[72:73], v217 offset:16384\n"
    "v_mfma_f32_16x16x32_bf16 v[16:19], v[84:87], a[232:235], v[16:19]\n"
    "ds_read_b64_tr_b16 v[74:75], v217 offset:20480\n"
    "v_mfma_f32_16x16x32_bf16 v[20:23], v[80:83], a[228:231], v[20:23]\n"
    "ds_read_b64_tr_b16 v[76:77], v217 offset:24576\n"
    "v_mfma_f32_16x16x32_bf16 v[20:23], v[84:87], a[236:239], v[20:23]\n"
    "ds_read_b64_tr_b16 v[78:79], v217 offset:28672\n"
    "s_waitcnt lgkmcnt(10)\n"
    "v_mfma_f32_16x16x32_bf16 v[28:31], v[88:91], a[228:231], v[28:31]\n"
    "ds_read_b64_tr_b16 v[80:81], v216 offset:16640\n"
    "v_mfma_f32_16x16x32_bf16 v[28:31], v[92:95], a[236:239], v[28:31]\n"
    "ds_read_b64_tr_b16 v[82:83], v216 offset:20736\n"
    "v_mfma_f32_16x16x32_bf16 v[24:27], v[88:91], a[224:227], v[24:27]\n"
    "ds_read_b64_tr_b16 v[84:85], v216 offset:24832\n"
    "v_mfma_f32_16x16x32_bf16 v[24:27], v[92:95], a[232:235], v[24:27]\n"
    "ds_read_b64_tr_b16 v[86:87], v216 offset:28928\n"
    "v_mfma_f32_16x16x32_bf16 v[32:35], v[96:99], a[224:227], v[32:35]\n"
    "ds_read_b64_tr_b16 v[88:89], v217 offset:16640\n"
    "v_mfma_f32_16x16x32_bf16 v[32:35], v[100:103], a[232:235], v[32:35]\n"
    "ds_read_b64_tr_b16 v[90:91], v217 offset:20736\n"
    "v_mfma_f32_16x16x32_bf16 v[36:39], v[96:99], a[228:231], v[36:39]\n"
    "ds_read_b64_tr_b16 v[92:93], v217 offset:24832\n"
    "v_mfma_f32_16x16x32_bf16 v[36:39], v[100:103], a[236:239], v[36:39]\n"
    "ds_read_b64_tr_b16 v[94:95], v217 offset:28928\n"
    "v_mfma_f32_16x16x32_bf16 v[44:47], v[104:107], a[228:231], v[44:47]\n"
    "ds_read_b64_tr_b16 v[96:97], v216 offset:16896\n"
    "s_waitcnt lgkmcnt(9)\n"
    "v_mfma_f32_16x16x32_bf16 v[44:47], v[108:111], a[236:239], v[44:47]\n"
    "ds_read_b64_tr_b16 v[98:99], v216 offset:20992\n"
    "v_mfma_f32_16x16x32_bf16 v[40:43], v[104:107], a[224:227], v[40:43]\n"
    "ds_read_b64_tr_b16 v[100:101], v216 offset:25088\n"
    "v_mfma_f32_16x16x32_bf16 v[40:43], v[108:111], a[232:235], v[40:43]\n"
    "ds_read_b64_tr_b16 v[102:103], v216 offset:29184\n"
    "s_waitcnt vmcnt(0)\n"
    "v_mfma_f32_16x16x32_bf16 v[0:3], v[64:67], a[240:243], v[0:3]\n"
    "ds_read_b64_tr_b16 v[104:105], v217 offset:16896\n"
    "v_mfma_f32_16x16x32_bf16 v[0:3], v[68:71], a[248:251], v[0:3]\n"
    "ds_read_b64_tr_b16 v[106:107], v217 offset:20992\n"
    "v_mfma_f32_16x16x32_bf16 v[4:7], v[64:67], a[244:247], v[4:7]\n"
    "ds_read_b64_tr_b16 v[108:109], v217 offset:25088\n"
    "v_mfma_f32_16x16x32_bf16 v[4:7], v[68:71], a[252:255], v[4:7]\n"
    "ds_read_b64_tr_b16 v[110:111], v217 offset:29184\n"
    "v_mfma_f32_16x16x32_bf16 v[12:15], v[72:75], a[244:247], v[12:15]\n"
    "ds_read_b64_tr_b16 v[128:129], v214 offset:768\n"
    "v_mfma_f32_16x16x32_bf16 v[12:15], v[76:79], a[252:255], v[12:15]\n"
    "ds_read_b64_tr_b16 v[130:131], v214 offset:4864\n"
    "v_mfma_f32_16x16x32_bf16 v[8:11], v[72:75], a[240:243], v[8:11]\n"
    "ds_read_b64_tr_b16 v[132:133], v214 offset:8960\n"
    "v_mfma_f32_16x16x32_bf16 v[8:11], v[76:79], a[248:251], v[8:11]\n"
    "ds_read_b64_tr_b16 v[134:135], v214 offset:13056\n"
    "s_waitcnt lgkmcnt(9)\n"
    "v_mfma_f32_16x16x32_bf16 v[16:19], v[80:83], a[240:243], v[16:19]\n"
    "ds_read_b64_tr_b16 v[144:145], v214 offset:17152\n"
    "v_mfma_f32_16x16x32_bf16 v[16:19], v[84:87], a[248:251], v[16:19]\n"
    "ds_read_b64_tr_b16 v[146:147], v214 offset:21248\n"
    "v_mfma_f32_16x16x32_bf16 v[20:23], v[80:83], a[244:247], v[20:23]\n"
    "ds_read_b64_tr_b16 v[148:149], v214 offset:25344\n"
    "v_cvt_pk_bf16_f32 v0, v0, v1\n"
    "v_mfma_f32_16x16x32_bf16 v[20:23], v[84:87], a[252:255], v[20:23]\n"
    "ds_read_b64_tr_b16 v[150:151], v214 offset:29440\n"
    "v_cvt_pk_bf16_f32 v1, v2, v3\n"
    "v_mfma_f32_16x16x32_bf16 v[28:31], v[88:91], a[244:247], v[28:31]\n"
    "ds_read_b64_tr_b16 v[136:137], v215 offset:768\n"
    "v_cvt_pk_bf16_f32 v2, v4, v5\n"
    "v_mfma_f32_16x16x32_bf16 v[28:31], v[92:95], a[252:255], v[28:31]\n"
    "ds_read_b64_tr_b16 v[138:139], v215 offset:4864\n"
    "v_cvt_pk_bf16_f32 v3, v6, v7\n"
    "v_mfma_f32_16x16x32_bf16 v[24:27], v[88:91], a[240:243], v[24:27]\n"
    "ds_read_b64_tr_b16 v[140:141], v215 offset:8960\n"
    "buffer_store_dwordx4 v[0:3], v209, s[52:55], s61 offen offset:0 sc1\n"
    "v_mfma_f32_16x16x32_bf16 v[24:27], v[92:95], a[248:251], v[24:27]\n"
    "ds_read_b64_tr_b16 v[142:143], v215 offset:13056\n"
    "v_cvt_pk_bf16_f32 v8, v8, v9\n"
    "v_mfma_f32_16x16x32_bf16 v[32:35], v[96:99], a[240:243], v[32:35]\n"
    "ds_read_b64_tr_b16 v[152:153], v215 offset:17152\n"
    "s_waitcnt lgkmcnt(7)\n"
    "v_mfma_f32_16x16x32_bf16 v[32:35], v[100:103], a[248:251], v[32:35]\n"
    "ds_read_b64_tr_b16 v[154:155], v215 offset:21248\n"
    "v_mfma_f32_16x16x32_bf16 v[36:39], v[96:99], a[244:247], v[36:39]\n"
    "ds_read_b64_tr_b16 v[156:157], v215 offset:25344\n"
    "v_cvt_pk_bf16_f32 v9, v10, v11\n"
    "v_mfma_f32_16x16x32_bf16 v[36:39], v[100:103], a[252:255], v[36:39]\n"
    "ds_read_b64_tr_b16 v[158:159], v215 offset:29440\n"
    "v_cvt_pk_bf16_f32 v10, v12, v13\n"
    "v_mfma_f32_16x16x32_bf16 v[44:47], v[104:107], a[244:247], v[44:47]\n"
    "v_cvt_pk_bf16_f32 v11, v14, v15\n"
    "v_mfma_f32_16x16x32_bf16 v[44:47], v[108:111], a[252:255], v[44:47]\n"
    "buffer_store_dwordx4 v[8:11], v209, s[52:55], s61 offen offset:1024 sc1\n"
    "v_mfma_f32_16x16x32_bf16 v[40:43], v[104:107], a[240:243], v[40:43]\n"
    "v_cvt_pk_bf16_f32 v16, v16, v17\n"
    "v_mfma_f32_16x16x32_bf16 v[40:43], v[108:111], a[248:251], v[40:43]\n"
    "v_cvt_pk_bf16_f32 v17, v18, v19\n"
    "v_mfma_f32_16x16x32_bf16 v[48:51], v[128:131], a[96:99], v[48:51]\n"
    "ds_read_b64_tr_b16 v[160:161], v214 offset:33536\n"
    "v_mfma_f32_16x16x32_bf16 v[48:51], v[132:135], a[104:107], v[48:51]\n"
    "ds_read_b64_tr_b16 v[162:163], v214 offset:37632\n"
    "v_mfma_f32_16x16x32_bf16 v[48:51], v[144:147], a[112:115], v[48:51]\n"
    "ds_read_b64_tr_b16 v[164:165], v214 offset:41728\n"
    "v_cvt_pk_bf16_f32 v18, v20, v21\n"
    "v_cvt_pk_bf16_f32 v19, v22, v23\n"
    "s_waitcnt lgkmcnt(4)\n"
    "v_mfma_f32_16x16x32_bf16 v[48:51], v[148:151], a[120:123], v[48:51]\n"
    "ds_read_b64_tr_b16 v[166:167], v214 offset:45824\n"
    "buffer_store_dwordx4 v[16:19], v209, s[52:55], s62 offen offset:0 sc1\n"
    "v_cvt_pk_bf16_f32 v24, v24, v25\n"
    "v_mfma_f32_16x16x32_bf16 v[52:55], v[148:151], a[124:127], v[52:55]\n"
    "ds_read_b64_tr_b16 v[176:177], v214 offset:49920\n"
    "v_cvt_pk_bf16_f32 v25, v26, v27\n"
    "v_cvt_pk_bf16_f32 v26, v28, v29\n"
    "v_mfma_f32_16x16x32_bf16 v[52:55], v[144:147], a[116:119], v[52:55]\n"
    "ds_read_b64_tr_b16 v[178:179], v214 offset:54016\n"
    "v_cvt_pk_bf16_f32 v27, v30, v31\n"
    "buffer_store_dwordx4 v[24:27], v209, s[52:55], s62 offen offset:1024 sc1\n"
    "v_mfma_f32_16x16x32_bf16 v[52:55], v[132:135], a[108:111], v[52:55]\n"
    "ds_read_b64_tr_b16 v[180:181], v214 offset:58112\n"
    "v_cvt_pk_bf16_f32 v32, v32, v33\n"
    "v_cvt_pk_bf16_f32 v33, v34, v35\n"
    "v_mfma_f32_16x16x32_bf16 v[52:55], v[128:131], a[100:103], v[52:55]\n"
    "ds_read_b64_tr_b16 v[182:183], v214 offset:62208\n"
    "v_cvt_pk_bf16_f32 v34, v36, v37\n"
    "v_cvt_pk_bf16_f32 v35, v38, v39\n"
    "v_mfma_f32_16x16x32_bf16 v[60:63], v[136:139], a[100:103], v[60:63]\n"
    "ds_read_b64_tr_b16 v[168:169], v215 offset:33536\n"
    "buffer_store_dwordx4 v[32:35], v209, s[52:55], s63 offen offset:0 sc1\n"
    "v_cvt_pk_bf16_f32 v40, v40, v41\n"
    "v_mfma_f32_16x16x32_bf16 v[60:63], v[140:143], a[108:111], v[60:63]\n"
    "ds_read_b64_tr_b16 v[170:171], v215 offset:37632\n"
    "v_cvt_pk_bf16_f32 v41, v42, v43\n"
    "v_cvt_pk_bf16_f32 v42, v44, v45\n"
    "v_mfma_f32_16x16x32_bf16 v[60:63], v[152:155], a[116:119], v[60:63]\n"
    "ds_read_b64_tr_b16 v[172:173], v215 offset:41728\n"
    "v_cvt_pk_bf16_f32 v43, v46, v47\n"
    "buffer_store_dwordx4 v[40:43], v209, s[52:55], s63 offen offset:1024 sc1\n"
    "s_waitcnt lgkmcnt(5)\n"
    "v_mfma_f32_16x16x32_bf16 v[60:63], v[156:159], a[124:127], v[60:63]\n"
    "ds_read_b64_tr_b16 v[174:175], v215 offset:45824\n"
    "v_mfma_f32_16x16x32_bf16 v[56:59], v[156:159], a[120:123], v[56:59]\n"
    "ds_read_b64_tr_b16 v[184:185], v215 offset:49920\n"
    "v_mfma_f32_16x16x32_bf16 v[56:59], v[152:155], a[112:115], v[56:59]\n"
    "ds_read_b64_tr_b16 v[186:187], v215 offset:54016\n"
    "v_mfma_f32_16x16x32_bf16 v[56:59], v[140:143], a[104:107], v[56:59]\n"
    "ds_read_b64_tr_b16 v[188:189], v215 offset:58112\n"
    "v_mfma_f32_16x16x32_bf16 v[56:59], v[136:139], a[96:99], v[56:59]\n"
    "ds_read_b64_tr_b16 v[190:191], v215 offset:62208\n"
    "v_mfma_f32_16x16x32_bf16 v[48:51], v[160:163], a[128:131], v[48:51]\n"
    "ds_read_b64_tr_b16 v[128:129], v216 offset:33536\n"
    "v_mfma_f32_16x16x32_bf16 v[48:51], v[164:167], a[136:139], v[48:51]\n"
    "ds_read_b64_tr_b16 v[130:131], v216 offset:37632\n"
    "v_mfma_f32_16x16x32_bf16 v[48:51], v[176:179], a[144:147], v[48:51]\n"
    "ds_read_b64_tr_b16 v[132:133], v216 offset:41728\n"
    "s_waitcnt lgkmcnt(8)\n"
    "v_mfma_f32_16x16x32_bf16 v[48:51], v[180:183], a[152:155], v[48:51]\n"
    "ds_read_b64_tr_b16 v[134:135], v216 offset:45824\n"
    "v_mfma_f32_16x16x32_bf16 v[52:55], v[180:183], a[156:159], v[52:55]\n"
    "ds_read_b64_tr_b16 v[144:145], v216 offset:49920\n"
    "v_mfma_f32_16x16x32_bf16 v[52:55], v[176:179], a[148:151], v[52:55]\n"
    "ds_read_b64_tr_b16 v[146:147], v216 offset:54016\n"
    "v_mfma_f32_16x16x32_bf16 v[52:55], v[164:167], a[140:143], v[52:55]\n"
    "ds_read_b64_tr_b16 v[148:149], v216 offset:58112\n"
    "v_mfma_f32_16x16x32_bf16 v[52:55], v[160:163], a[132:135], v[52:55]\n"
    "ds_read_b64_tr_b16 v[150:151], v216 offset:62208\n"
    "v_mfma_f32_16x16x32_bf16 v[60:63], v[168:171], a[132:135], v[60:63]\n"
    "ds_read_b64_tr_b16 v[136:137], v217 offset:33536\n"
    "s_waitcnt lgkmcnt(9)\n"
    "v_mfma_f32_16x16x32_bf16 v[60:63], v[172:175], a[140:143], v[60:63]\n"
    "ds_read_b64_tr_b16 v[138:139], v217 offset:37632\n"
    "v_mfma_f32_16x16x32_bf16 v[60:63], v[184:187], a[148:151], v[60:63]\n"
    "ds_read_b64_tr_b16 v[140:141], v217 offset:41728\n"
    "v_mfma_f32_16x16x32_bf16 v[60:63], v[188:191], a[156:159], v[60:63]\n"
    "ds_read_b64_tr_b16 v[142:143], v217 offset:45824\n"
    "v_mfma_f32_16x16x32_bf16 v[56:59], v[188:191], a[152:155], v[56:59]\n"
    "ds_read_b64_tr_b16 v[152:153], v217 offset:49920\n"
    "v_mfma_f32_16x16x32_bf16 v[56:59], v[184:187], a[144:147], v[56:59]\n"
    "ds_read_b64_tr_b16 v[154:155], v217 offset:54016\n"
    "v_mfma_f32_16x16x32_bf16 v[56:59], v[172:175], a[136:139], v[56:59]\n"
    "ds_read_b64_tr_b16 v[156:157], v217 offset:58112\n"
    "v_mfma_f32_16x16x32_bf16 v[56:59], v[168:171], a[128:131], v[56:59]\n"
    "ds_read_b64_tr_b16 v[158:159], v217 offset:62208\n"
    "s_waitcnt vmcnt(0) lgkmcnt(0)\n"
    "s_barrier\n"
    "s_cmp_lg_u32 %[w], 0\n"
    "s_cbranch_scc1 NOFLAG2R_%=\n"
    "s_mov_b64 exec, 1\n"
    "global_store_dwordx2 v223, v[228:229], s[28:29] offset:0 sc1\n"
    "s_mov_b64 exec, -1\n"
    "s_nop 3\n"
    "NOFLAG2R_%=:\n"
    "v_mfma_f32_16x16x32_bf16 v[48:51], v[128:131], a[160:163], v[48:51]\n"
    "ds_read_b64_tr_b16 v[160:161], v218 offset:768\n"
    "v_mfma_f32_16x16x32_bf16 v[48:51], v[132:135], a[168:171], v[48:51]\n"
    "ds_read_b64_tr_b16 v[162:163], v218 offset:4864\n"
    "v_mfma_f32_16x16x32_bf16 v[48:51], v[144:147], a[176:179], v[48:51]\n"
    "ds_read_b64_tr_b16 v[164:165], v218 offset:8960\n"
    "v_mfma_f32_16x16x32_bf16 v[48:51], v[148:151], a[184:187], v[48:51]\n"
    "ds_read_b64_tr_b16 v[166:167], v218 offset:13056\n"
    "v_mfma_f32_16x16x32_bf16 v[52:55], v[148:151], a[188:191], v[52:55]\n"
    "ds_read_b64_tr_b16 v[176:177], v218 offset:17152\n"
    "v_mfma_f32_16x16x32_bf16 v[52:55], v[144:147], a[180:183], v[52:55]\n"
    "ds_read_b64_tr_b16 v[178:179], v218 offset:21248\n"
    "v_mfma_f32_16x16x32_bf16 v[52:55], v[132:135], a[172:175], v[52:55]\n"
    "ds_read_b64_tr_b16 v[180:181], v218 offset:25344\n"
    "v_mfma_f32_16x16x32_bf16 v[52:55], v[128:131], a[164:167], v[52:55]\n"
    "ds_read_b64_tr_b16 v[182:183], v218 offset:29440\n"
    "v_mfma_f32_16x16x32_bf16 v[60:63], v[136:139], a[164:167], v[60:63]\n"
    "ds_read_b64_tr_b16 v[168:169], v219 offset:768\n"
    "v_mfma_f32_16x16x32_bf16 v[60:63], v[140:143], a[172:175], v[60:63]\n"
    "ds_read_b64_tr_b16 v[170:171], v219 offset:4864\n"
    "v_mfma_f32_16x16x32_bf16 v[60:63], v[152:155], a[180:183], v[60:63]\n"
    "ds_read_b64_tr_b16 v[172:173], v219 offset:8960\n"
    "v_mfma_f32_16x16x32_bf16 v[60:63], v[156:159], a[188:191], v[60:63]\n"
    "ds_read_b64_tr_b16 v[174:175], v219 offset:13056\n"
    "v_mfma_f32_16x16x32_bf16 v[56:59], v[156:159], a[184:187], v[56:59]\n"
    "ds_read_b64_tr_b16 v[184:185], v219 offset:17152\n"
    "v_mfma_f32_16x16x32_bf16 v[56:59], v[152:155], a[176:179], v[56:59]\n"
    "ds_read_b64_tr_b16 v[186:187], v219 offset:21248\n"
    "v_mfma_f32_16x16x32_bf16 v[56:59], v[140:143], a[168:171], v[56:59]\n"
    "ds_read_b64_tr_b16 v[188:189], v219 offset:25344\n"
    "v_mfma_f32_16x16x32_bf16 v[56:59], v[136:139], a[160:163], v[56:59]\n"
    "ds_read_b64_tr_b16 v[190:191], v219 offset:29440\n"
    "s_waitcnt lgkmcnt(10)\n"
    "v_mfma_f32_16x16x32_bf16 v[48:51], v[160:163], a[192:195], v[48:51]\n"
    "ds_read_b64_tr_b16 v[128:129], v216 offset:768\n"
    "v_mfma_f32_16x16x32_bf16 v[48:51], v[164:167], a[200:203], v[48:51]\n"
    "ds_read_b64_tr_b16 v[130:131], v216 offset:4864\n"
    "v_mfma_f32_16x16x32_bf16 v[48:51], v[176:179], a[208:211], v[48:51]\n"
    "ds_read_b64_tr_b16 v[132:133], v216 offset:8960\n"
    "s_waitcnt lgkmcnt(9)\n"
    "v_mfma_f32_16x16x32_bf16 v[48:51], v[180:183], a[216:219], v[48:51]\n"
    "ds_read_b64_tr_b16 v[134:135], v216 offset:13056\n"
    "v_mfma_f32_16x16x32_bf16 v[52:55], v[180:183], a[220:223], v[52:55]\n"
    "ds_read_b64_tr_b16 v[144:145], v216 offset:17152\n"
    "v_mfma_f32_16x16x32_bf16 v[52:55], v[176:179], a[212:215], v[52:55]\n"
    "ds_read_b64_tr_b16 v[146:147], v216 offset:21248\n"
    "v_mfma_f32_16x16x32_bf16 v[52:55], v[164:167], a[204:207], v[52:55]\n"
    "ds_read_b64_tr_b16 v[148:149], v216 offset:25344\n"
    "v_mfma_f32_16x16x32_bf16 v[52:55], v[160:163], a[196:199], v[52:55]\n"
    "ds_read_b64_tr_b16 v[150:151], v216 offset:29440\n"
    "v_mfma_f32_16x16x32_bf16 v[60:63], v[168:171], a[196:199], v[60:63]\n"
    "ds_read_b64_tr_b16 v[136:137], v217 offset:768\n"
    "s_waitcnt lgkmcnt(9)\n"
    "v_mfma_f32_16x16x32_bf16 v[60:63], v[172:175], a[204:207], v[60:63]\n"
    "ds_read_b64_tr_b16 v[138:139], v217 offset:4864\n"
    "v_mfma_f32_16x16x32_bf16 v[60:63], v[184:187], a[212:215], v[60:63]\n"
    "ds_read_b64_tr_b16 v[140:141], v217 offset:8960\n"
    "v_mfma_f32_16x16x32_bf16 v[60:63], v[188:191], a[220:223], v[60:63]\n"
    "ds_read_b64_tr_b16 v[142:143], v217 offset:13056\n"
    "global_load_dwordx2 v[230:231], v221, s[28:29] sc1\n"
    "v_mfma_f32_16x16x32_bf16 v[56:59], v[188:191], a[216:219], v[56:59]\n"
    "ds_read_b64_tr_b16 v[152:153], v217 offset:17152\n"
    "v_mfma_f32_16x16x32_bf16 v[56:59], v[184:187], a[208:211], v[56:59]\n"
    "ds_read_b64_tr_b16 v[154:155], v217 offset:21248\n"
    "v_mfma_f32_16x16x32_bf16 v[56:59], v[172:175], a[200:203], v[56:59]\n"
    "ds_read_b64_tr_b16 v[156:157], v217 offset:25344\n"
    "v_mfma_f32_16x16x32_bf16 v[56:59], v[168:171], a[192:195], v[56:59]\n"
    "ds_read_b64_tr_b16 v[158:159], v217 offset:29440\n"
    "s_waitcnt lgkmcnt(9)\n"
    "v_mfma_f32_16x16x32_bf16 v[48:51], v[128:131], a[224:227], v[48:51]\n"
    "v_mfma_f32_16x16x32_bf16 v[48:51], v[132:135], a[232:235], v[48:51]\n"
    "v_mfma_f32_16x16x32_bf16 v[48:51], v[144:147], a[240:243], v[48:51]\n"
    "s_waitcnt lgkmcnt(7)\n"
    "v_mfma_f32_16x16x32_bf16 v[48:51], v[148:151], a[248:251], v[48:51]\n"
    "v_mfma_f32_16x16x32_bf16 v[52:55], v[148:151], a[252:255], v[52:55]\n"
    "v_mfma_f32_16x16x32_bf16 v[52:55], v[144:147], a[244:247], v[52:55]\n"
    "v_mfma_f32_16x16x32_bf16 v[52:55], v[132:135], a[236:239], v[52:55]\n"
    "v_mfma_f32_16x16x32_bf16 v[52:55], v[128:131], a[228:231], v[52:55]\n"
    "s_waitcnt lgkmcnt(4)\n"
    "v_mfma_f32_16x16x32_bf16 v[60:63], v[136:139], a[228:231], v[60:63]\n"
    "v_mfma_f32_16x16x32_bf16 v[60:63], v[140:143], a[236:239], v[60:63]\n"
    "s_waitcnt lgkmcnt(2)\n"
    "v_mfma_f32_16x16x32_bf16 v[60:63], v[152:155], a[244:247], v[60:63]\n"
    "s_waitcnt lgkmcnt(0)\n"
    "v_mfma_f32_16x16x32_bf16 v[60:63], v[156:159], a[252:255], v[60:63]\n"
    "v_mfma_f32_16x16x32_bf16 v[56:59], v[156:159], a[248:251], v[56:59]\n"
    "v_mfma_f32_16x16x32_bf16 v[56:59], v[152:155], a[240:243], v[56:59]\n"
    "v_mfma_f32_16x16x32_bf16 v[56:59], v[140:143], a[232:235], v[56:59]\n"
    "v_mfma_f32_16x16x32_bf16 v[56:59], v[136:139], a[224:227], v[56:59]\n"
    "s_waitcnt vmcnt(0)\n"
    "v_cmp_ne_u64 vcc, v[230:231], v[228:229]\n"
    "s_and_b32 s73, vcc_lo, 7\n"
    "s_cbranch_scc1 P2SR_%=\n"
    "P2BR_%=:\n"
    "buffer_load_dwordx4 v[64:67], v209, s[52:55], s67 offen offset:0 sc1\n"
    "buffer_load_dwordx4 v[68:71], v209, s[52:55], s67 offen offset:1024 sc1\n"
    "buffer_load_dwordx4 v[72:75], v209, s[52:55], s68 offen offset:0 sc1\n"
    "buffer_load_dwordx4 v[76:79], v209, s[52:55], s68 offen offset:1024 sc1\n"
    "buffer_load_dwordx4 v[80:83], v209, s[52:55], s69 offen offset:0 sc1\n"
    "buffer_load_dwordx4 v[84:87], v209, s[52:55], s69 offen offset:1024 sc1\n"
    "s_waitcnt vmcnt(4)\n"
    "v_lshlrev_b32 v100, 16, v64\n"
    "v_and_b32 v101, 0xffff0000, v64\n"
    "v_pk_add_f32 v[48:49], v[48:49], v[100:101]\n"
    "v_lshlrev_b32 v102, 16, v65\n"
    "v_and_b32 v103, 0xffff0000, v65\n"
    "v_pk_add_f32 v[50:51], v[50:51], v[102:103]\n"
    "v_lshlrev_b32 v104, 16, v66\n"
    "v_and_b32 v105, 0xffff0000, v66\n"
    "v_pk_add_f32 v[52:53], v[52:53], v[104:105]\n"
    "v_lshlrev_b32 v106, 16, v67\n"
    "v_and_b32 v107, 0xffff0000, v67\n"
    "v_pk_add_f32 v[54:55], v[54:55], v[106:107]\n"
    "v_lshlrev_b32 v100, 16, v68\n"
    "v_and_b32 v101, 0xffff0000, v68\n"
    "v_pk_add_f32 v[56:57], v[56:57], v[100:101]\n"
    "v_lshlrev_b32 v102, 16, v69\n"
    "v_and_b32 v103, 0xffff0000, v69\n"
    "v_pk_add_f32 v[58:59], v[58:59], v[102:103]\n"
    "v_lshlrev_b32 v104, 16, v70\n"
    "v_and_b32 v105, 0xffff0000, v70\n"
    "v_pk_add_f32 v[60:61], v[60:61], v[104:105]\n"
    "v_lshlrev_b32 v106, 16, v71\n"
    "v_and_b32 v107, 0xffff0000, v71\n"
    "v_pk_add_f32 v[62:63], v[62:63], v[106:107]\n"
    "s_waitcnt vmcnt(2)\n"
    "v_lshlrev_b32 v100, 16, v72\n"
    "v_and_b32 v101, 0xffff0000, v72\n"
    "v_pk_add_f32 v[48:49], v[48:49], v[100:101]\n"
    "v_lshlrev_b32 v102, 16, v73\n"
    "v_and_b32 v103, 0xffff0000, v73\n"
    "v_pk_add_f32 v[50:51], v[50:51], v[102:103]\n"
    "v_lshlrev_b32 v104, 16, v74\n"
    "v_and_b32 v105, 0xffff0000, v74\n"
    "v_pk_add_f32 v[52:53], v[52:53], v[104:105]\n"
    "v_lshlrev_b32 v106, 16, v75\n"
    "v_and_b32 v107, 0xffff0000, v75\n"
    "v_pk_add_f32 v[54:55], v[54:55], v[106:107]\n"
    "v_lshlrev_b32 v100, 16, v76\n"
    "v_and_b32 v101, 0xffff0000, v76\n"
    "v_pk_add_f32 v[56:57], v[56:57], v[100:101]\n"
    "v_lshlrev_b32 v102, 16, v77\n"
    "v_and_b32 v103, 0xffff0000, v77\n"
    "v_pk_add_f32 v[58:59], v[58:59], v[102:103]\n"
    "v_lshlrev_b32 v104, 16, v78\n"
    "v_and_b32 v105, 0xffff0000, v78\n"
    "v_pk_add_f32 v[60:61], v[60:61], v[104:105]\n"
    "v_lshlrev_b32 v106, 16, v79\n"
    "v_and_b32 v107, 0xffff0000, v79\n"
    "v_pk_add_f32 v[62:63], v[62:63], v[106:107]\n"
    "s_waitcnt vmcnt(0)\n"
    "v_lshlrev_b32 v100, 16, v80\n"
    "v_and_b32 v101, 0xffff0000, v80\n"
    "v_pk_add_f32 v[48:49], v[48:49], v[100:101]\n"
    "v_lshlrev_b32 v102, 16, v81\n"
    "v_and_b32 v103, 0xffff0000, v81\n"
    "v_pk_add_f32 v[50:51], v[50:51], v[102:103]\n"
    "v_lshlrev_b32 v104, 16, v82\n"
    "v_and_b32 v105, 0xffff0000, v82\n"
    "v_pk_add_f32 v[52:53], v[52:53], v[104:105]\n"
    "v_lshlrev_b32 v106, 16, v83\n"
    "v_and_b32 v107, 0xffff0000, v83\n"
    "v_pk_add_f32 v[54:55], v[54:55], v[106:107]\n"
    "v_lshlrev_b32 v100, 16, v84\n"
    "v_and_b32 v101, 0xffff0000, v84\n"
    "v_pk_add_f32 v[56:57], v[56:57], v[100:101]\n"
    "v_lshlrev_b32 v102, 16, v85\n"
    "v_and_b32 v103, 0xffff0000, v85\n"
    "v_pk_add_f32 v[58:59], v[58:59], v[102:103]\n"
    "v_lshlrev_b32 v104, 16, v86\n"
    "v_and_b32 v105, 0xffff0000, v86\n"
    "v_pk_add_f32 v[60:61], v[60:61], v[104:105]\n"
    "v_lshlrev_b32 v106, 16, v87\n"
    "v_and_b32 v107, 0xffff0000, v87\n"
    "v_pk_add_f32 v[62:63], v[62:63], v[106:107]\n"
    "v_cvt_pk_bf16_f32 v48, v48, v49\n"
    "v_cvt_pk_bf16_f32 v49, v50, v51\n"
    "buffer_store_dwordx2 v[48:49], v224, s[36:39], s43 offen offset:0\n"
    "v_cvt_pk_bf16_f32 v52, v52, v53\n"
    "v_cvt_pk_bf16_f32 v53, v54, v55\n"
    "buffer_store_dwordx2 v[52:53], v225, s[36:39], s43 offen offset:0\n"
    "v_cvt_pk_bf16_f32 v56, v56, v57\n"
    "v_cvt_pk_bf16_f32 v57, v58, v59\n"
    "buffer_store_dwordx2 v[56:57], v224, s[36:39], s43 offen offset:32\n"
    "v_cvt_pk_bf16_f32 v60, v60, v61\n"
    "v_cvt_pk_bf16_f32 v61, v62, v63\n"
    "buffer_store_dwordx2 v[60:61], v225, s[36:39], s43 offen offset:32\n"
    "s_branch ENDR_%=\n"
    "P1SR_%=:\n"
    "s_mov_b32 s42, 0x40000\n"
    "P1LR_%=:\n"
    "global_load_dwordx2 v[230:231], v220, s[28:29] offset:0 sc1\n"
    "s_waitcnt vmcnt(0)\n"
    "s_sub_u32 s42, s42, 1\n"
    "s_cbranch_scc1 P1BR_%=\n"
    "v_cmp_ne_u64 vcc, v[230:231], v[228:229]\n"
    "s_and_b32 s73, vcc_lo, 255\n"
    "s_cbranch_scc1 P1LR_%=\n"
    "s_branch P1BR_%=\n"
    "P2SR_%=:\n"
    "s_mov_b32 s42, 0x40000\n"
    "P2LR_%=:\n"
    "global_load_dwordx2 v[230:231], v221, s[28:29] offset:0 sc1\n"
    "s_waitcnt vmcnt(0)\n"
    "s_sub_u32 s42, s42, 1\n"
    "s_cbranch_scc1 P2BR_%=\n"
    "v_cmp_ne_u64 vcc, v[230:231], v[228:229]\n"
    "s_and_b32 s73, vcc_lo, 7\n"
    "s_cbranch_scc1 P2LR_%=\n"
    "s_branch P2BR_%=\n"
    "IDLER_%=:\n"
    "s_lshl_b32 s72, %[rk], 3\n"
    "s_add_u32 s72, s72, %[xcd]\n"
    "s_lshl_b32 s72, s72, 2\n"
    "s_add_u32 s72, s72, %[w]\n"
    "s_cmp_lg_u32 s72, 992\n"
    "s_cbranch_scc1 ENDR_%=\n"
    "v_lshrrev_b32 v100, 3, v208\n"
    "v_lshlrev_b32 v100, 7, v100\n"
    "v_and_b32 v101, 7, v208\n"
    "v_lshl_add_u32 v100, v101, 3, v100\n"
    "v_add_u32 v100, 41943040, v100\n"
    "s_mov_b32 s42, 0x40000\n"
    "PE0LR_%=:\n"
    "global_load_dwordx2 v[230:231], v100, s[28:29] offset:0 sc1\n"
    "s_waitcnt vmcnt(0)\n"
    "s_sub_u32 s42, s42, 1\n"
    "s_cbranch_scc1 PE0XR_%=\n"
    "v_cmp_ne_u64 vcc, v[230:231], v[228:229]\n"
    "s_or_b32 s73, vcc_lo, vcc_hi\n"
    "s_cbranch_scc1 PE0LR_%=\n"
    "PE0XR_%=:\n"
    "v_add_u32 v100, 1024, v100\n"
    "s_mov_b32 s42, 0x40000\n"
    "PE1LR_%=:\n"
    "global_load_dwordx2 v[230:231], v100, s[28:29] offset:0 sc1\n"
    "s_waitcnt vmcnt(0)\n"
    "s_sub_u32 s42, s42, 1\n"
    "s_cbranch_scc1 PE1XR_%=\n"
    "v_cmp_ne_u64 vcc, v[230:231], v[228:229]\n"
    "s_or_b32 s73, vcc_lo, vcc_hi\n"
    "s_cbranch_scc1 PE1LR_%=\n"
    "PE1XR_%=:\n"
    "v_add_u32 v100, 1024, v100\n"
    "s_mov_b32 s42, 0x40000\n"
    "PE2LR_%=:\n"
    "global_load_dwordx2 v[230:231], v100, s[28:29] offset:0 sc1\n"
    "s_waitcnt vmcnt(0)\n"
    "s_sub_u32 s42, s42, 1\n"
    "s_cbranch_scc1 PE2XR_%=\n"
    "v_cmp_ne_u64 vcc, v[230:231], v[228:229]\n"
    "s_or_b32 s73, vcc_lo, vcc_hi\n"
    "s_cbranch_scc1 PE2LR_%=\n"
    "PE2XR_%=:\n"
    "v_add_u32 v100, 1024, v100\n"
    "s_mov_b32 s42, 0x40000\n"
    "PE3LR_%=:\n"
    "global_load_dwordx2 v[230:231], v100, s[28:29] offset:0 sc1\n"
    "s_waitcnt vmcnt(0)\n"
    "s_sub_u32 s42, s42, 1\n"
    "s_cbranch_scc1 PE3XR_%=\n"
    "v_cmp_ne_u64 vcc, v[230:231], v[228:229]\n"
    "s_or_b32 s73, vcc_lo, vcc_hi\n"
    "s_cbranch_scc1 PE3LR_%=\n"
    "PE3XR_%=:\n"
    "v_add_u32 v100, 1024, v100\n"
    "v_mov_b32 v100, 42008576\n"
    "s_mov_b64 exec, 1\n"
    "global_store_dwordx2 v100, v[228:229], s[28:29] sc1\n"
    "s_mov_b64 exec, -1\n"
    "ENDR_%=:\n"
    "s_nop 1\n"
    :
    : [e0] "s"(e0), [e1] "s"(e1), [ka] "s"(ka), [w] "s"(w), [b] "s"(b), [h] "s"(h), [rk] "s"(rk), [xcd] "s"(xcd)
    : "v0", "v1", "v2", "v3", "v4", "v5", "v6", "v7", "v8", "v9", "v10", "v11", "v12", "v13", "v14", "v15", "v16", "v17", "v18", "v19", "v20", "v21", "v22", "v23", "v24", "v25", "v26", "v27", "v28", "v29", "v30", "v31", "v32", "v33", "v34", "v35", "v36", "v37", "v38", "v39", "v40", "v41", "v42", "v43", "v44", "v45", "v46", "v47", "v48", "v49", "v50", "v51", "v52", "v53", "v54", "v55", "v56", "v57", "v58", "v59", "v60", "v61", "v62", "v63", "v64", "v65", "v66", "v67", "v68", "v69", "v70", "v71", "v72", "v73", "v74", "v75", "v76", "v77", "v78", "v79", "v80", "v81", "v82", "v83", "v84", "v85", "v86", "v87", "v88", "v89", "v90", "v91", "v92", "v93", "v94", "v95", "v96", "v97", "v98", "v99", "v100", "v101", "v102", "v103", "v104", "v105", "v106", "v107", "v108", "v109", "v110", "v111", "v112", "v113", "v114", "v115", "v116", "v117", "v118", "v119", "v120", "v121", "v122", "v123", "v124", "v125", "v126", "v127", "v128", "v129", "v130", "v131", "v132", "v133", "v134", "v135", "v136", "v137", "v138", "v139", "v140", "v141", "v142", "v143", "v144", "v145", "v146", "v147", "v148", "v149", "v150", "v151", "v152", "v153", "v154", "v155", "v156", "v157", "v158", "v159", "v160", "v161", "v162", "v163", "v164", "v165", "v166", "v167", "v168", "v169", "v170", "v171", "v172", "v173", "v174", "v175", "v176", "v177", "v178", "v179", "v180", "v181", "v182", "v183", "v184", "v185", "v186", "v187", "v188", "v189", "v190", "v191", "v192", "v193", "v194", "v195", "v196", "v197", "v198", "v199", "v200", "v201", "v202", "v203", "v204", "v205", "v206", "v207", "v208", "v209", "v210", "v211", "v212", "v213", "v214", "v215", "v216", "v217", "v218", "v219", "v220", "v221", "v222", "v223", "v224", "v225", "v226", "v227", "v228", "v229", "v230", "v231", "v232", "v233", "v234", "v235", "v236", "v237", "v238", "v239", "v240", "v241", "v242", "v243", "v244", "v245", "v246", "v247", "v248", "v249", "v250", "v251", "v252", "v253", "v254", "v255", "a0", "a1", "a2", "a3", "a4", "a5", "a6", "a7", "a8", "a9", "a10", "a11", "a12", "a13", "a14", "a15", "a16", "a17", "a18", "a19", "a20", "a21", "a22", "a23", "a24", "a25", "a26", "a27", "a28", "a29", "a30", "a31", "a32", "a33", "a34", "a35", "a36", "a37", "a38", "a39", "a40", "a41", "a42", "a43", "a44", "a45", "a46", "a47", "a48", "a49", "a50", "a51", "a52", "a53", "a54", "a55", "a56", "a57", "a58", "a59", "a60", "a61", "a62", "a63", "a64", "a65", "a66", "a67", "a68", "a69", "a70", "a71", "a72", "a73", "a74", "a75", "a76", "a77", "a78", "a79", "a80", "a81", "a82", "a83", "a84", "a85", "a86", "a87", "a88", "a89", "a90", "a91", "a92", "a93", "a94", "a95", "a96", "a97", "a98", "a99", "a100", "a101", "a102", "a103", "a104", "a105", "a106", "a107", "a108", "a109", "a110", "a111", "a112", "a113", "a114", "a115", "a116", "a117", "a118", "a119", "a120", "a121", "a122", "a123", "a124", "a125", "a126", "a127", "a128", "a129", "a130", "a131", "a132", "a133", "a134", "a135", "a136", "a137", "a138", "a139", "a140", "a141", "a142", "a143", "a144", "a145", "a146", "a147", "a148", "a149", "a150", "a151", "a152", "a153", "a154", "a155", "a156", "a157", "a158", "a159", "a160", "a161", "a162", "a163", "a164", "a165", "a166", "a167", "a168", "a169", "a170", "a171", "a172", "a173", "a174", "a175", "a176", "a177", "a178", "a179", "a180", "a181", "a182", "a183", "a184", "a185", "a186", "a187", "a188", "a189", "a190", "a191", "a192", "a193", "a194", "a195", "a196", "a197", "a198", "a199", "a200", "a201", "a202", "a203", "a204", "a205", "a206", "a207", "a208", "a209", "a210", "a211", "a212", "a213", "a214", "a215", "a216", "a217", "a218", "a219", "a220", "a221", "a222", "a223", "a224", "a225", "a226", "a227", "a228", "a229", "a230", "a231", "a232", "a233", "a234", "a235", "a236", "a237", "a238", "a239", "a240", "a241", "a242", "a243", "a244", "a245", "a246", "a247", "a248", "a249", "a250", "a251", "a252", "a253", "a254", "a255", "s16", "s17", "s18", "s19", "s20", "s21", "s22", "s23", "s24", "s25", "s26", "s27", "s28", "s29", "s36", "s37", "s38", "s39", "s40", "s41", "s42", "s43", "s44", "s45", "s46", "s47", "s48", "s49", "s50", "s51", "s52", "s53", "s54", "s55", "s56", "s57", "s58", "s59", "s60", "s61", "s62", "s63", "s64", "s65", "s66", "s67", "s68", "s69", "s70", "s71", "s72", "s73", "s74", "s75", "s76", "s77", "s78", "s79", "s80", "s81", "s82", "s83", "s84", "s85", "s86", "s87", "s88", "s89", "s90", "s91", "s92", "s93", "s94", "s95", "s96", "s97", "s98", "s99", "vcc", "scc", "memory");
}

extern "C" void kernel_launch(void* const* d_in, const int* in_sizes, int n_in, void* d_out, int out_size, void* d_ws, size_t ws_size,
                              hipStream_t stream) {
  const u16* x = (const u16*)d_in[0]; const u16* k = (const u16*)d_in[1]; const u16* v = (const u16*)d_in[2];
  const u16* wq = (const u16*)d_in[3]; const u16* wo = (const u16*)d_in[4];
  char* ows = (char*)d_ws;
  (void)hipFuncSetAttribute((const void*)mha_kernel, hipFuncAttributeMaxDynamicSharedMemorySize, 163840);
  mha_kernel<<<256, 256, 163840, stream>>>(x, k, v, wq, wo, (u16*)d_out, ows);
}
```
